```python
import torch

def _softmax(x, axis=-1):
    return torch.softmax(x, dim=axis)
def _gelu(x):
    return torch.nn.functional.gelu(x, approximate="tanh")
def _silu(x):
    return torch.nn.functional.silu(x)
def _sigmoid(x):
    return torch.sigmoid(x)
def _layernorm(x, axis=-1, eps=1e-5):
    m = x.mean(dim=axis, keepdim=True)
    v = x.var(dim=axis, keepdim=True, unbiased=False)
    return (x - m) / torch.sqrt(v + eps)
def _scatter_add(tgt, idx, src, axis=0):
    return tgt.clone().index_add_(axis, idx, src)
def _segment_sum(src, seg, n):
    out = torch.zeros((n,) + src.shape[1:], dtype=src.dtype)
    return out.index_add_(0, seg, src)
def _pad(x, pw):
    flat = tuple(v for pair in reversed(pw) for v in pair)
    return torch.nn.functional.pad(x, flat)

kernel_name = 'sequential_preLN__linear_kernel_hedgehog__glu_gated_bilinear__prompt'

def setup_inputs():

    _w = (lambda a: (lambda t: t.to(torch.bfloat16) if t.is_floating_point() else t)(torch.as_tensor(a)))
    def _A_setup():
        B, S, D, H = 1, 2048, 512, 32
        Dh = D // H
        return {
            "x": _w((lambda *s: torch.randn(*s, dtype=torch.bfloat16) * 0.05)(B, S, D)),
            "wqkv": _w((lambda *s: torch.randn(*s, dtype=torch.bfloat16) * 0.2)(D, 3 * D)),
            "w_phi": _w((lambda *s: torch.randn(*s, dtype=torch.bfloat16) * 0.2)(H, Dh, Dh) * (Dh ** -0.5)),
            "wo": _w((lambda *s: torch.randn(*s, dtype=torch.bfloat16) * 0.05)(D, D)),
        }
    _A_in = _A_setup()

    def _B_setup():
        B, S, D, F = 1, 2048, 512, 1376
        return {
            "x": _w((lambda *s: torch.randn(*s, dtype=torch.bfloat16) * 0.05)(B, S, D)),
            "w_gate": _w((lambda *s: torch.randn(*s, dtype=torch.bfloat16) * 0.4)(D, F) * (D ** -0.5)),
            "w_up": _w((lambda *s: torch.randn(*s, dtype=torch.bfloat16) * 0.4)(D, F) * (D ** -0.5)),
            "w_down": _w((lambda *s: torch.randn(*s, dtype=torch.bfloat16) * 0.4)(F, D) * (F ** -0.5)),
        }
    _B_in = _B_setup()

    return {
        "A_x": _A_in["x"],
        "A_wqkv": _A_in["wqkv"],
        "A_w_phi": _A_in["w_phi"],
        "A_wo": _A_in["wo"],
        "B_x": _B_in["x"],
        "B_w_gate": _B_in["w_gate"],
        "B_w_up": _B_in["w_up"],
        "B_w_down": _B_in["w_down"],
    }

def reference(A_x, A_wqkv, A_w_phi, A_wo, B_x, B_w_gate, B_w_up, B_w_down):
    def _A_ref(x, wqkv, w_phi, wo):
        B, S, D = x.shape
        H = 32; Dh = D // H
        qkv = (lambda x, *d: x.permute(*d))((x @ wqkv).reshape(B, S, 3, H, Dh), 2, 0, 3, 1, 4)
        q, k, v = qkv[0], qkv[1], qkv[2]
        pq = torch.einsum("bhsd,hdf->bhsf", q, w_phi)
        pk = torch.einsum("bhsd,hdf->bhsf", k, w_phi)
        phi_q = torch.concatenate([_softmax(pq, axis=-1), _softmax(-pq, axis=-1)], axis=-1)
        phi_k = torch.concatenate([_softmax(pk, axis=-1), _softmax(-pk, axis=-1)], axis=-1)
        kv = torch.einsum("bhsf,bhsd->bhfd", phi_k, v)
        z  = torch.einsum("bhsf->bhf", phi_k)
        num = torch.einsum("bhqf,bhfd->bhqd", phi_q, kv)
        den = torch.einsum("bhqf,bhf->bhq", phi_q, z)[..., None] + 1e-6
        o = num / den
        return (lambda x, *d: x.permute(*d))(o, 0, 2, 1, 3).reshape(B, S, D) @ wo

    def _B_ref(x, w_gate, w_up, w_down):
        g = torch.einsum("bsd,df->bsf", x, w_gate)
        u = torch.einsum("bsd,df->bsf", x, w_up)
        h = g * u
        return torch.einsum("bsf,fd->bsd", h, w_down)

    x = A_x
    h = x + _A_ref(_layernorm(x, axis=-1), A_wqkv, A_w_phi, A_wo)
    h2 = h + _B_ref(_layernorm(h, axis=-1), B_w_gate, B_w_up, B_w_down)
    return h2
```

```cpp
#include <hip/hip_runtime.h>
#include <hip/hip_bf16.h>
#include <stdint.h>

#define GAS __attribute__((address_space(1)))
#define LAS __attribute__((address_space(3)))
typedef unsigned v4u __attribute__((ext_vector_type(4)));
typedef unsigned v2u __attribute__((ext_vector_type(2)));
typedef float f32x2 __attribute__((ext_vector_type(2)));
typedef float f32x4 __attribute__((ext_vector_type(4)));
typedef float f32x16 __attribute__((ext_vector_type(16)));
typedef short s16x4 __attribute__((ext_vector_type(4)));
typedef short s16x8 __attribute__((ext_vector_type(8)));
typedef __bf16 bf16x2 __attribute__((ext_vector_type(2)));
typedef __amdgpu_buffer_rsrc_t rsrc_t;
typedef unsigned long long u64;

#define STAMPS 0
#define GU_ROT 4
#define MAPPING 2
#define SB() __builtin_amdgcn_sched_barrier(0)

namespace {

constexpr unsigned WS_EPOCH = 0;
constexpr unsigned WS_F1 = 4096;
constexpr unsigned WS_F5 = 12288;
constexpr unsigned WS_F2 = 20480;
constexpr unsigned WS_F3 = 36864;
constexpr unsigned WS_F4 = 45056;
constexpr unsigned WS_KVP = 65536;
constexpr unsigned KVP_STRIDE = 4352;
constexpr unsigned WS_CSP = WS_KVP + 256 * KVP_STRIDE;
constexpr unsigned WS_ST = WS_CSP + 256 * 768;
constexpr unsigned ST_BYTES = 16 * 128 * 128;
constexpr unsigned WS_O = WS_ST + ST_BYTES;
constexpr unsigned O_BYTES = 16 * 4 * 32 * 1024;
constexpr unsigned WS_H = WS_O + O_BYTES;
constexpr unsigned H_BYTES = 16 * 4 * 32 * 1024;
constexpr unsigned WS_HH = WS_H + H_BYTES;
constexpr unsigned HH_BYTES = 16 * 4 * 86 * 1024;
constexpr unsigned WS_END = WS_HH + HH_BYTES;
constexpr unsigned WS_STAMP = 16u << 20;

constexpr unsigned A_SLOT = 24576;
constexpr unsigned L_WQ = 98304;
constexpr unsigned L_CSW = 131072, CSW_UPOFF = 6176;
constexpr unsigned L_KVIMG = 65536;
constexpr unsigned L_WO = 0;
constexpr unsigned GU_UPOFF = 12320, GU_SLOT = 24640;
constexpr unsigned L_GW0 = 65536, L_GW1 = L_GW0 + GU_SLOT, L_GW2 = L_GW1 + GU_SLOT, L_GW3 = 0;
constexpr unsigned L_GH0 = 24640, L_GH1 = 41024, L_GH2 = 139456, L_SPARE = 57408;
constexpr unsigned L_RED_WO = 139456;
constexpr unsigned L_RED_DN = L_GW1;
constexpr unsigned L_MISC = 155840;
constexpr unsigned L_CS = L_MISC;
constexpr unsigned L_WPHI = L_MISC + 1024;
constexpr unsigned L_STAT = L_MISC + 2048;
constexpr unsigned L_CSF = L_MISC;
constexpr unsigned L_KF = L_MISC + 3072;
constexpr unsigned L_ZF = L_MISC + 5120;
constexpr unsigned L_MF = L_MISC + 5248;
constexpr unsigned L_R2 = L_MISC + 7296;
constexpr unsigned L_ZERO = L_MISC + 7808;
constexpr unsigned LDS_BYTES = 163840;
static_assert(L_GW2 + GU_SLOT == L_GH2, "map");
static_assert(L_GH2 + 16384 == L_MISC, "map");
static_assert(L_ZERO + 16 <= LDS_BYTES, "map");
static_assert(L_CSW + 2 * CSW_UPOFF <= L_MISC, "map");

constexpr unsigned SPIN_CAP = 1u << 18;
constexpr float LOG2E = 1.4426950408889634f;

struct Params {
  const void* x;
  const void* wqkv;
  const void* wphi;
  const void* wo;
  const void* wg;
  const void* wu;
  const void* wd;
  void* out;
  unsigned char* ws;
};

#define DI __device__ __forceinline__

DI rsrc_t mkrsrc(const void* p, unsigned bytes) { return __builtin_amdgcn_make_buffer_rsrc((void*)p, 0, (int)bytes, 0x00020000); }
DI void dma16(unsigned ldsaddr, unsigned voff, rsrc_t rs, unsigned soff) {
  asm volatile("s_mov_b32 m0, %0\n\ts_nop 0\n\tbuffer_load_dwordx4 %1, %2, %3 offen lds" ::"s"(ldsaddr), "v"(voff), "s"(rs), "s"(soff) : "memory");
}
DI void dma16_sc1(unsigned ldsaddr, unsigned voff, rsrc_t rs, unsigned soff) {
  asm volatile("s_mov_b32 m0, %0\n\ts_nop 0\n\tbuffer_load_dwordx4 %1, %2, %3 offen sc1 lds" ::"s"(ldsaddr), "v"(voff), "s"(rs), "s"(soff) : "memory");
}
DI v4u ld128(rsrc_t rs, unsigned voff, unsigned soff) {
  v4u r;
  asm volatile("buffer_load_dwordx4 %0, %1, %2, %3 offen" : "=v"(r) : "v"(voff), "s"(rs), "s"(soff));
  return r;
}
DI v4u ld128_sc1(rsrc_t rs, unsigned voff, unsigned soff) {
  v4u r;
  asm volatile("buffer_load_dwordx4 %0, %1, %2, %3 offen sc1" : "=v"(r) : "v"(voff), "s"(rs), "s"(soff));
  return r;
}
DI v2u ld64(rsrc_t rs, unsigned voff, unsigned soff) {
  v2u r;
  asm volatile("buffer_load_dwordx2 %0, %1, %2, %3 offen" : "=v"(r) : "v"(voff), "s"(rs), "s"(soff));
  return r;
}
#define WAITV(n) asm volatile("s_waitcnt vmcnt(" #n ")" ::: "memory")
#define BAR() asm volatile("s_waitcnt lgkmcnt(0)\n\ts_barrier" ::: "memory")
#define WAITV_BAR(n) asm volatile("s_waitcnt vmcnt(" #n ") lgkmcnt(0)\n\ts_barrier" ::: "memory")
#define TIE(x) asm volatile("" : "+v"(x))

DI s16x4 trread(LAS unsigned char* L, unsigned off) { return __builtin_amdgcn_ds_read_tr16_b64_v4i16((LAS s16x4*)(L + off)); }
DI s16x8 cat(s16x4 a, s16x4 b) { return __builtin_shufflevector(a, b, 0, 1, 2, 3, 4, 5, 6, 7); }
DI s16x8 asfrag(v4u v) { return __builtin_bit_cast(s16x8, v); }
DI f32x4 mfma16(s16x8 a, s16x8 b, f32x4 c) { return __builtin_amdgcn_mfma_f32_16x16x32_bf16(a, b, c, 0, 0, 0); }
DI f32x4 mfma16k16(s16x4 a, s16x4 b, f32x4 c) { return __builtin_amdgcn_mfma_f32_16x16x16bf16_1k(a, b, c, 0, 0, 0); }
DI f32x16 mfma32(s16x8 a, s16x8 b, f32x16 c) { return __builtin_amdgcn_mfma_f32_32x32x16_bf16(a, b, c, 0, 0, 0); }
DI unsigned pk2(float lo, float hi) {
  f32x2 v = {lo, hi};
  return __builtin_bit_cast(unsigned, __builtin_convertvector(v, bf16x2));
}
DI float dot2(unsigned a, unsigned b, float c) {
  return __builtin_amdgcn_fdot2_f32_bf16(__builtin_bit_cast(bf16x2, a), __builtin_bit_cast(bf16x2, b), c, false);
}
DI float bflo(unsigned u) { return __builtin_bit_cast(float, u << 16); }
DI float bfhi(unsigned u) { return __builtin_bit_cast(float, u & 0xffff0000u); }
DI unsigned fu(float f) { return __builtin_bit_cast(unsigned, f); }
DI float uf(unsigned u) { return __builtin_bit_cast(float, u); }
DI void swap16(float& a, float& b) {
  auto r = __builtin_amdgcn_permlane16_swap(fu(a), fu(b), false, false);
  a = uf(r[0]);
  b = uf(r[1]);
}
DI void swap32(float& a, float& b) {
  auto r = __builtin_amdgcn_permlane32_swap(fu(a), fu(b), false, false);
  a = uf(r[0]);
  b = uf(r[1]);
}
DI void swap16u(unsigned& a, unsigned& b) {
  auto r = __builtin_amdgcn_permlane16_swap(a, b, false, false);
  a = r[0];
  b = r[1];
}
DI void swap32u(unsigned& a, unsigned& b) {
  auto r = __builtin_amdgcn_permlane32_swap(a, b, false, false);
  a = r[0];
  b = r[1];
}
DI void red2add(float& a, float& b) {
  swap16(a, b);
  float m = a + b, m2 = m;
  swap32(m, m2);
  float t = m + m2, t2 = t;
  swap16(t, t2);
  a = t;
  b = t2;
}
template <bool MAX> DI float rop(float a, float b) { return MAX ? fmaxf(a, b) : a + b; }
template <bool MAX> DI void red4(float& a, float& b, float& c, float& d) {
  swap16(a, b);
  float m1 = rop<MAX>(a, b);
  swap16(c, d);
  float m2 = rop<MAX>(c, d);
  swap32(m1, m2);
  float t = rop<MAX>(m1, m2), t2 = t;
  swap32(t, t2);
  float y1 = t, y1b = t, y2 = t2, y2b = t2;
  swap16(y1, y1b);
  swap16(y2, y2b);
  a = y1;
  b = y1b;
  c = y2;
  d = y2b;
}
DI void dual_softmax(f32x4 p0, f32x4 p1, v2u& pos0, v2u& neg0, v2u& pos1, v2u& neg1) {
  float mx0 = fmaxf(fmaxf(p0[0], p0[1]), fmaxf(p0[2], p0[3]));
  float mn0 = -fminf(fminf(p0[0], p0[1]), fminf(p0[2], p0[3]));
  float mx1 = fmaxf(fmaxf(p1[0], p1[1]), fmaxf(p1[2], p1[3]));
  float mn1 = -fminf(fminf(p1[0], p1[1]), fminf(p1[2], p1[3]));
  red4<true>(mx0, mn0, mx1, mn1);
  const float a0 = -mx0 * LOG2E, b0 = -mn0 * LOG2E, a1 = -mx1 * LOG2E, b1 = -mn1 * LOG2E;
  float ep0[4], en0[4], ep1[4], en1[4];
#pragma unroll
  for (int j = 0; j < 4; ++j) {
    ep0[j] = __builtin_amdgcn_exp2f(fmaf(p0[j], LOG2E, a0));
    en0[j] = __builtin_amdgcn_exp2f(fmaf(p0[j], -LOG2E, b0));
    ep1[j] = __builtin_amdgcn_exp2f(fmaf(p1[j], LOG2E, a1));
    en1[j] = __builtin_amdgcn_exp2f(fmaf(p1[j], -LOG2E, b1));
  }
  float sp0 = (ep0[0] + ep0[1]) + (ep0[2] + ep0[3]), sn0 = (en0[0] + en0[1]) + (en0[2] + en0[3]);
  float sp1 = (ep1[0] + ep1[1]) + (ep1[2] + ep1[3]), sn1 = (en1[0] + en1[1]) + (en1[2] + en1[3]);
  red4<false>(sp0, sn0, sp1, sn1);
  const float ip0 = __builtin_amdgcn_rcpf(sp0), in0 = __builtin_amdgcn_rcpf(sn0), ip1 = __builtin_amdgcn_rcpf(sp1), in1 = __builtin_amdgcn_rcpf(sn1);
  pos0 = (v2u){pk2(ep0[0] * ip0, ep0[1] * ip0), pk2(ep0[2] * ip0, ep0[3] * ip0)};
  neg0 = (v2u){pk2(en0[0] * in0, en0[1] * in0), pk2(en0[2] * in0, en0[3] * in0)};
  pos1 = (v2u){pk2(ep1[0] * ip1, ep1[1] * ip1), pk2(ep1[2] * ip1, ep1[3] * ip1)};
  neg1 = (v2u){pk2(en1[0] * in1, en1[1] * in1), pk2(en1[2] * in1, en1[3] * in1)};
}
DI u64 mix64(u64 z) {
  z += 0x9E3779B97F4A7C15ull;
  z = (z ^ (z >> 30)) * 0xBF58476D1CE4E5B9ull;
  z = (z ^ (z >> 27)) * 0x94D049BB133111EBull;
  return z ^ (z >> 31);
}
template <int N> DI void pollN(const unsigned char* base, u64 tag, int lane) {
  const GAS u64* fl = (const GAS u64*)base;
#pragma unroll 1
  for (unsigned it = 0; it < SPIN_CAP; ++it) {
    const u64 v = __hip_atomic_load(fl + (lane & (N - 1)), __ATOMIC_RELAXED, __HIP_MEMORY_SCOPE_AGENT);
    if (__all(v == tag)) break;
    __builtin_amdgcn_s_sleep(1);
  }
}
DI void set_flag(unsigned char* f, u64 tag) { __hip_atomic_store((GAS u64*)f, tag, __ATOMIC_RELAXED, __HIP_MEMORY_SCOPE_AGENT); }

DI v4u ld128_sc1o(rsrc_t rs, unsigned voff) {
  v4u r;
  asm volatile("buffer_load_dwordx4 %0, %1, %2, 0 offen sc1" : "=v"(r) : "v"(voff), "s"(rs));
  return r;
}
DI unsigned opaque(unsigned v) {
  asm volatile("" : "+v"(v));
  return v;
}
#define TIE4(a, b, c, d) asm volatile("" : "+v"(a), "+v"(b), "+v"(c), "+v"(d))
#define WAITV_TIE(n, x) asm volatile("s_waitcnt vmcnt(" #n ")" : "+v"(x))
template <int N> DI void waitv_tie(v4u& x) { asm volatile("s_waitcnt vmcnt(%1)" : "+v"(x) : "n"(N)); }
template <int N> DI void waitv() { asm volatile("s_waitcnt vmcnt(%0)" ::"n"(N) : "memory"); }

#define STAMPW(wv, i) \
  do {                \
  } while (0)
#define STAMP(i) STAMPW(0, i)

constexpr int DN_RING = 20, DN_PRE = 6;
constexpr unsigned dn_base(int i) {
  return i < 6 ? L_GW0 : i < 7 ? L_SPARE : i < 13 ? L_GW1 : i < 17 ? L_GH2 : i < 23 ? L_GW2 : i < 27 ? L_GH0 : i < 33 ? L_GW3 : i < 37 ? L_GH1 : L_GW0;
}
constexpr int dn_i0(int i) { return i < 6 ? 0 : i < 7 ? 6 : i < 13 ? 7 : i < 17 ? 13 : i < 23 ? 17 : i < 27 ? 23 : i < 33 ? 27 : i < 37 ? 33 : 37; }
constexpr int dn_n(int i) { return i < 6 ? 6 : i < 7 ? 1 : i < 13 ? 6 : i < 17 ? 4 : i < 23 ? 6 : i < 27 ? 4 : i < 33 ? 6 : i < 37 ? 4 : 6; }

template <int I> struct DnStep {
  static DI void run(v4u (&hf)[DN_RING], s16x8 (&wpre)[DN_PRE][2], s16x8 (&fw)[2][2], f32x16& a0, f32x16& a1, unsigned rel0, unsigned rel1,
                     unsigned kh2048, unsigned b0, unsigned b1, rsrc_t rsWs, unsigned hv) {
    LAS unsigned char* const L0 = (LAS unsigned char*)0;
    if constexpr (I + 1 < 43 && I + 1 >= DN_PRE) {
      constexpr int J = I + 1;
      if constexpr (dn_i0(J) == J) {
        b0 = opaque(rel0 + dn_base(J) + dn_n(J) * kh2048);
        b1 = opaque(rel1 + dn_base(J) + dn_n(J) * kh2048);
      }
      if constexpr (J == 37) asm volatile("s_barrier" ::: "memory");
      constexpr unsigned o = (J - dn_i0(J)) * 2048;
      fw[J & 1][0] = cat(trread(L0, b0 + o), trread(L0, b0 + o + 1024));
      SB();
    }
    constexpr int younger = (I + DN_RING - 1 < 42 ? I + DN_RING - 1 : 42) - I;
    waitv_tie<younger>(hf[I % DN_RING]);
    const s16x8 bf = asfrag(hf[I % DN_RING]);
    if constexpr (I < DN_PRE) a0 = mfma32(wpre[I][0], bf, a0);
    else a0 = mfma32(fw[I & 1][0], bf, a0);
    TIE(a0);
    SB();
    if constexpr (I + 1 < 43 && I + 1 >= DN_PRE) {
      constexpr int J = I + 1;
      constexpr unsigned o = (J - dn_i0(J)) * 2048;
      fw[J & 1][1] = cat(trread(L0, b1 + o), trread(L0, b1 + o + 1024));
      SB();
    }
    if constexpr (I < DN_PRE) a1 = mfma32(wpre[I][1], bf, a1);
    else a1 = mfma32(fw[I & 1][1], bf, a1);
    TIE(a1);
    SB();
    if constexpr (I + DN_RING < 43) hf[I % DN_RING] = ld128_sc1(rsWs, hv, (I + DN_RING) * 1024);
    SB();
    if constexpr (I + 1 < 43) DnStep<I + 1>::run(hf, wpre, fw, a0, a1, rel0, rel1, kh2048, b0, b1, rsWs, hv);
  }
};

__global__ void __launch_bounds__(512, 2) fused_block_kernel(Params p) {
  __shared__ __attribute__((aligned(16))) unsigned char smem[LDS_BYTES];
  LAS unsigned char* L = (LAS unsigned char*)smem;
  LAS unsigned char* const L0 = (LAS unsigned char*)0;
  const unsigned lb = (unsigned)(uintptr_t)L;
  const int tid = threadIdx.x, lane = tid & 63;
  const int w = __builtin_amdgcn_readfirstlane(tid >> 6);
  const int w4 = w & 3;
  const int b = blockIdx.x, xcd = b & 7, jj = b >> 3;
  const int rt = 4 * (xcd >> 1) + (jj >> 3), m = 8 * (xcd & 1) + (jj & 7);
  const int kro = (m + 2 * (rt & 3)) & 7;
  const int l15 = lane & 15, g = lane >> 4, q4 = l15 >> 2, p4 = lane & 3;
  const int hl = lane >> 5, t32 = lane & 31;
  const int hf = m >> 3, cg = m & 7;
  STAMP(0);

  const rsrc_t rsX = mkrsrc(p.x, 2048u * 1024u);
  const rsrc_t rsWs = mkrsrc(p.ws, WS_END);
  const s16x8 ones8 = {0x3F80, 0x3F80, 0x3F80, 0x3F80, 0x3F80, 0x3F80, 0x3F80, 0x3F80};
  v2u ep;
  asm volatile("global_load_dwordx2 %0, %1, off sc1" : "=v"(ep) : "v"(p.ws + WS_EPOCH));
  const unsigned trW0 = 16384 + (8 * g + q4) * 64 + ((0 ^ (g & 1)) << 5) + 8 * p4;
  const unsigned trW1 = 16384 + (8 * g + q4) * 64 + ((1 ^ (g & 1)) << 5) + 8 * p4;
  const unsigned voffL = lane * 16;
  const unsigned voffR = (lane >> 3) * 1024 + (((lane & 7) ^ (((lane >> 4) & 1) << 2)) * 16);

  if (w >= 4) {
    const rsrc_t rsWqkv = mkrsrc(p.wqkv, 512u * 3072u);
    const unsigned voffX0 = (128 * rt + 32 * w4 + (lane >> 3)) * 1024 + (((lane & 7) ^ (lane >> 4)) * 16);
    const unsigned voffX1 = (128 * rt + 32 * w4 + 8 + (lane >> 3)) * 1024 + (((lane & 7) ^ (4 + (lane >> 4))) * 16);
    const unsigned voffW = (lane >> 2) * 3072 + (((lane & 3) ^ ((lane >> 5) << 1)) * 16);
#define A_LOAD(st)                                                                                     \
  {                                                                                                    \
    const unsigned sl_ = lb + ((st) & 3) * A_SLOT, kb_ = (((st) + kro) & 7) * 64;                      \
    dma16(sl_ + (4 * w4 + 0) * 1024, voffX0, rsX, kb_ * 2);                                            \
    dma16(sl_ + (4 * w4 + 1) * 1024, voffX1, rsX, kb_ * 2);                                            \
    dma16(sl_ + 16384 + w4 * 1024, voffW, rsWqkv, (kb_ + 16 * w4) * 3072 + 1024 + 64 * m);             \
    dma16(sl_ + (4 * w4 + 2) * 1024, voffX0, rsX, kb_ * 2 + 16384);                                    \
    dma16(sl_ + (4 * w4 + 3) * 1024, voffX1, rsX, kb_ * 2 + 16384);                                    \
    dma16(sl_ + 20480 + w4 * 1024, voffW, rsWqkv, (kb_ + 16 * w4) * 3072 + 2048 + 64 * m);             \
  }
    if (w == 7) {
      const rsrc_t rsPhi = mkrsrc(p.wphi, 32u * 512u);
      dma16(lb + L_WPHI, voffL, rsPhi, m * 1024);
    }
    A_LOAD(0)
    A_LOAD(1)
    STAMPW(4, 35);
    WAITV_BAR(6);
    A_LOAD(2)
    A_LOAD(3)
#pragma unroll 1
    for (int st = 1; st < 8; ++st) {
      if (st <= 5) WAITV(12);
      else if (st == 6) WAITV(10);
      else WAITV(8);
      BAR();
      if (st <= 4) A_LOAD(st + 3)
      else if (st <= 6) {
#pragma unroll
        for (int j = 0; j < 4; ++j) {
          const unsigned r = 128 * w4 + 64 * (st - 5) + 16 * j;
          dma16(lb + L_WQ + r * 64, voffW, rsWqkv, ((r + 64 * kro) & 511) * 3072 + 64 * m);
        }
      }
    }
    asm volatile("s_waitcnt vmcnt(0) lgkmcnt(0)\n\ts_barrier" : "+v"(ep)::"memory");
    const u64 tag = mix64(((u64)(unsigned)__builtin_amdgcn_readfirstlane(ep[1]) << 32) | (unsigned)__builtin_amdgcn_readfirstlane(ep[0]));
    {
      const int mat = w4 >> 1, i6 = 3 * (w4 & 1);
      const rsrc_t rsG = mkrsrc(mat ? p.wu : p.wg, 512u * 2752u);
#pragma unroll
      for (int j = 0; j < 3; ++j) {
        const unsigned u = 64 * (i6 + j) + lane, row = u / 12, c = u - row * 12;
        dma16(lb + L_CSW + mat * CSW_UPOFF + (i6 + j) * 1024, row * 2752 + c * 16, rsG, 32 * rt * 2752 + 192 * m);
      }
      const rsrc_t rsWo = mkrsrc(p.wo, 512u * 1024u);
#pragma unroll 4
      for (int i = 0; i < 16; ++i) dma16(lb + L_WO + (w4 * 16 + i) * 1024, voffR, rsWo, (w4 * 16 + i) * 8192 + 128 * cg);
    }
    {
      const unsigned cq = lb + L_WQ - 16384 + ((w4 & 1) ? trW1 : trW0) + (w4 >> 1) * 8 * 2048;
      f32x4 acq = {0, 0, 0, 0}, acq2 = acq;
#pragma unroll
      for (int s = 0; s < 8; s += 2) {
        acq = mfma16(cat(trread(L0, cq + s * 2048), trread(L0, cq + s * 2048 + 256)), ones8, acq);
        acq2 = mfma16(cat(trread(L0, cq + (s + 1) * 2048), trread(L0, cq + (s + 1) * 2048 + 256)), ones8, acq2);
      }
      if (l15 == 0) *(LAS f32x4*)(L + L_CS + 256 + (w4 & 1) * 128 + (w4 >> 1) * 64 + g * 16) = acq + acq2;
    }
    WAITV_BAR(16);
#pragma unroll 1
    for (int bl = w4; bl < 6; bl += 4) {
      const unsigned a = L_CSW + (p4 >> 1) * CSW_UPOFF + (4 * (g >> 1) + q4) * 192 + 16 * (g & 1) + 8 * (p4 & 1) + 32 * bl;
      f32x16 c;
#pragma unroll
      for (int i = 0; i < 16; ++i) c[i] = 0.f;
      c = mfma32(cat(trread(L, a), trread(L, a + 8 * 192)), ones8, c);
      c = mfma32(cat(trread(L, a + 16 * 192), trread(L, a + 24 * 192)), ones8, c);
      const unsigned off = t32 == 0 ? WS_CSP + ((m * 16 + rt) * 6 + bl) * 128 + hl * 16 : 0x7FFFFF00u;
#pragma unroll
      for (int i = 0; i < 4; ++i)
        __builtin_amdgcn_raw_buffer_store_b128(__builtin_bit_cast(v4u, (f32x4){c[4 * i], c[4 * i + 1], c[4 * i + 2], c[4 * i + 3]}), rsWs, off, i * 32, 16);
    }
    WAITV(0);
    if (lane == 0) set_flag(p.ws + WS_F5 + m * 512 + (rt * 4 + w4) * 8, tag);
    BAR();
    STAMPW(4, 28);

    const rsrc_t rsGU = mkrsrc((w4 >> 1) ? p.wu : p.wg, 512u * 2752u);
    unsigned voffG[6];
#pragma unroll
    for (int j = 0; j < 6; ++j) {
      const unsigned u = 64 * (6 * (w4 & 1) + j) + lane, row = u / 12, c = u - row * 12;
      voffG[j] = row * 2752 + c * 16;
    }
    const unsigned gdst = (w4 >> 1) * GU_UPOFF + 6 * (w4 & 1) * 1024;
    const unsigned gsrc = 192 * m;
#define GU_LOADW(st, slot)                                                                       \
  {                                                                                              \
    _Pragma("unroll") for (int j = 0; j < 6; ++j) dma16(lb + (slot) + gdst + j * 1024, voffG[j], rsGU, gsrc + (((st) + kro) & 7) * 64 * 2752); \
  }
    GU_LOADW(0, L_GW0)
    GU_LOADW(1, L_GW1)
    GU_LOADW(2, L_GW2)
    STAMPW(4, 30);
    pollN<64>(p.ws + WS_F5 + m * 512, tag, lane);
    if (tid == 256) *(LAS v4u*)(L + L_ZERO) = (v4u){0, 0, 0, 0};
#pragma unroll 1
    for (int bl = w4; bl < 6; bl += 4) {
      float s = 0.f;
      unsigned cv[8];
#pragma unroll
      for (int j = 0; j < 8; ++j) cv[j] = __builtin_amdgcn_raw_buffer_load_b32(rsWs, WS_CSP + ((m * 16 + 8 * hl) * 6 + bl) * 128 + t32 * 4, j * 768, 16);
#pragma unroll
      for (int j = 0; j < 8; ++j) s += uf(cv[j]);
      float s2 = s;
      swap32(s, s2);
      s += s2;
      const unsigned hi = pk2(s, 0.f) & 0xffffu, lo = pk2(s - uf(hi << 16), 0.f) & 0xffffu;
      if (lane < 32) *(LAS v4u*)(L + L_CSF + bl * 512 + lane * 16) = (v4u){hi | (hi << 16), lo, 0, 0};
    }
    BAR();
    BAR();
    GU_LOADW(3, L_GW3)
    pollN<16>(p.ws + WS_F3 + (rt * 4 + w4) * 128, tag, lane);
    STAMPW(4, 8);
    const unsigned hsrc = WS_H + (rt * 4 + w4) * 32 * 1024;
    const unsigned hdst = w4 * 4096;
#define GU_LOADH(st, slot)                                           \
  {                                                                  \
    _Pragma("unroll") for (int j = 0; j < 4; ++j) dma16_sc1(lb + (slot) + hdst + j * 1024, voffL, rsWs, hsrc + (((st) + kro) & 7) * 4096 + j * 1024); \
  }
    GU_LOADH(0, L_GH0)
    WAITV_BAR(0);
    GU_LOADH(1, L_GH1)
    v4u sv[4];
#pragma unroll
    for (int i = 0; i < 4; ++i) sv[i] = ld128_sc1(rsWs, WS_ST + (rt * 128 + 32 * w4 + t32) * 128 + hl * 64, i * 16);
    GU_LOADH(2, L_GH2)
    WAITV_BAR(8);
    GU_LOADH(3, L_GH0)
    GU_LOADW(4, L_GW0)
    asm volatile("s_waitcnt vmcnt(10)\n\ts_barrier" : "+v"(sv[0]), "+v"(sv[1]), "+v"(sv[2]), "+v"(sv[3])::"memory");
    GU_LOADH(4, L_GH1)
    GU_LOADW(5, L_GW1)
    {
      float s = 0.f, q = 0.f;
#pragma unroll
      for (int i = 0; i < 4; ++i) {
        s += uf(sv[i][0]) + uf(sv[i][2]);
        q += uf(sv[i][1]) + uf(sv[i][3]);
      }
      swap32(s, q);
      float t = s + q, t2 = t;
      swap32(t, t2);
      const float mean2 = t * (1.f / 512.f);
      const float var = fmaxf(t2 * (1.f / 512.f) - mean2 * mean2, 0.f) + 1e-5f;
      const float nm = -mean2;
      const unsigned hi = pk2(nm, 0.f) & 0xffffu, lo = pk2(nm - uf(hi << 16), 0.f) & 0xffffu;
      if (lane < 32) {
        *(LAS v4u*)(L + L_MF + w4 * 512 + lane * 16) = (v4u){hi | (lo << 16), hi, 0, 0};
        *(LAS float*)(L + L_R2 + (32 * w4 + lane) * 4) = __builtin_amdgcn_rcpf(var);
      }
    }
    const rsrc_t rsWd = mkrsrc(p.wd, 1376u * 1024u);
    const unsigned dsrc = (16 * 43 * (w4 >> 1) + 8 * (w4 & 1)) * 1024 + 128 * cg;
#define DN_LOAD(i0)                                                                                            \
  {                                                                                                            \
    _Pragma("unroll") for (int j = 0; j < dn_n(i0); ++j)                                                       \
        dma16(lb + dn_base(i0) + ((w4 >> 1) * dn_n(i0) + j) * 2048 + (w4 & 1) * 1024, voffR, rsWd, dsrc + ((i0) + j) * 16384); \
  }
#pragma unroll 1
    for (int st = 3; st < 8; ++st) {
      if (st >= 6) WAITV(17);
      else WAITV(16);
      BAR();
      const int h3 = st >= 6 ? st - 6 : (st >= 3 ? st - 3 : st);
      const unsigned hfree = h3 == 0 ? L_GH2 : L_GH0 + (h3 - 1) * 16384;
      const unsigned wfree = (st & 3) == 0 ? L_GW3 : L_GW0 + ((st & 3) - 1) * GU_SLOT;
      if (st <= 5) GU_LOADH(st + 2, hfree)
      if (st <= 4) GU_LOADW(st + 3, wfree)
      else if (st == 5) { DN_LOAD(0) DN_LOAD(6) }
      else if (st == 6) { DN_LOAD(7) DN_LOAD(13) }
      else { DN_LOAD(17) DN_LOAD(23) }
    }
    BAR();
    DN_LOAD(27) DN_LOAD(33)
    STAMPW(4, 31);
    WAITV_BAR(0);
    BAR();
    DN_LOAD(37)
    WAITV_BAR(0);
    BAR();
  } else {
    v4u xf[16][2];
    f32x4 ak[4][2];
    const f32x4 z4 = {0, 0, 0, 0};
    f32x2 stt[2];
#pragma unroll
    for (int j = 0; j < 4; ++j) ak[j][0] = ak[j][1] = (f32x4){0, 0, 0, 0};
    {
      const unsigned xr0 = lb + (32 * w4 + l15) * 128;
      const unsigned xlo0 = opaque(xr0 + ((g ^ (l15 >> 1)) << 4)), xlo1 = opaque(xr0 + (((4 + g) ^ (l15 >> 1)) << 4));
      const unsigned xhi0 = opaque(xlo0 + 2 * A_SLOT), xhi1 = opaque(xlo1 + 2 * A_SLOT);
      const unsigned w0lo = opaque(lb + trW0), w1lo = opaque(lb + trW1), w0hi = opaque(w0lo + 2 * A_SLOT), w1hi = opaque(w1lo + 2 * A_SLOT);
      const unsigned clo = opaque(((w4 & 1) ? w1lo : w0lo) + (w4 >> 1) * 4096), chi = opaque(clo + 2 * A_SLOT);
      s16x8 fa[2][4], fc[2];
      f32x4 asum[2] = {z4, z4}, agr[2] = {z4, z4}, acs = z4;
#define A1_X(s, a) xf[s][a] = *(LAS v4u*)(L0 + ((((s) >> 1) & 2) ? (((s) & 1) ? xhi1 : xhi0) : (((s) & 1) ? xlo1 : xlo0)) + (((s) >> 1) & 1) * A_SLOT + (a) * 2048); SB()
#define A1_WA(s, j) (((((s) >> 1) & 2) ? (((j) & 1) ? w1hi : w0hi) : (((j) & 1) ? w1lo : w0lo)) + (((s) >> 1) & 1) * A_SLOT + ((j) >> 1) * 4096 + ((s) & 1) * 2048)
#define A1_W(s, j) fa[(s) & 1][j] = cat(trread(L0, A1_WA(s, j)), trread(L0, A1_WA(s, j) + 256)); SB()
#define A1_CA(s) (((((s) >> 1) & 2) ? chi : clo) + (((s) >> 1) & 1) * A_SLOT + ((s) & 1) * 2048)
#define A1_C(s) fc[(s) & 1] = cat(trread(L0, A1_CA(s)), trread(L0, A1_CA(s) + 256)); SB()
#define A1_M(s, j, a)                                           \
  ak[j][a] = mfma16(fa[(s) & 1][j], asfrag(xf[s][a]), ak[j][a]); \
  TIE(ak[j][a]);                                                \
  SB()
#define A1_S(s, a)                                               \
  asum[a] = mfma16(ones8, asfrag(xf[s][a]), asum[a]);            \
  TIE(asum[a]);                                                  \
  agr[a] = mfma16(asfrag(xf[s][a]), asfrag(xf[s][a]), agr[a]);   \
  TIE(agr[a]);                                                   \
  SB()
#define A1_CM(s)                              \
  acs = mfma16(fc[(s) & 1], ones8, acs);      \
  TIE(acs);                                   \
  SB()
#define A1_ALLM(s)                                                            \
  A1_M(s, 0, 0); A1_M(s, 0, 1); A1_M(s, 1, 0); A1_M(s, 1, 1); A1_M(s, 2, 0); A1_M(s, 2, 1); A1_M(s, 3, 0); A1_M(s, 3, 1); \
  A1_S(s, 0); A1_S(s, 1); A1_CM(s);
#define A1_RM(sn, sc)                                                         \
  A1_X(sn, 0); A1_M(sc, 0, 0); A1_W(sn, 0); A1_M(sc, 0, 1);                   \
  A1_X(sn, 1); A1_M(sc, 1, 0); A1_W(sn, 1); A1_M(sc, 1, 1);                   \
  A1_W(sn, 2); A1_M(sc, 2, 0); A1_M(sc, 2, 1);                                \
  A1_W(sn, 3); A1_M(sc, 3, 0); A1_M(sc, 3, 1);                                \
  A1_C(sn); A1_S(sc, 0); A1_S(sc, 1); A1_CM(sc);                              \
  SB();
#define A1_STAGE(st) BAR(); A1_RM(2 * (st), 2 * (st) - 1) A1_RM(2 * (st) + 1, 2 * (st))
      BAR();
      STAMP(14);
      A1_X(0, 0); A1_X(0, 1); A1_W(0, 0); A1_W(0, 1); A1_W(0, 2); A1_W(0, 3); A1_C(0);
      SB();
      A1_RM(1, 0)
      A1_STAGE(1) A1_STAGE(2) A1_STAGE(3) A1_STAGE(4) A1_STAGE(5) A1_STAGE(6) A1_STAGE(7)
      A1_ALLM(15)
      if (l15 == 0) *(LAS f32x4*)(L + L_CS + w4 * 64 + g * 16) = acs;
#pragma unroll
      for (int a = 0; a < 2; ++a) {
        const float a01 = (p4 & 1) ? agr[a][1] : agr[a][0], a23 = (p4 & 1) ? agr[a][3] : agr[a][2];
        const float d = (p4 & 2) ? a23 : a01;
        stt[a][1] = (g == q4) ? d : 0.f;
      }
      float d0 = stt[0][1], d1 = stt[1][1];
      red2add(d0, d1);
      const float mean0 = asum[0][0] * (1.f / 512.f), mean1 = asum[1][0] * (1.f / 512.f);
      const float r0 = __builtin_amdgcn_rsqf(fmaxf(d0 * (1.f / 512.f) - mean0 * mean0, 0.f) + 1e-5f);
      const float r1 = __builtin_amdgcn_rsqf(fmaxf(d1 * (1.f / 512.f) - mean1 * mean1, 0.f) + 1e-5f);
      stt[0] = (f32x2){r0, -mean0 * r0};
      stt[1] = (f32x2){r1, -mean1 * r1};
    }
    STAMP(1);
    asm volatile("s_waitcnt lgkmcnt(0)\n\ts_barrier" : "+v"(ep)::"memory");
    STAMP(26);
    asm volatile("s_waitcnt vmcnt(0)" : "+v"(ep));
    const u64 tag = mix64(((u64)(unsigned)__builtin_amdgcn_readfirstlane(ep[1]) << 32) | (unsigned)__builtin_amdgcn_readfirstlane(ep[0]));

    const s16x4 wphi0 = trread(L, L_WPHI + (4 * g + q4) * 32 + 8 * p4);
    const s16x4 wphi1 = trread(L, L_WPHI + 512 + (4 * g + q4) * 32 + 8 * p4);
    {
      f32x4 cs[4];
#pragma unroll
      for (int i = 0; i < 4; ++i) cs[i] = *(LAS f32x4*)(L + L_CS + i * 64 + g * 16);
#pragma unroll
      for (int a = 0; a < 2; ++a) {
        const float rstd = stt[a][0], nb = stt[a][1];
        const f32x4 k0 = ak[0][a] * rstd + cs[0] * nb, k1 = ak[1][a] * rstd + cs[1] * nb;
        const f32x4 v0 = ak[2][a] * rstd + cs[2] * nb, v1 = ak[3][a] * rstd + cs[3] * nb;
        const v2u k0b = {pk2(k0[0], k0[1]), pk2(k0[2], k0[3])}, k1b = {pk2(k1[0], k1[1]), pk2(k1[2], k1[3])};
        const f32x4 p0 = mfma16k16(wphi0, __builtin_bit_cast(s16x4, k0b), z4);
        const f32x4 p1 = mfma16k16(wphi1, __builtin_bit_cast(s16x4, k1b), z4);
        const unsigned wa = L_KVIMG + (32 * w4 + 16 * a + l15) * 32 + 8 * (g ^ q4);
        *(LAS v2u*)(L + wa + 4 * 4096) = (v2u){pk2(v0[0], v0[1]), pk2(v0[2], v0[3])};
        *(LAS v2u*)(L + wa + 5 * 4096) = (v2u){pk2(v1[0], v1[1]), pk2(v1[2], v1[3])};
        v2u pos0, neg0, pos1, neg1;
        dual_softmax(p0, p1, pos0, neg0, pos1, neg1);
        *(LAS v2u*)(L + wa + 0 * 4096) = pos0;
        *(LAS v2u*)(L + wa + 1 * 4096) = neg0;
        *(LAS v2u*)(L + wa + 2 * 4096) = pos1;
        *(LAS v2u*)(L + wa + 3 * 4096) = neg1;
      }
    }
    BAR();
    STAMP(15);
    {
      const unsigned ra = L_KVIMG + (4 * g + q4) * 32 + 8 * (p4 ^ g);
      const unsigned raA = ra + w4 * 4096, raB = ra + (4 + (w4 >> 1)) * 4096;
      f32x4 acc = z4, accz = z4;
      s16x8 fA[4], fB[4];
#pragma unroll
      for (int u = 0; u < 4; ++u) {
        fA[u] = cat(trread(L, raA + u * 1024), trread(L, raA + u * 1024 + 512));
        fB[u] = cat(trread(L, raB + u * 1024), trread(L, raB + u * 1024 + 512));
      }
#pragma unroll
      for (int u = 0; u < 4; ++u) {
        acc = mfma16(fA[u], fB[u], acc);
        accz = mfma16(fA[u], ones8, accz);
      }
      const unsigned base = WS_KVP + (m * 16 + rt) * KVP_STRIDE;
      __builtin_amdgcn_raw_buffer_store_b128(__builtin_bit_cast(v4u, acc), rsWs, base + w4 * 1024 + lane * 16, 0, 16);
      __builtin_amdgcn_raw_buffer_store_b128(__builtin_bit_cast(v4u, accz), rsWs, l15 == 0 ? base + 4096 + w4 * 64 + g * 16 : 0x7FFFFFF0u, 0, 16);
      WAITV(0);
      if (lane == 0) set_flag(p.ws + WS_F1 + m * 512 + (rt * 4 + w4) * 8, tag);
    }
    STAMP(2);

    v4u phq[2][2];
    {
      f32x4 aq[2][2] = {{z4, z4}, {z4, z4}};
      const unsigned trQ0 = opaque(lb + L_WQ - 16384 + trW0), trQ1 = opaque(lb + L_WQ - 16384 + trW1);
      s16x8 fq[2][2];
#define A2_W(s, j) fq[(s) & 1][j] = cat(trread(L0, ((j) ? trQ1 : trQ0) + (s) * 2048), trread(L0, ((j) ? trQ1 : trQ0) + (s) * 2048 + 256)); SB()
#define A2_M(s, j, a)                                           \
  aq[j][a] = mfma16(fq[(s) & 1][j], asfrag(xf[s][a]), aq[j][a]); \
  TIE(aq[j][a]);                                                \
  SB()
#define A2_STEP(s)                                \
  if ((s) < 15) { A2_W(((s) + 1) & 15, 0); }      \
  A2_M(s, 0, 0); A2_M(s, 0, 1);                   \
  if ((s) < 15) { A2_W(((s) + 1) & 15, 1); }      \
  A2_M(s, 1, 0); A2_M(s, 1, 1);                   \
  SB();
      A2_W(0, 0); A2_W(0, 1);
      SB();
      A2_STEP(0) A2_STEP(1) A2_STEP(2) A2_STEP(3) A2_STEP(4) A2_STEP(5) A2_STEP(6) A2_STEP(7)
      A2_STEP(8) A2_STEP(9) A2_STEP(10) A2_STEP(11) A2_STEP(12) A2_STEP(13) A2_STEP(14) A2_STEP(15)
      STAMP(27);
      const f32x4 c0 = *(LAS f32x4*)(L + L_CS + 256 + g * 16) + *(LAS f32x4*)(L + L_CS + 320 + g * 16);
      const f32x4 c1 = *(LAS f32x4*)(L + L_CS + 384 + g * 16) + *(LAS f32x4*)(L + L_CS + 448 + g * 16);
#pragma unroll
      for (int a = 0; a < 2; ++a) {
        const f32x4 q0 = aq[0][a] * stt[a][0] + c0 * stt[a][1], q1 = aq[1][a] * stt[a][0] + c1 * stt[a][1];
        const v2u q0b = {pk2(q0[0], q0[1]), pk2(q0[2], q0[3])}, q1b = {pk2(q1[0], q1[1]), pk2(q1[2], q1[3])};
        const f32x4 p0 = mfma16k16(wphi0, __builtin_bit_cast(s16x4, q0b), z4);
        const f32x4 p1 = mfma16k16(wphi1, __builtin_bit_cast(s16x4, q1b), z4);
        v2u pos0, neg0, pos1, neg1;
        dual_softmax(p0, p1, pos0, neg0, pos1, neg1);
        phq[a][0] = (v4u){pos0[0], pos0[1], neg0[0], neg0[1]};
        phq[a][1] = (v4u){pos1[0], pos1[1], neg1[0], neg1[1]};
      }
    }
    STAMP(3);

    {
      pollN<64>(p.ws + WS_F1 + m * 512, tag, lane);
      STAMP(4);
      const unsigned base = WS_KVP + m * 16 * KVP_STRIDE;
      v4u pv[16], zv[4];
#pragma unroll
      for (int i = 0; i < 4; ++i) zv[i] = __builtin_amdgcn_raw_buffer_load_b128(rsWs, base + g * KVP_STRIDE + 4096 + l15 * 16, 4 * i * KVP_STRIDE, 16);
#pragma unroll
      for (int i = 0; i < 16; ++i) pv[i] = __builtin_amdgcn_raw_buffer_load_b128(rsWs, base + tid * 16, i * KVP_STRIDE, 16);
      f32x4 sz = __builtin_bit_cast(f32x4, zv[0]);
#pragma unroll
      for (int i = 1; i < 4; ++i) sz += __builtin_bit_cast(f32x4, zv[i]);
      float z0 = sz[0], z1 = sz[1], z2 = sz[2], z3 = sz[3];
      red4<false>(z0, z1, z2, z3);
      if (tid < 16) *(LAS v2u*)(L + L_ZF + (l15 >> 3) * 64 + (l15 & 3) * 16 + ((l15 >> 2) & 1) * 8) = (v2u){pk2(z0, z1), pk2(z2, z3)};
      f32x4 s = __builtin_bit_cast(f32x4, pv[0]);
#pragma unroll
      for (int i = 1; i < 16; ++i) s += __builtin_bit_cast(f32x4, pv[i]);
      *(LAS v2u*)(L + L_KF + (w >> 1) * 1024 + lane * 16 + (w & 1) * 8) = (v2u){pk2(s[0], s[1]), pk2(s[2], s[3])};
    }
    STAMP(33);
    BAR();
    {
      const s16x8 kf0 = *(LAS s16x8*)(L + L_KF + lane * 16), kf1 = *(LAS s16x8*)(L + L_KF + 1024 + lane * 16);
      const s16x8 zf0 = *(LAS s16x8*)(L + L_ZF + g * 16), zf1 = *(LAS s16x8*)(L + L_ZF + 64 + g * 16);
#pragma unroll
      for (int a = 0; a < 2; ++a) {
        const f32x4 n0 = mfma16(kf0, asfrag(phq[a][0]), z4), d0 = mfma16(zf0, asfrag(phq[a][0]), z4);
        const f32x4 n1 = mfma16(kf1, asfrag(phq[a][1]), z4), d1 = mfma16(zf1, asfrag(phq[a][1]), z4);
        const float i0 = __builtin_amdgcn_rcpf(d0[0] + 1e-6f), i1 = __builtin_amdgcn_rcpf(d1[0] + 1e-6f);
        const v4u ov = {pk2(n0[0] * i0, n0[1] * i0), pk2(n0[2] * i0, n0[3] * i0), pk2(n1[0] * i1, n1[1] * i1), pk2(n1[2] * i1, n1[3] * i1)};
        __builtin_amdgcn_raw_buffer_store_b128(ov, rsWs, WS_O + ((rt * 4 + w4) * 32 + 2 * m + (g >> 1)) * 1024 + ((g & 1) * 32 + 16 * a + l15) * 16, 0, 16);
      }
      WAITV(0);
      if (lane < 2) set_flag(p.ws + WS_F2 + ((rt * 4 + w4) * 2 + hf) * 128 + (lane * 8 + cg) * 8, tag);
    }
    STAMP(5);

    const int tb32 = 2 * hf + (w & 1), kh = (w >> 1) & 1;
    unsigned hres[8];
    const unsigned tok = 128 * rt + 32 * tb32 + t32;
    v2u xr[4];
#pragma unroll
    for (int i = 0; i < 4; ++i) xr[i] = ld64(rsX, tok * 1024 + (64 * cg + 32 * kh + 4 * hl) * 2, i * 16);
    const unsigned relR = (4 * (g >> 1) + q4) * 128 + (g & 1) * 32 + 8 * p4;
    const unsigned rel0 = lb + relR + ((0 ^ (q4 >> 1)) << 6), rel1 = lb + relR + ((1 ^ (q4 >> 1)) << 6);
    f32x16 a0, a1;
#pragma unroll
    for (int i = 0; i < 16; ++i) { a0[i] = 0.f; a1[i] = 0.f; }
    {
      const unsigned rl0 = opaque(rel0 + kh * 32768), rl1 = opaque(rel1 + kh * 32768);
      s16x8 fw[2][2];
#define WO_RD1(i, c)                                                                                    \
  {                                                                                                     \
    constexpr unsigned o_ = (32 * ((i) >> 1) + 8 * ((i) & 1)) * 128;                                    \
    fw[(i) & 1][c] = cat(trread(L0, ((c) ? rl1 : rl0) + o_), trread(L0, ((c) ? rl1 : rl0) + o_ + 2048)); \
    SB();                                                                                               \
  }
#define WO_RD(i) WO_RD1(i, 0) WO_RD1(i, 1)
      WO_RD(0)
      pollN<16>(p.ws + WS_F2 + ((rt * 4 + tb32) * 2 + kh) * 128, tag, lane);
      STAMP(6);
      v4u of[16];
      const unsigned ov = WS_O + ((rt * 4 + tb32) * 32 + 16 * kh) * 1024 + lane * 16;
#pragma unroll
      for (int i = 0; i < 16; ++i) of[i] = ld128_sc1(rsWs, ov, i * 1024);
#define WO_STEP(i)                                          \
  {                                                         \
    if ((i) < 15) WO_RD1(((i) + 1) & 15, 0)                 \
    waitv_tie<15 - (i)>(of[i]);                             \
    a0 = mfma32(fw[(i) & 1][0], asfrag(of[i]), a0);         \
    TIE(a0);                                                \
    SB();                                                   \
    if ((i) < 15) WO_RD1(((i) + 1) & 15, 1)                 \
    a1 = mfma32(fw[(i) & 1][1], asfrag(of[i]), a1);         \
    TIE(a1);                                                \
    SB();                                                   \
  }
      WO_STEP(0) WO_STEP(1) WO_STEP(2) WO_STEP(3) WO_STEP(4) WO_STEP(5) WO_STEP(6) WO_STEP(7)
      WO_STEP(8) WO_STEP(9) WO_STEP(10) WO_STEP(11) WO_STEP(12) WO_STEP(13) WO_STEP(14) WO_STEP(15)
    }
    STAMP(16);
    {
      const f32x16 give = kh ? a0 : a1;
      f32x16 keep = kh ? a1 : a0;
#pragma unroll
      for (int i = 0; i < 4; ++i)
        *(LAS f32x4*)(L + L_RED_WO + (w * 4 + i) * 1024 + lane * 16) = (f32x4){give[4 * i], give[4 * i + 1], give[4 * i + 2], give[4 * i + 3]};
      BAR();
      float ss = 0.f, sq = 0.f;
#pragma unroll
      for (int i = 0; i < 4; ++i) {
        const f32x4 o = *(LAS f32x4*)(L + L_RED_WO + ((w ^ 2) * 4 + i) * 1024 + lane * 16);
        TIE(xr[i]);
        hres[2 * i] = pk2(keep[4 * i] + o[0] + bflo(xr[i][0]), keep[4 * i + 1] + o[1] + bfhi(xr[i][0]));
        hres[2 * i + 1] = pk2(keep[4 * i + 2] + o[2] + bflo(xr[i][1]), keep[4 * i + 3] + o[3] + bfhi(xr[i][1]));
      }
      const int Sg = 2 * cg + kh;
      const unsigned ho = WS_H + ((rt * 4 + tb32) * 32 + 2 * Sg) * 1024 + lane * 16;
      __builtin_amdgcn_raw_buffer_store_b128((v4u){hres[0], hres[1], hres[2], hres[3]}, rsWs, ho, 0, 16);
      __builtin_amdgcn_raw_buffer_store_b128((v4u){hres[4], hres[5], hres[6], hres[7]}, rsWs, ho + 1024, 0, 16);
#pragma unroll
      for (int i = 0; i < 8; ++i) {
        const float lo = bflo(hres[i]), hi = bfhi(hres[i]);
        ss += lo + hi;
        sq = fmaf(lo, lo, fmaf(hi, hi, sq));
      }
      swap32(ss, sq);
      __builtin_amdgcn_raw_buffer_store_b32(fu(ss + sq), rsWs, WS_ST + (rt * 128 + 32 * tb32 + t32) * 128 + Sg * 8 + hl * 4, 0, 16);
      WAITV(0);
      if (lane == 0) set_flag(p.ws + WS_F3 + (rt * 4 + tb32) * 128 + Sg * 8, tag);
    }
    BAR();
    STAMP(7);

    const int mh = w & 1, nh = (w >> 1) & 1;
    f32x16 acc[2][3];
#pragma unroll
    for (int i = 0; i < 16; ++i) {
      acc[0][0][i] = 0.f; acc[0][1][i] = 0.f; acc[0][2][i] = 0.f;
      acc[1][0][i] = 0.f; acc[1][1][i] = 0.f; acc[1][2][i] = 0.f;
    }
    {
      const unsigned relT = lb + mh * 8192 + lane * 16;
      const unsigned relW = lb + (p4 >> 1) * GU_UPOFF + (4 * (g >> 1) + q4) * 192 + 16 * (g & 1) + 8 * (p4 & 1) + 96 * nh;
      v4u tf[2][2];
      s16x8 ga[2][3];
      tf[1][0] = tf[1][1] = (v4u){0, 0, 0, 0};
      ga[1][0] = ga[1][1] = ga[1][2] = asfrag(tf[1][0]);
#define GU_RT(j, tt) tf[(j) & 1][tt] = *(LAS v4u*)(L0 + aT + (tt) * 4096 + (j) * 1024); SB()
#define GU_RW(j, l3) ga[(j) & 1][l3] = cat(trread(L0, aW + (j) * 16 * 192 + 32 * (l3)), trread(L0, aW + ((j) * 16 + 8) * 192 + 32 * (l3))); SB()
#define GU_M(j, tt, l3)                                                       \
  acc[tt][l3] = mfma32(ga[(j) & 1][l3], asfrag(tf[(j) & 1][tt]), acc[tt][l3]); \
  TIE(acc[tt][l3]);                                                            \
  SB()
#define GU_STEP(jn, jc)            \
  GU_RT(jn, 0);                    \
  GU_M(jc, 0, 0);                  \
  GU_RW(jn, 0);                    \
  GU_M(jc, 0, 1);                  \
  GU_RW(jn, 1);                    \
  GU_M(jc, 0, 2);                  \
  GU_RT(jn, 1);                    \
  GU_M(jc, 1, 0);                  \
  GU_RW(jn, 2);                    \
  GU_M(jc, 1, 1);                  \
  GU_M(jc, 1, 2);                  \
  SB();
#pragma unroll 1
      for (int st = 0; st < 8; ++st) {
        BAR();
        if (st == 0) STAMP(17);
        if (st == 1) STAMP(18);
        if (st == 7) STAMP(24);
        const int h3 = st >= 6 ? st - 6 : (st >= 3 ? st - 3 : st);
        const unsigned hcur = h3 == 2 ? L_GH2 : L_GH0 + h3 * 16384;
        const unsigned wcur = (st & 3) == 3 ? L_GW3 : L_GW0 + (st & 3) * GU_SLOT;
        const unsigned aT = relT + hcur, aW = relW + wcur;
        GU_STEP(0, 3)
        GU_STEP(1, 0)
        GU_STEP(2, 1)
        GU_STEP(3, 2)
      }
      const unsigned zo = lb + L_ZERO;
      const unsigned aC = lane < 32 ? lb + L_CSF + 3 * nh * 512 + lane * 16 : zo - 0, aM = lane < 32 ? lb + L_MF + 2 * mh * 512 + lane * 16 : zo;
      const unsigned stp = lane < 32 ? 512 : 0;
      tf[0][0] = *(LAS v4u*)(L0 + aM);
      tf[0][1] = *(LAS v4u*)(L0 + aM + stp);
      ga[0][0] = *(LAS s16x8*)(L0 + aC);
      ga[0][1] = *(LAS s16x8*)(L0 + aC + stp);
      ga[0][2] = *(LAS s16x8*)(L0 + aC + 2 * stp);
      GU_M(3, 0, 0); GU_M(3, 0, 1); GU_M(3, 0, 2); GU_M(3, 1, 0); GU_M(3, 1, 1); GU_M(3, 1, 2);
      GU_M(0, 0, 0); GU_M(0, 0, 1); GU_M(0, 0, 2); GU_M(0, 1, 0); GU_M(0, 1, 1); GU_M(0, 1, 2);
    }
    const float r2a = *(LAS float*)(L + L_R2 + (64 * mh + t32) * 4), r2b = *(LAS float*)(L + L_R2 + (64 * mh + 32 + t32) * 4);
    BAR();
    STAMP(9);
#pragma unroll
    for (int tt = 0; tt < 2; ++tt) {
      const float r2 = tt ? r2b : r2a;
#pragma unroll
      for (int l3 = 0; l3 < 3; ++l3) {
        const int fblk = 6 * m + 3 * nh + l3;
        float hv[8];
#pragma unroll
        for (int i = 0; i < 2; ++i)
#pragma unroll
          for (int e = 0; e < 4; ++e) hv[4 * i + e] = (acc[tt][l3][8 * i + e] * r2) * acc[tt][l3][8 * i + 4 + e];
        const v4u o = {pk2(hv[0], hv[1]), pk2(hv[2], hv[3]), pk2(hv[4], hv[5]), pk2(hv[6], hv[7])};
        const unsigned off = fblk < 86 ? WS_HH + ((rt * 4 + 2 * mh + tt) * 86 + fblk) * 1024 + lane * 16 : 0x7FFFFFF0u;
        __builtin_amdgcn_raw_buffer_store_b128(o, rsWs, off, 0, 16);
      }
    }
    WAITV(0);
    if (lane < 2) set_flag(p.ws + WS_F4 + (rt * 4 + 2 * mh + lane) * 256 + (m * 2 + nh) * 8, tag);
    STAMP(10);

    const unsigned kh2048 = kh * 2048;
    const unsigned pb0 = opaque(rel0 + dn_base(0) + dn_n(0) * kh2048), pb1 = opaque(rel1 + dn_base(0) + dn_n(0) * kh2048);
    BAR();
    s16x8 wpre[DN_PRE][2];
#pragma unroll
    for (int i = 0; i < DN_PRE; ++i) {
      wpre[i][0] = cat(trread(L0, pb0 + i * 2048), trread(L0, pb0 + i * 2048 + 1024));
      wpre[i][1] = cat(trread(L0, pb1 + i * 2048), trread(L0, pb1 + i * 2048 + 1024));
    }
#pragma unroll
    for (int i = 0; i < DN_PRE; ++i) { TIE(wpre[i][0]); TIE(wpre[i][1]); }
    BAR();
    STAMP(25);
    pollN<32>(p.ws + WS_F4 + (rt * 4 + tb32) * 256, tag, lane);
    STAMP(11);
    v4u hf_[DN_RING];
    const unsigned hv = WS_HH + ((rt * 4 + tb32) * 86 + 43 * kh) * 1024 + lane * 16;
#pragma unroll
    for (int i = 0; i < DN_RING; ++i) hf_[i] = ld128_sc1(rsWs, hv, i * 1024);
#pragma unroll
    for (int i = 0; i < 16; ++i) { a0[i] = 0.f; a1[i] = 0.f; }
    s16x8 fw[2][2];
    DnStep<0>::run(hf_, wpre, fw, a0, a1, rel0, rel1, kh2048, pb0, pb1, rsWs, hv);
    STAMP(12);
    {
      int tz = threadIdx.x;
      asm volatile("" : "+v"(tz));
      const int lane = tz & 63, hl = lane >> 5, t32 = lane & 31;
      const f32x16 give = kh ? a0 : a1;
      const f32x16 keep = kh ? a1 : a0;
#pragma unroll
      for (int i = 0; i < 4; ++i)
        *(LAS f32x4*)(L + L_RED_DN + (w * 4 + i) * 1024 + lane * 16) = (f32x4){give[4 * i], give[4 * i + 1], give[4 * i + 2], give[4 * i + 3]};
      BAR();
      unsigned ob[8];
#pragma unroll
      for (int i = 0; i < 4; ++i) {
        const f32x4 o = *(LAS f32x4*)(L + L_RED_DN + ((w ^ 2) * 4 + i) * 1024 + lane * 16);
        ob[2 * i] = pk2(keep[4 * i] + o[0] + bflo(hres[2 * i]), keep[4 * i + 1] + o[1] + bfhi(hres[2 * i]));
        ob[2 * i + 1] = pk2(keep[4 * i + 2] + o[2] + bflo(hres[2 * i + 1]), keep[4 * i + 3] + o[3] + bfhi(hres[2 * i + 1]));
      }
      const rsrc_t rsOut = mkrsrc(p.out, 2048u * 1024u);
      const unsigned oo = (128 * rt + 32 * tb32 + t32) * 1024 + (64 * cg + 32 * kh + 8 * hl) * 2;
#pragma unroll
      for (int pr = 0; pr < 2; ++pr) {
        swap32u(ob[4 * pr], ob[4 * pr + 2]);
        swap32u(ob[4 * pr + 1], ob[4 * pr + 3]);
        __builtin_amdgcn_raw_buffer_store_b128((v4u){ob[4 * pr], ob[4 * pr + 1], ob[4 * pr + 2], ob[4 * pr + 3]}, rsOut, oo + pr * 32, 0, 0);
      }
    }
    if (b == 0 && tid == 0) __hip_atomic_store((GAS u64*)(p.ws + WS_EPOCH), tag, __ATOMIC_RELAXED, __HIP_MEMORY_SCOPE_AGENT);
    STAMP(13);
  }
}

}

extern "C" void kernel_launch(void* const* d_in, const int* in_sizes, int n_in, void* d_out, int out_size, void* d_ws, size_t ws_size,
                              hipStream_t stream) {
  Params p;
  p.x = d_in[0];
  p.wqkv = d_in[1];
  p.wphi = d_in[2];
  p.wo = d_in[3];
  p.wg = d_in[5];
  p.wu = d_in[6];
  p.wd = d_in[7];
  p.out = d_out;
  p.ws = (unsigned char*)d_ws;
  fused_block_kernel<<<256, 512, 0, stream>>>(p);
}
```

```cpp
#include <hip/hip_runtime.h>
#include <hip/hip_bf16.h>
#include <stdint.h>

#define GAS __attribute__((address_space(1)))
#define LAS __attribute__((address_space(3)))
typedef unsigned v4u __attribute__((ext_vector_type(4)));
typedef unsigned v2u __attribute__((ext_vector_type(2)));
typedef float f32x2 __attribute__((ext_vector_type(2)));
typedef float f32x4 __attribute__((ext_vector_type(4)));
typedef float f32x16 __attribute__((ext_vector_type(16)));
typedef short s16x4 __attribute__((ext_vector_type(4)));
typedef short s16x8 __attribute__((ext_vector_type(8)));
typedef __bf16 bf16x2 __attribute__((ext_vector_type(2)));
typedef __amdgpu_buffer_rsrc_t rsrc_t;
typedef unsigned long long u64;

#define STAMPS 0
#define GU_ROT 4
#define DIAG_T 31.5f
#define DIAG_B 0
#define MAPPING 2
#define SB() __builtin_amdgcn_sched_barrier(0)

namespace {

constexpr unsigned WS_EPOCH = 0;
constexpr unsigned WS_F1 = 4096;
constexpr unsigned WS_F5 = 12288;
constexpr unsigned WS_F2 = 20480;
constexpr unsigned WS_F3 = 36864;
constexpr unsigned WS_F4 = 45056;
constexpr unsigned WS_KVP = 65536;
constexpr unsigned KVP_STRIDE = 4352;
constexpr unsigned WS_CSP = WS_KVP + 256 * KVP_STRIDE;
constexpr unsigned WS_ST = WS_CSP + 256 * 768;
constexpr unsigned ST_BYTES = 16 * 128 * 128;
constexpr unsigned WS_O = WS_ST + ST_BYTES;
constexpr unsigned O_BYTES = 16 * 4 * 32 * 1024;
constexpr unsigned WS_H = WS_O + O_BYTES;
constexpr unsigned H_BYTES = 16 * 4 * 32 * 1024;
constexpr unsigned WS_HH = WS_H + H_BYTES;
constexpr unsigned HH_BYTES = 16 * 4 * 86 * 1024;
constexpr unsigned WS_END = WS_HH + HH_BYTES;
constexpr unsigned WS_STAMP = 16u << 20;

constexpr unsigned A_SLOT = 24576;
constexpr unsigned L_WQ = 98304;
constexpr unsigned L_CSW = 131072, CSW_UPOFF = 6176;
constexpr unsigned L_KVIMG = 65536;
constexpr unsigned L_WO = 0;
constexpr unsigned GU_UPOFF = 12320, GU_SLOT = 24640;
constexpr unsigned L_GW0 = 65536, L_GW1 = L_GW0 + GU_SLOT, L_GW2 = L_GW1 + GU_SLOT, L_GW3 = 0;
constexpr unsigned L_GH0 = 24640, L_GH1 = 41024, L_GH2 = 139456, L_SPARE = 57408;
constexpr unsigned L_RED_WO = 139456;
constexpr unsigned L_RED_DN = L_GW1;
constexpr unsigned L_MISC = 155840;
constexpr unsigned L_CS = L_MISC;
constexpr unsigned L_WPHI = L_MISC + 1024;
constexpr unsigned L_STAT = L_MISC + 2048;
constexpr unsigned L_CSF = L_MISC;
constexpr unsigned L_KF = L_MISC + 3072;
constexpr unsigned L_ZF = L_MISC + 5120;
constexpr unsigned L_MF = L_MISC + 5248;
constexpr unsigned L_R2 = L_MISC + 7296;
constexpr unsigned L_ZERO = L_MISC + 7808;
constexpr unsigned LDS_BYTES = 163840;
static_assert(L_GW2 + GU_SLOT == L_GH2, "map");
static_assert(L_GH2 + 16384 == L_MISC, "map");
static_assert(L_ZERO + 16 <= LDS_BYTES, "map");
static_assert(L_CSW + 2 * CSW_UPOFF <= L_MISC, "map");

constexpr unsigned SPIN_CAP = 1u << 18;
constexpr float LOG2E = 1.4426950408889634f;

struct Params {
  const void* x;
  const void* wqkv;
  const void* wphi;
  const void* wo;
  const void* wg;
  const void* wu;
  const void* wd;
  void* out;
  unsigned char* ws;
};

#define DI __device__ __forceinline__

DI rsrc_t mkrsrc(const void* p, unsigned bytes) { return __builtin_amdgcn_make_buffer_rsrc((void*)p, 0, (int)bytes, 0x00020000); }
DI void dma16(unsigned ldsaddr, unsigned voff, rsrc_t rs, unsigned soff) {
  asm volatile("s_mov_b32 m0, %0\n\ts_nop 0\n\tbuffer_load_dwordx4 %1, %2, %3 offen lds" ::"s"(ldsaddr), "v"(voff), "s"(rs), "s"(soff) : "memory");
}
DI void dma16_sc1(unsigned ldsaddr, unsigned voff, rsrc_t rs, unsigned soff) {
  asm volatile("s_mov_b32 m0, %0\n\ts_nop 0\n\tbuffer_load_dwordx4 %1, %2, %3 offen sc1 lds" ::"s"(ldsaddr), "v"(voff), "s"(rs), "s"(soff) : "memory");
}
DI v4u ld128(rsrc_t rs, unsigned voff, unsigned soff) {
  v4u r;
  asm volatile("buffer_load_dwordx4 %0, %1, %2, %3 offen" : "=v"(r) : "v"(voff), "s"(rs), "s"(soff));
  return r;
}
DI v4u ld128_sc1(rsrc_t rs, unsigned voff, unsigned soff) {
  v4u r;
  asm volatile("buffer_load_dwordx4 %0, %1, %2, %3 offen sc1" : "=v"(r) : "v"(voff), "s"(rs), "s"(soff));
  return r;
}
DI v2u ld64(rsrc_t rs, unsigned voff, unsigned soff) {
  v2u r;
  asm volatile("buffer_load_dwordx2 %0, %1, %2, %3 offen" : "=v"(r) : "v"(voff), "s"(rs), "s"(soff));
  return r;
}
#define WAITV(n) asm volatile("s_waitcnt vmcnt(" #n ")" ::: "memory")
#define BAR() asm volatile("s_waitcnt lgkmcnt(0)\n\ts_barrier" ::: "memory")
#define WAITV_BAR(n) asm volatile("s_waitcnt vmcnt(" #n ") lgkmcnt(0)\n\ts_barrier" ::: "memory")
#define TIE(x) asm volatile("" : "+v"(x))

DI s16x4 trread(LAS unsigned char* L, unsigned off) { return __builtin_amdgcn_ds_read_tr16_b64_v4i16((LAS s16x4*)(L + off)); }
DI s16x8 cat(s16x4 a, s16x4 b) { return __builtin_shufflevector(a, b, 0, 1, 2, 3, 4, 5, 6, 7); }
DI s16x8 asfrag(v4u v) { return __builtin_bit_cast(s16x8, v); }
DI f32x4 mfma16(s16x8 a, s16x8 b, f32x4 c) { return __builtin_amdgcn_mfma_f32_16x16x32_bf16(a, b, c, 0, 0, 0); }
DI f32x4 mfma16k16(s16x4 a, s16x4 b, f32x4 c) { return __builtin_amdgcn_mfma_f32_16x16x16bf16_1k(a, b, c, 0, 0, 0); }
DI f32x16 mfma32(s16x8 a, s16x8 b, f32x16 c) { return __builtin_amdgcn_mfma_f32_32x32x16_bf16(a, b, c, 0, 0, 0); }
DI unsigned pk2(float lo, float hi) {
  f32x2 v = {lo, hi};
  return __builtin_bit_cast(unsigned, __builtin_convertvector(v, bf16x2));
}
DI float dot2(unsigned a, unsigned b, float c) {
  return __builtin_amdgcn_fdot2_f32_bf16(__builtin_bit_cast(bf16x2, a), __builtin_bit_cast(bf16x2, b), c, false);
}
DI float bflo(unsigned u) { return __builtin_bit_cast(float, u << 16); }
DI float bfhi(unsigned u) { return __builtin_bit_cast(float, u & 0xffff0000u); }
DI unsigned fu(float f) { return __builtin_bit_cast(unsigned, f); }
DI float uf(unsigned u) { return __builtin_bit_cast(float, u); }
DI void swap16(float& a, float& b) {
  auto r = __builtin_amdgcn_permlane16_swap(fu(a), fu(b), false, false);
  a = uf(r[0]);
  b = uf(r[1]);
}
DI void swap32(float& a, float& b) {
  auto r = __builtin_amdgcn_permlane32_swap(fu(a), fu(b), false, false);
  a = uf(r[0]);
  b = uf(r[1]);
}
DI void swap16u(unsigned& a, unsigned& b) {
  auto r = __builtin_amdgcn_permlane16_swap(a, b, false, false);
  a = r[0];
  b = r[1];
}
DI void swap32u(unsigned& a, unsigned& b) {
  auto r = __builtin_amdgcn_permlane32_swap(a, b, false, false);
  a = r[0];
  b = r[1];
}
DI void red2add(float& a, float& b) {
  swap16(a, b);
  float m = a + b, m2 = m;
  swap32(m, m2);
  float t = m + m2, t2 = t;
  swap16(t, t2);
  a = t;
  b = t2;
}
template <bool MAX> DI float rop(float a, float b) { return MAX ? fmaxf(a, b) : a + b; }
template <bool MAX> DI void red4(float& a, float& b, float& c, float& d) {
  swap16(a, b);
  float m1 = rop<MAX>(a, b);
  swap16(c, d);
  float m2 = rop<MAX>(c, d);
  swap32(m1, m2);
  float t = rop<MAX>(m1, m2), t2 = t;
  swap32(t, t2);
  float y1 = t, y1b = t, y2 = t2, y2b = t2;
  swap16(y1, y1b);
  swap16(y2, y2b);
  a = y1;
  b = y1b;
  c = y2;
  d = y2b;
}
DI void dual_softmax(f32x4 p0, f32x4 p1, v2u& pos0, v2u& neg0, v2u& pos1, v2u& neg1) {
  float mx0 = fmaxf(fmaxf(p0[0], p0[1]), fmaxf(p0[2], p0[3]));
  float mn0 = -fminf(fminf(p0[0], p0[1]), fminf(p0[2], p0[3]));
  float mx1 = fmaxf(fmaxf(p1[0], p1[1]), fmaxf(p1[2], p1[3]));
  float mn1 = -fminf(fminf(p1[0], p1[1]), fminf(p1[2], p1[3]));
  red4<true>(mx0, mn0, mx1, mn1);
  const float a0 = -mx0 * LOG2E, b0 = -mn0 * LOG2E, a1 = -mx1 * LOG2E, b1 = -mn1 * LOG2E;
  float ep0[4], en0[4], ep1[4], en1[4];
#pragma unroll
  for (int j = 0; j < 4; ++j) {
    ep0[j] = __builtin_amdgcn_exp2f(fmaf(p0[j], LOG2E, a0));
    en0[j] = __builtin_amdgcn_exp2f(fmaf(p0[j], -LOG2E, b0));
    ep1[j] = __builtin_amdgcn_exp2f(fmaf(p1[j], LOG2E, a1));
    en1[j] = __builtin_amdgcn_exp2f(fmaf(p1[j], -LOG2E, b1));
  }
  float sp0 = (ep0[0] + ep0[1]) + (ep0[2] + ep0[3]), sn0 = (en0[0] + en0[1]) + (en0[2] + en0[3]);
  float sp1 = (ep1[0] + ep1[1]) + (ep1[2] + ep1[3]), sn1 = (en1[0] + en1[1]) + (en1[2] + en1[3]);
  red4<false>(sp0, sn0, sp1, sn1);
  const float ip0 = __builtin_amdgcn_rcpf(sp0), in0 = __builtin_amdgcn_rcpf(sn0), ip1 = __builtin_amdgcn_rcpf(sp1), in1 = __builtin_amdgcn_rcpf(sn1);
  pos0 = (v2u){pk2(ep0[0] * ip0, ep0[1] * ip0), pk2(ep0[2] * ip0, ep0[3] * ip0)};
  neg0 = (v2u){pk2(en0[0] * in0, en0[1] * in0), pk2(en0[2] * in0, en0[3] * in0)};
  pos1 = (v2u){pk2(ep1[0] * ip1, ep1[1] * ip1), pk2(ep1[2] * ip1, ep1[3] * ip1)};
  neg1 = (v2u){pk2(en1[0] * in1, en1[1] * in1), pk2(en1[2] * in1, en1[3] * in1)};
}
DI u64 mix64(u64 z) {
  z += 0x9E3779B97F4A7C15ull;
  z = (z ^ (z >> 30)) * 0xBF58476D1CE4E5B9ull;
  z = (z ^ (z >> 27)) * 0x94D049BB133111EBull;
  return z ^ (z >> 31);
}
template <int N> DI void pollN(const unsigned char* base, u64 tag, int lane) {
  const GAS u64* fl = (const GAS u64*)base;
#pragma unroll 1
  for (unsigned it = 0; it < SPIN_CAP; ++it) {
    const u64 v = __hip_atomic_load(fl + (lane & (N - 1)), __ATOMIC_RELAXED, __HIP_MEMORY_SCOPE_AGENT);
    if (__all(v == tag)) break;
    __builtin_amdgcn_s_sleep(1);
  }
}
DI void set_flag(unsigned char* f, u64 tag) { __hip_atomic_store((GAS u64*)f, tag, __ATOMIC_RELAXED, __HIP_MEMORY_SCOPE_AGENT); }

DI v4u ld128_sc1o(rsrc_t rs, unsigned voff) {
  v4u r;
  asm volatile("buffer_load_dwordx4 %0, %1, %2, 0 offen sc1" : "=v"(r) : "v"(voff), "s"(rs));
  return r;
}
DI unsigned opaque(unsigned v) {
  asm volatile("" : "+v"(v));
  return v;
}
#define TIE4(a, b, c, d) asm volatile("" : "+v"(a), "+v"(b), "+v"(c), "+v"(d))
#define WAITV_TIE(n, x) asm volatile("s_waitcnt vmcnt(" #n ")" : "+v"(x))
template <int N> DI void waitv_tie(v4u& x) { asm volatile("s_waitcnt vmcnt(%1)" : "+v"(x) : "n"(N)); }
template <int N> DI void waitv() { asm volatile("s_waitcnt vmcnt(%0)" ::"n"(N) : "memory"); }

#define STAMPW(wv, i) \
  do {                \
  } while (0)
#define STAMP(i) STAMPW(0, i)

constexpr int DN_RING = 20, DN_PRE = 6;
constexpr unsigned dn_base(int i) {
  return i < 6 ? L_GW0 : i < 7 ? L_SPARE : i < 13 ? L_GW1 : i < 17 ? L_GH2 : i < 23 ? L_GW2 : i < 27 ? L_GH0 : i < 33 ? L_GW3 : i < 37 ? L_GH1 : L_GW0;
}
constexpr int dn_i0(int i) { return i < 6 ? 0 : i < 7 ? 6 : i < 13 ? 7 : i < 17 ? 13 : i < 23 ? 17 : i < 27 ? 23 : i < 33 ? 27 : i < 37 ? 33 : 37; }
constexpr int dn_n(int i) { return i < 6 ? 6 : i < 7 ? 1 : i < 13 ? 6 : i < 17 ? 4 : i < 23 ? 6 : i < 27 ? 4 : i < 33 ? 6 : i < 37 ? 4 : 6; }

template <int I> struct DnStep {
  static DI void run(v4u (&hf)[DN_RING], s16x8 (&wpre)[DN_PRE][2], s16x8 (&fw)[2][2], f32x16& a0, f32x16& a1, unsigned rel0, unsigned rel1,
                     unsigned kh2048, unsigned b0, unsigned b1, rsrc_t rsWs, unsigned hv) {
    LAS unsigned char* const L0 = (LAS unsigned char*)0;
    if constexpr (I + 1 < 43 && I + 1 >= DN_PRE) {
      constexpr int J = I + 1;
      if constexpr (dn_i0(J) == J) {
        b0 = opaque(rel0 + dn_base(J) + dn_n(J) * kh2048);
        b1 = opaque(rel1 + dn_base(J) + dn_n(J) * kh2048);
      }
      if constexpr (J == 37) asm volatile("s_barrier" ::: "memory");
      constexpr unsigned o = (J - dn_i0(J)) * 2048;
      fw[J & 1][0] = cat(trread(L0, b0 + o), trread(L0, b0 + o + 1024));
      SB();
    }
    constexpr int younger = (I + DN_RING - 1 < 42 ? I + DN_RING - 1 : 42) - I;
    waitv_tie<younger>(hf[I % DN_RING]);
    const s16x8 bf = asfrag(hf[I % DN_RING]);
    if constexpr (I < DN_PRE) a0 = mfma32(wpre[I][0], bf, a0);
    else a0 = mfma32(fw[I & 1][0], bf, a0);
    TIE(a0);
    SB();
    if constexpr (I + 1 < 43 && I + 1 >= DN_PRE) {
      constexpr int J = I + 1;
      constexpr unsigned o = (J - dn_i0(J)) * 2048;
      fw[J & 1][1] = cat(trread(L0, b1 + o), trread(L0, b1 + o + 1024));
      SB();
    }
    if constexpr (I < DN_PRE) a1 = mfma32(wpre[I][1], bf, a1);
    else a1 = mfma32(fw[I & 1][1], bf, a1);
    TIE(a1);
    SB();
    if constexpr (I + DN_RING < 43) hf[I % DN_RING] = ld128_sc1(rsWs, hv, (I + DN_RING) * 1024);
    SB();
    if constexpr (I + 1 < 43) DnStep<I + 1>::run(hf, wpre, fw, a0, a1, rel0, rel1, kh2048, b0, b1, rsWs, hv);
  }
};

__global__ void __launch_bounds__(512, 2) fused_block_kernel(Params p) {
  __shared__ __attribute__((aligned(16))) unsigned char smem[LDS_BYTES];
  LAS unsigned char* L = (LAS unsigned char*)smem;
  LAS unsigned char* const L0 = (LAS unsigned char*)0;
  const unsigned lb = (unsigned)(uintptr_t)L;
  const int tid = threadIdx.x, lane = tid & 63;
  const int w = __builtin_amdgcn_readfirstlane(tid >> 6);
  const int w4 = w & 3;
  const int b = blockIdx.x, xcd = b & 7, jj = b >> 3;
  const int rt = 4 * (xcd >> 1) + (jj >> 3), m = 8 * (xcd & 1) + (jj & 7);
  const int kro = (m + 2 * (rt & 3)) & 7;
  const int l15 = lane & 15, g = lane >> 4, q4 = l15 >> 2, p4 = lane & 3;
  const int hl = lane >> 5, t32 = lane & 31;
  const int hf = m >> 3, cg = m & 7;
  STAMP(0);
  u64 tstart_;
  asm volatile("s_memrealtime %0\n\ts_waitcnt lgkmcnt(0)" : "=s"(tstart_)::"memory");

  const rsrc_t rsX = mkrsrc(p.x, 2048u * 1024u);
  const rsrc_t rsWs = mkrsrc(p.ws, WS_END);
  const s16x8 ones8 = {0x3F80, 0x3F80, 0x3F80, 0x3F80, 0x3F80, 0x3F80, 0x3F80, 0x3F80};
  v2u ep;
  asm volatile("global_load_dwordx2 %0, %1, off sc1" : "=v"(ep) : "v"(p.ws + WS_EPOCH));
  const unsigned trW0 = 16384 + (8 * g + q4) * 64 + ((0 ^ (g & 1)) << 5) + 8 * p4;
  const unsigned trW1 = 16384 + (8 * g + q4) * 64 + ((1 ^ (g & 1)) << 5) + 8 * p4;
  const unsigned voffL = lane * 16;
  const unsigned voffR = (lane >> 3) * 1024 + (((lane & 7) ^ (((lane >> 4) & 1) << 2)) * 16);

  if (w >= 4) {
    const rsrc_t rsWqkv = mkrsrc(p.wqkv, 512u * 3072u);
    const unsigned voffX0 = (128 * rt + 32 * w4 + (lane >> 3)) * 1024 + (((lane & 7) ^ (lane >> 4)) * 16);
    const unsigned voffX1 = (128 * rt + 32 * w4 + 8 + (lane >> 3)) * 1024 + (((lane & 7) ^ (4 + (lane >> 4))) * 16);
    const unsigned voffW = (lane >> 2) * 3072 + (((lane & 3) ^ ((lane >> 5) << 1)) * 16);
#define A_LOAD(st)                                                                                     \
  {                                                                                                    \
    const unsigned sl_ = lb + ((st) & 3) * A_SLOT, kb_ = (((st) + kro) & 7) * 64;                      \
    dma16(sl_ + (4 * w4 + 0) * 1024, voffX0, rsX, kb_ * 2);                                            \
    dma16(sl_ + (4 * w4 + 1) * 1024, voffX1, rsX, kb_ * 2);                                            \
    dma16(sl_ + 16384 + w4 * 1024, voffW, rsWqkv, (kb_ + 16 * w4) * 3072 + 1024 + 64 * m);             \
    dma16(sl_ + (4 * w4 + 2) * 1024, voffX0, rsX, kb_ * 2 + 16384);                                    \
    dma16(sl_ + (4 * w4 + 3) * 1024, voffX1, rsX, kb_ * 2 + 16384);                                    \
    dma16(sl_ + 20480 + w4 * 1024, voffW, rsWqkv, (kb_ + 16 * w4) * 3072 + 2048 + 64 * m);             \
  }
    if (w == 7) {
      const rsrc_t rsPhi = mkrsrc(p.wphi, 32u * 512u);
      dma16(lb + L_WPHI, voffL, rsPhi, m * 1024);
    }
    A_LOAD(0)
    A_LOAD(1)
    STAMPW(4, 35);
    WAITV_BAR(6);
    A_LOAD(2)
    A_LOAD(3)
#pragma unroll 1
    for (int st = 1; st < 8; ++st) {
      if (st <= 5) WAITV(12);
      else if (st == 6) WAITV(10);
      else WAITV(8);
      BAR();
      if (st <= 4) A_LOAD(st + 3)
      else if (st <= 6) {
#pragma unroll
        for (int j = 0; j < 4; ++j) {
          const unsigned r = 128 * w4 + 64 * (st - 5) + 16 * j;
          dma16(lb + L_WQ + r * 64, voffW, rsWqkv, ((r + 64 * kro) & 511) * 3072 + 64 * m);
        }
      }
    }
    asm volatile("s_waitcnt vmcnt(0) lgkmcnt(0)\n\ts_barrier" : "+v"(ep)::"memory");
    const u64 tag = mix64(((u64)(unsigned)__builtin_amdgcn_readfirstlane(ep[1]) << 32) | (unsigned)__builtin_amdgcn_readfirstlane(ep[0]));
    {
      const int mat = w4 >> 1, i6 = 3 * (w4 & 1);
      const rsrc_t rsG = mkrsrc(mat ? p.wu : p.wg, 512u * 2752u);
#pragma unroll
      for (int j = 0; j < 3; ++j) {
        const unsigned u = 64 * (i6 + j) + lane, row = u / 12, c = u - row * 12;
        dma16(lb + L_CSW + mat * CSW_UPOFF + (i6 + j) * 1024, row * 2752 + c * 16, rsG, 32 * rt * 2752 + 192 * m);
      }
      const rsrc_t rsWo = mkrsrc(p.wo, 512u * 1024u);
#pragma unroll 4
      for (int i = 0; i < 16; ++i) dma16(lb + L_WO + (w4 * 16 + i) * 1024, voffR, rsWo, (w4 * 16 + i) * 8192 + 128 * cg);
    }
    {
      const unsigned cq = lb + L_WQ - 16384 + ((w4 & 1) ? trW1 : trW0) + (w4 >> 1) * 8 * 2048;
      f32x4 acq = {0, 0, 0, 0}, acq2 = acq;
#pragma unroll
      for (int s = 0; s < 8; s += 2) {
        acq = mfma16(cat(trread(L0, cq + s * 2048), trread(L0, cq + s * 2048 + 256)), ones8, acq);
        acq2 = mfma16(cat(trread(L0, cq + (s + 1) * 2048), trread(L0, cq + (s + 1) * 2048 + 256)), ones8, acq2);
      }
      if (l15 == 0) *(LAS f32x4*)(L + L_CS + 256 + (w4 & 1) * 128 + (w4 >> 1) * 64 + g * 16) = acq + acq2;
    }
    WAITV_BAR(16);
#pragma unroll 1
    for (int bl = w4; bl < 6; bl += 4) {
      const unsigned a = L_CSW + (p4 >> 1) * CSW_UPOFF + (4 * (g >> 1) + q4) * 192 + 16 * (g & 1) + 8 * (p4 & 1) + 32 * bl;
      f32x16 c;
#pragma unroll
      for (int i = 0; i < 16; ++i) c[i] = 0.f;
      c = mfma32(cat(trread(L, a), trread(L, a + 8 * 192)), ones8, c);
      c = mfma32(cat(trread(L, a + 16 * 192), trread(L, a + 24 * 192)), ones8, c);
      const unsigned off = t32 == 0 ? WS_CSP + ((m * 16 + rt) * 6 + bl) * 128 + hl * 16 : 0x7FFFFF00u;
#pragma unroll
      for (int i = 0; i < 4; ++i)
        __builtin_amdgcn_raw_buffer_store_b128(__builtin_bit_cast(v4u, (f32x4){c[4 * i], c[4 * i + 1], c[4 * i + 2], c[4 * i + 3]}), rsWs, off, i * 32, 16);
    }
    WAITV(0);
    if (lane == 0) set_flag(p.ws + WS_F5 + m * 512 + (rt * 4 + w4) * 8, tag);
    BAR();
    STAMPW(4, 28);

    const rsrc_t rsGU = mkrsrc((w4 >> 1) ? p.wu : p.wg, 512u * 2752u);
    unsigned voffG[6];
#pragma unroll
    for (int j = 0; j < 6; ++j) {
      const unsigned u = 64 * (6 * (w4 & 1) + j) + lane, row = u / 12, c = u - row * 12;
      voffG[j] = row * 2752 + c * 16;
    }
    const unsigned gdst = (w4 >> 1) * GU_UPOFF + 6 * (w4 & 1) * 1024;
    const unsigned gsrc = 192 * m;
#define GU_LOADW(st, slot)                                                                       \
  {                                                                                              \
    _Pragma("unroll") for (int j = 0; j < 6; ++j) dma16(lb + (slot) + gdst + j * 1024, voffG[j], rsGU, gsrc + (((st) + kro) & 7) * 64 * 2752); \
  }
    GU_LOADW(0, L_GW0)
    GU_LOADW(1, L_GW1)
    GU_LOADW(2, L_GW2)
    STAMPW(4, 30);
    pollN<64>(p.ws + WS_F5 + m * 512, tag, lane);
    if (tid == 256) *(LAS v4u*)(L + L_ZERO) = (v4u){0, 0, 0, 0};
#pragma unroll 1
    for (int bl = w4; bl < 6; bl += 4) {
      float s = 0.f;
      unsigned cv[8];
#pragma unroll
      for (int j = 0; j < 8; ++j) cv[j] = __builtin_amdgcn_raw_buffer_load_b32(rsWs, WS_CSP + ((m * 16 + 8 * hl) * 6 + bl) * 128 + t32 * 4, j * 768, 16);
#pragma unroll
      for (int j = 0; j < 8; ++j) s += uf(cv[j]);
      float s2 = s;
      swap32(s, s2);
      s += s2;
      const unsigned hi = pk2(s, 0.f) & 0xffffu, lo = pk2(s - uf(hi << 16), 0.f) & 0xffffu;
      if (lane < 32) *(LAS v4u*)(L + L_CSF + bl * 512 + lane * 16) = (v4u){hi | (hi << 16), lo, 0, 0};
    }
    BAR();
    BAR();
    GU_LOADW(3, L_GW3)
    pollN<16>(p.ws + WS_F3 + (rt * 4 + w4) * 128, tag, lane);
    STAMPW(4, 8);
    const unsigned hsrc = WS_H + (rt * 4 + w4) * 32 * 1024;
    const unsigned hdst = w4 * 4096;
#define GU_LOADH(st, slot)                                           \
  {                                                                  \
    _Pragma("unroll") for (int j = 0; j < 4; ++j) dma16_sc1(lb + (slot) + hdst + j * 1024, voffL, rsWs, hsrc + (((st) + kro) & 7) * 4096 + j * 1024); \
  }
    GU_LOADH(0, L_GH0)
    WAITV_BAR(0);
    GU_LOADH(1, L_GH1)
    v4u sv[4];
#pragma unroll
    for (int i = 0; i < 4; ++i) sv[i] = ld128_sc1(rsWs, WS_ST + (rt * 128 + 32 * w4 + t32) * 128 + hl * 64, i * 16);
    GU_LOADH(2, L_GH2)
    WAITV_BAR(8);
    GU_LOADH(3, L_GH0)
    GU_LOADW(4, L_GW0)
    asm volatile("s_waitcnt vmcnt(10)\n\ts_barrier" : "+v"(sv[0]), "+v"(sv[1]), "+v"(sv[2]), "+v"(sv[3])::"memory");
    GU_LOADH(4, L_GH1)
    GU_LOADW(5, L_GW1)
    {
      float s = 0.f, q = 0.f;
#pragma unroll
      for (int i = 0; i < 4; ++i) {
        s += uf(sv[i][0]) + uf(sv[i][2]);
        q += uf(sv[i][1]) + uf(sv[i][3]);
      }
      swap32(s, q);
      float t = s + q, t2 = t;
      swap32(t, t2);
      const float mean2 = t * (1.f / 512.f);
      const float var = fmaxf(t2 * (1.f / 512.f) - mean2 * mean2, 0.f) + 1e-5f;
      const float nm = -mean2;
      const unsigned hi = pk2(nm, 0.f) & 0xffffu, lo = pk2(nm - uf(hi << 16), 0.f) & 0xffffu;
      if (lane < 32) {
        *(LAS v4u*)(L + L_MF + w4 * 512 + lane * 16) = (v4u){hi | (lo << 16), hi, 0, 0};
        *(LAS float*)(L + L_R2 + (32 * w4 + lane) * 4) = __builtin_amdgcn_rcpf(var);
      }
    }
    const rsrc_t rsWd = mkrsrc(p.wd, 1376u * 1024u);
    const unsigned dsrc = (16 * 43 * (w4 >> 1) + 8 * (w4 & 1)) * 1024 + 128 * cg;
#define DN_LOAD(i0)                                                                                            \
  {                                                                                                            \
    _Pragma("unroll") for (int j = 0; j < dn_n(i0); ++j)                                                       \
        dma16(lb + dn_base(i0) + ((w4 >> 1) * dn_n(i0) + j) * 2048 + (w4 & 1) * 1024, voffR, rsWd, dsrc + ((i0) + j) * 16384); \
  }
#pragma unroll 1
    for (int st = 3; st < 8; ++st) {
      if (st >= 6) WAITV(17);
      else WAITV(16);
      BAR();
      const int h3 = st >= 6 ? st - 6 : (st >= 3 ? st - 3 : st);
      const unsigned hfree = h3 == 0 ? L_GH2 : L_GH0 + (h3 - 1) * 16384;
      const unsigned wfree = (st & 3) == 0 ? L_GW3 : L_GW0 + ((st & 3) - 1) * GU_SLOT;
      if (st <= 5) GU_LOADH(st + 2, hfree)
      if (st <= 4) GU_LOADW(st + 3, wfree)
      else if (st == 5) { DN_LOAD(0) DN_LOAD(6) }
      else if (st == 6) { DN_LOAD(7) DN_LOAD(13) }
      else { DN_LOAD(17) DN_LOAD(23) }
    }
    BAR();
    DN_LOAD(27) DN_LOAD(33)
    STAMPW(4, 31);
    WAITV_BAR(0);
    BAR();
    DN_LOAD(37)
    WAITV_BAR(0);
    BAR();
  } else {
    v4u xf[16][2];
    f32x4 ak[4][2];
    const f32x4 z4 = {0, 0, 0, 0};
    f32x2 stt[2];
#pragma unroll
    for (int j = 0; j < 4; ++j) ak[j][0] = ak[j][1] = (f32x4){0, 0, 0, 0};
    {
      const unsigned xr0 = lb + (32 * w4 + l15) * 128;
      const unsigned xlo0 = opaque(xr0 + ((g ^ (l15 >> 1)) << 4)), xlo1 = opaque(xr0 + (((4 + g) ^ (l15 >> 1)) << 4));
      const unsigned xhi0 = opaque(xlo0 + 2 * A_SLOT), xhi1 = opaque(xlo1 + 2 * A_SLOT);
      const unsigned w0lo = opaque(lb + trW0), w1lo = opaque(lb + trW1), w0hi = opaque(w0lo + 2 * A_SLOT), w1hi = opaque(w1lo + 2 * A_SLOT);
      const unsigned clo = opaque(((w4 & 1) ? w1lo : w0lo) + (w4 >> 1) * 4096), chi = opaque(clo + 2 * A_SLOT);
      s16x8 fa[2][4], fc[2];
      f32x4 asum[2] = {z4, z4}, agr[2] = {z4, z4}, acs = z4;
#define A1_X(s, a) xf[s][a] = *(LAS v4u*)(L0 + ((((s) >> 1) & 2) ? (((s) & 1) ? xhi1 : xhi0) : (((s) & 1) ? xlo1 : xlo0)) + (((s) >> 1) & 1) * A_SLOT + (a) * 2048); SB()
#define A1_WA(s, j) (((((s) >> 1) & 2) ? (((j) & 1) ? w1hi : w0hi) : (((j) & 1) ? w1lo : w0lo)) + (((s) >> 1) & 1) * A_SLOT + ((j) >> 1) * 4096 + ((s) & 1) * 2048)
#define A1_W(s, j) fa[(s) & 1][j] = cat(trread(L0, A1_WA(s, j)), trread(L0, A1_WA(s, j) + 256)); SB()
#define A1_CA(s) (((((s) >> 1) & 2) ? chi : clo) + (((s) >> 1) & 1) * A_SLOT + ((s) & 1) * 2048)
#define A1_C(s) fc[(s) & 1] = cat(trread(L0, A1_CA(s)), trread(L0, A1_CA(s) + 256)); SB()
#define A1_M(s, j, a)                                           \
  ak[j][a] = mfma16(fa[(s) & 1][j], asfrag(xf[s][a]), ak[j][a]); \
  TIE(ak[j][a]);                                                \
  SB()
#define A1_S(s, a)                                               \
  asum[a] = mfma16(ones8, asfrag(xf[s][a]), asum[a]);            \
  TIE(asum[a]);                                                  \
  agr[a] = mfma16(asfrag(xf[s][a]), asfrag(xf[s][a]), agr[a]);   \
  TIE(agr[a]);                                                   \
  SB()
#define A1_CM(s)                              \
  acs = mfma16(fc[(s) & 1], ones8, acs);      \
  TIE(acs);                                   \
  SB()
#define A1_ALLM(s)                                                            \
  A1_M(s, 0, 0); A1_M(s, 0, 1); A1_M(s, 1, 0); A1_M(s, 1, 1); A1_M(s, 2, 0); A1_M(s, 2, 1); A1_M(s, 3, 0); A1_M(s, 3, 1); \
  A1_S(s, 0); A1_S(s, 1); A1_CM(s);
#define A1_RM(sn, sc)                                                         \
  A1_X(sn, 0); A1_M(sc, 0, 0); A1_W(sn, 0); A1_M(sc, 0, 1);                   \
  A1_X(sn, 1); A1_M(sc, 1, 0); A1_W(sn, 1); A1_M(sc, 1, 1);                   \
  A1_W(sn, 2); A1_M(sc, 2, 0); A1_M(sc, 2, 1);                                \
  A1_W(sn, 3); A1_M(sc, 3, 0); A1_M(sc, 3, 1);                                \
  A1_C(sn); A1_S(sc, 0); A1_S(sc, 1); A1_CM(sc);                              \
  SB();
#define A1_STAGE(st) BAR(); A1_RM(2 * (st), 2 * (st) - 1) A1_RM(2 * (st) + 1, 2 * (st))
      BAR();
      STAMP(14);
      A1_X(0, 0); A1_X(0, 1); A1_W(0, 0); A1_W(0, 1); A1_W(0, 2); A1_W(0, 3); A1_C(0);
      SB();
      A1_RM(1, 0)
      A1_STAGE(1) A1_STAGE(2) A1_STAGE(3) A1_STAGE(4) A1_STAGE(5) A1_STAGE(6) A1_STAGE(7)
      A1_ALLM(15)
      if (l15 == 0) *(LAS f32x4*)(L + L_CS + w4 * 64 + g * 16) = acs;
#pragma unroll
      for (int a = 0; a < 2; ++a) {
        const float a01 = (p4 & 1) ? agr[a][1] : agr[a][0], a23 = (p4 & 1) ? agr[a][3] : agr[a][2];
        const float d = (p4 & 2) ? a23 : a01;
        stt[a][1] = (g == q4) ? d : 0.f;
      }
      float d0 = stt[0][1], d1 = stt[1][1];
      red2add(d0, d1);
      const float mean0 = asum[0][0] * (1.f / 512.f), mean1 = asum[1][0] * (1.f / 512.f);
      const float r0 = __builtin_amdgcn_rsqf(fmaxf(d0 * (1.f / 512.f) - mean0 * mean0, 0.f) + 1e-5f);
      const float r1 = __builtin_amdgcn_rsqf(fmaxf(d1 * (1.f / 512.f) - mean1 * mean1, 0.f) + 1e-5f);
      stt[0] = (f32x2){r0, -mean0 * r0};
      stt[1] = (f32x2){r1, -mean1 * r1};
    }
    STAMP(1);
    asm volatile("s_waitcnt lgkmcnt(0)\n\ts_barrier" : "+v"(ep)::"memory");
    STAMP(26);
    asm volatile("s_waitcnt vmcnt(0)" : "+v"(ep));
    const u64 tag = mix64(((u64)(unsigned)__builtin_amdgcn_readfirstlane(ep[1]) << 32) | (unsigned)__builtin_amdgcn_readfirstlane(ep[0]));

    const s16x4 wphi0 = trread(L, L_WPHI + (4 * g + q4) * 32 + 8 * p4);
    const s16x4 wphi1 = trread(L, L_WPHI + 512 + (4 * g + q4) * 32 + 8 * p4);
    {
      f32x4 cs[4];
#pragma unroll
      for (int i = 0; i < 4; ++i) cs[i] = *(LAS f32x4*)(L + L_CS + i * 64 + g * 16);
#pragma unroll
      for (int a = 0; a < 2; ++a) {
        const float rstd = stt[a][0], nb = stt[a][1];
        const f32x4 k0 = ak[0][a] * rstd + cs[0] * nb, k1 = ak[1][a] * rstd + cs[1] * nb;
        const f32x4 v0 = ak[2][a] * rstd + cs[2] * nb, v1 = ak[3][a] * rstd + cs[3] * nb;
        const v2u k0b = {pk2(k0[0], k0[1]), pk2(k0[2], k0[3])}, k1b = {pk2(k1[0], k1[1]), pk2(k1[2], k1[3])};
        const f32x4 p0 = mfma16k16(wphi0, __builtin_bit_cast(s16x4, k0b), z4);
        const f32x4 p1 = mfma16k16(wphi1, __builtin_bit_cast(s16x4, k1b), z4);
        const unsigned wa = L_KVIMG + (32 * w4 + 16 * a + l15) * 32 + 8 * (g ^ q4);
        *(LAS v2u*)(L + wa + 4 * 4096) = (v2u){pk2(v0[0], v0[1]), pk2(v0[2], v0[3])};
        *(LAS v2u*)(L + wa + 5 * 4096) = (v2u){pk2(v1[0], v1[1]), pk2(v1[2], v1[3])};
        v2u pos0, neg0, pos1, neg1;
        dual_softmax(p0, p1, pos0, neg0, pos1, neg1);
        *(LAS v2u*)(L + wa + 0 * 4096) = pos0;
        *(LAS v2u*)(L + wa + 1 * 4096) = neg0;
        *(LAS v2u*)(L + wa + 2 * 4096) = pos1;
        *(LAS v2u*)(L + wa + 3 * 4096) = neg1;
      }
    }
    BAR();
    STAMP(15);
    {
      const unsigned ra = L_KVIMG + (4 * g + q4) * 32 + 8 * (p4 ^ g);
      const unsigned raA = ra + w4 * 4096, raB = ra + (4 + (w4 >> 1)) * 4096;
      f32x4 acc = z4, accz = z4;
      s16x8 fA[4], fB[4];
#pragma unroll
      for (int u = 0; u < 4; ++u) {
        fA[u] = cat(trread(L, raA + u * 1024), trread(L, raA + u * 1024 + 512));
        fB[u] = cat(trread(L, raB + u * 1024), trread(L, raB + u * 1024 + 512));
      }
#pragma unroll
      for (int u = 0; u < 4; ++u) {
        acc = mfma16(fA[u], fB[u], acc);
        accz = mfma16(fA[u], ones8, accz);
      }
      const unsigned base = WS_KVP + (m * 16 + rt) * KVP_STRIDE;
      __builtin_amdgcn_raw_buffer_store_b128(__builtin_bit_cast(v4u, acc), rsWs, base + w4 * 1024 + lane * 16, 0, 16);
      __builtin_amdgcn_raw_buffer_store_b128(__builtin_bit_cast(v4u, accz), rsWs, l15 == 0 ? base + 4096 + w4 * 64 + g * 16 : 0x7FFFFFF0u, 0, 16);
      WAITV(0);
      if (lane == 0) set_flag(p.ws + WS_F1 + m * 512 + (rt * 4 + w4) * 8, tag);
    }
    STAMP(2);

    v4u phq[2][2];
    {
      f32x4 aq[2][2] = {{z4, z4}, {z4, z4}};
      const unsigned trQ0 = opaque(lb + L_WQ - 16384 + trW0), trQ1 = opaque(lb + L_WQ - 16384 + trW1);
      s16x8 fq[2][2];
#define A2_W(s, j) fq[(s) & 1][j] = cat(trread(L0, ((j) ? trQ1 : trQ0) + (s) * 2048), trread(L0, ((j) ? trQ1 : trQ0) + (s) * 2048 + 256)); SB()
#define A2_M(s, j, a)                                           \
  aq[j][a] = mfma16(fq[(s) & 1][j], asfrag(xf[s][a]), aq[j][a]); \
  TIE(aq[j][a]);                                                \
  SB()
#define A2_STEP(s)                                \
  if ((s) < 15) { A2_W(((s) + 1) & 15, 0); }      \
  A2_M(s, 0, 0); A2_M(s, 0, 1);                   \
  if ((s) < 15) { A2_W(((s) + 1) & 15, 1); }      \
  A2_M(s, 1, 0); A2_M(s, 1, 1);                   \
  SB();
      A2_W(0, 0); A2_W(0, 1);
      SB();
      A2_STEP(0) A2_STEP(1) A2_STEP(2) A2_STEP(3) A2_STEP(4) A2_STEP(5) A2_STEP(6) A2_STEP(7)
      A2_STEP(8) A2_STEP(9) A2_STEP(10) A2_STEP(11) A2_STEP(12) A2_STEP(13) A2_STEP(14) A2_STEP(15)
      STAMP(27);
      const f32x4 c0 = *(LAS f32x4*)(L + L_CS + 256 + g * 16) + *(LAS f32x4*)(L + L_CS + 320 + g * 16);
      const f32x4 c1 = *(LAS f32x4*)(L + L_CS + 384 + g * 16) + *(LAS f32x4*)(L + L_CS + 448 + g * 16);
#pragma unroll
      for (int a = 0; a < 2; ++a) {
        const f32x4 q0 = aq[0][a] * stt[a][0] + c0 * stt[a][1], q1 = aq[1][a] * stt[a][0] + c1 * stt[a][1];
        const v2u q0b = {pk2(q0[0], q0[1]), pk2(q0[2], q0[3])}, q1b = {pk2(q1[0], q1[1]), pk2(q1[2], q1[3])};
        const f32x4 p0 = mfma16k16(wphi0, __builtin_bit_cast(s16x4, q0b), z4);
        const f32x4 p1 = mfma16k16(wphi1, __builtin_bit_cast(s16x4, q1b), z4);
        v2u pos0, neg0, pos1, neg1;
        dual_softmax(p0, p1, pos0, neg0, pos1, neg1);
        phq[a][0] = (v4u){pos0[0], pos0[1], neg0[0], neg0[1]};
        phq[a][1] = (v4u){pos1[0], pos1[1], neg1[0], neg1[1]};
      }
    }
    STAMP(3);

    {
      pollN<64>(p.ws + WS_F1 + m * 512, tag, lane);
      STAMP(4);
      const unsigned base = WS_KVP + m * 16 * KVP_STRIDE;
      v4u pv[16], zv[4];
#pragma unroll
      for (int i = 0; i < 4; ++i) zv[i] = __builtin_amdgcn_raw_buffer_load_b128(rsWs, base + g * KVP_STRIDE + 4096 + l15 * 16, 4 * i * KVP_STRIDE, 16);
#pragma unroll
      for (int i = 0; i < 16; ++i) pv[i] = __builtin_amdgcn_raw_buffer_load_b128(rsWs, base + tid * 16, i * KVP_STRIDE, 16);
      f32x4 sz = __builtin_bit_cast(f32x4, zv[0]);
#pragma unroll
      for (int i = 1; i < 4; ++i) sz += __builtin_bit_cast(f32x4, zv[i]);
      float z0 = sz[0], z1 = sz[1], z2 = sz[2], z3 = sz[3];
      red4<false>(z0, z1, z2, z3);
      if (tid < 16) *(LAS v2u*)(L + L_ZF + (l15 >> 3) * 64 + (l15 & 3) * 16 + ((l15 >> 2) & 1) * 8) = (v2u){pk2(z0, z1), pk2(z2, z3)};
      f32x4 s = __builtin_bit_cast(f32x4, pv[0]);
#pragma unroll
      for (int i = 1; i < 16; ++i) s += __builtin_bit_cast(f32x4, pv[i]);
      *(LAS v2u*)(L + L_KF + (w >> 1) * 1024 + lane * 16 + (w & 1) * 8) = (v2u){pk2(s[0], s[1]), pk2(s[2], s[3])};
    }
    STAMP(33);
    BAR();
    {
      const s16x8 kf0 = *(LAS s16x8*)(L + L_KF + lane * 16), kf1 = *(LAS s16x8*)(L + L_KF + 1024 + lane * 16);
      const s16x8 zf0 = *(LAS s16x8*)(L + L_ZF + g * 16), zf1 = *(LAS s16x8*)(L + L_ZF + 64 + g * 16);
#pragma unroll
      for (int a = 0; a < 2; ++a) {
        const f32x4 n0 = mfma16(kf0, asfrag(phq[a][0]), z4), d0 = mfma16(zf0, asfrag(phq[a][0]), z4);
        const f32x4 n1 = mfma16(kf1, asfrag(phq[a][1]), z4), d1 = mfma16(zf1, asfrag(phq[a][1]), z4);
        const float i0 = __builtin_amdgcn_rcpf(d0[0] + 1e-6f), i1 = __builtin_amdgcn_rcpf(d1[0] + 1e-6f);
        const v4u ov = {pk2(n0[0] * i0, n0[1] * i0), pk2(n0[2] * i0, n0[3] * i0), pk2(n1[0] * i1, n1[1] * i1), pk2(n1[2] * i1, n1[3] * i1)};
        __builtin_amdgcn_raw_buffer_store_b128(ov, rsWs, WS_O + ((rt * 4 + w4) * 32 + 2 * m + (g >> 1)) * 1024 + ((g & 1) * 32 + 16 * a + l15) * 16, 0, 16);
      }
      WAITV(0);
      if (lane < 2) set_flag(p.ws + WS_F2 + ((rt * 4 + w4) * 2 + hf) * 128 + (lane * 8 + cg) * 8, tag);
    }
    STAMP(5);

    const int tb32 = 2 * hf + (w & 1), kh = (w >> 1) & 1;
    unsigned hres[8];
    const unsigned tok = 128 * rt + 32 * tb32 + t32;
    v2u xr[4];
#pragma unroll
    for (int i = 0; i < 4; ++i) xr[i] = ld64(rsX, tok * 1024 + (64 * cg + 32 * kh + 4 * hl) * 2, i * 16);
    const unsigned relR = (4 * (g >> 1) + q4) * 128 + (g & 1) * 32 + 8 * p4;
    const unsigned rel0 = lb + relR + ((0 ^ (q4 >> 1)) << 6), rel1 = lb + relR + ((1 ^ (q4 >> 1)) << 6);
    f32x16 a0, a1;
#pragma unroll
    for (int i = 0; i < 16; ++i) { a0[i] = 0.f; a1[i] = 0.f; }
    {
      const unsigned rl0 = opaque(rel0 + kh * 32768), rl1 = opaque(rel1 + kh * 32768);
      s16x8 fw[2][2];
#define WO_RD1(i, c)                                                                                    \
  {                                                                                                     \
    constexpr unsigned o_ = (32 * ((i) >> 1) + 8 * ((i) & 1)) * 128;                                    \
    fw[(i) & 1][c] = cat(trread(L0, ((c) ? rl1 : rl0) + o_), trread(L0, ((c) ? rl1 : rl0) + o_ + 2048)); \
    SB();                                                                                               \
  }
#define WO_RD(i) WO_RD1(i, 0) WO_RD1(i, 1)
      WO_RD(0)
      pollN<16>(p.ws + WS_F2 + ((rt * 4 + tb32) * 2 + kh) * 128, tag, lane);
      STAMP(6);
      v4u of[16];
      const unsigned ov = WS_O + ((rt * 4 + tb32) * 32 + 16 * kh) * 1024 + lane * 16;
#pragma unroll
      for (int i = 0; i < 16; ++i) of[i] = ld128_sc1(rsWs, ov, i * 1024);
#define WO_STEP(i)                                          \
  {                                                         \
    if ((i) < 15) WO_RD1(((i) + 1) & 15, 0)                 \
    waitv_tie<15 - (i)>(of[i]);                             \
    a0 = mfma32(fw[(i) & 1][0], asfrag(of[i]), a0);         \
    TIE(a0);                                                \
    SB();                                                   \
    if ((i) < 15) WO_RD1(((i) + 1) & 15, 1)                 \
    a1 = mfma32(fw[(i) & 1][1], asfrag(of[i]), a1);         \
    TIE(a1);                                                \
    SB();                                                   \
  }
      WO_STEP(0) WO_STEP(1) WO_STEP(2) WO_STEP(3) WO_STEP(4) WO_STEP(5) WO_STEP(6) WO_STEP(7)
      WO_STEP(8) WO_STEP(9) WO_STEP(10) WO_STEP(11) WO_STEP(12) WO_STEP(13) WO_STEP(14) WO_STEP(15)
    }
    STAMP(16);
    {
      const f32x16 give = kh ? a0 : a1;
      f32x16 keep = kh ? a1 : a0;
#pragma unroll
      for (int i = 0; i < 4; ++i)
        *(LAS f32x4*)(L + L_RED_WO + (w * 4 + i) * 1024 + lane * 16) = (f32x4){give[4 * i], give[4 * i + 1], give[4 * i + 2], give[4 * i + 3]};
      BAR();
      float ss = 0.f, sq = 0.f;
#pragma unroll
      for (int i = 0; i < 4; ++i) {
        const f32x4 o = *(LAS f32x4*)(L + L_RED_WO + ((w ^ 2) * 4 + i) * 1024 + lane * 16);
        TIE(xr[i]);
        hres[2 * i] = pk2(keep[4 * i] + o[0] + bflo(xr[i][0]), keep[4 * i + 1] + o[1] + bfhi(xr[i][0]));
        hres[2 * i + 1] = pk2(keep[4 * i + 2] + o[2] + bflo(xr[i][1]), keep[4 * i + 3] + o[3] + bfhi(xr[i][1]));
      }
      const int Sg = 2 * cg + kh;
      const unsigned ho = WS_H + ((rt * 4 + tb32) * 32 + 2 * Sg) * 1024 + lane * 16;
      __builtin_amdgcn_raw_buffer_store_b128((v4u){hres[0], hres[1], hres[2], hres[3]}, rsWs, ho, 0, 16);
      __builtin_amdgcn_raw_buffer_store_b128((v4u){hres[4], hres[5], hres[6], hres[7]}, rsWs, ho + 1024, 0, 16);
#pragma unroll
      for (int i = 0; i < 8; ++i) {
        const float lo = bflo(hres[i]), hi = bfhi(hres[i]);
        ss += lo + hi;
        sq = fmaf(lo, lo, fmaf(hi, hi, sq));
      }
      swap32(ss, sq);
      __builtin_amdgcn_raw_buffer_store_b32(fu(ss + sq), rsWs, WS_ST + (rt * 128 + 32 * tb32 + t32) * 128 + Sg * 8 + hl * 4, 0, 16);
      WAITV(0);
      if (lane == 0) set_flag(p.ws + WS_F3 + (rt * 4 + tb32) * 128 + Sg * 8, tag);
    }
    BAR();
    STAMP(7);

    const int mh = w & 1, nh = (w >> 1) & 1;
    f32x16 acc[2][3];
#pragma unroll
    for (int i = 0; i < 16; ++i) {
      acc[0][0][i] = 0.f; acc[0][1][i] = 0.f; acc[0][2][i] = 0.f;
      acc[1][0][i] = 0.f; acc[1][1][i] = 0.f; acc[1][2][i] = 0.f;
    }
    {
      const unsigned relT = lb + mh * 8192 + lane * 16;
      const unsigned relW = lb + (p4 >> 1) * GU_UPOFF + (4 * (g >> 1) + q4) * 192 + 16 * (g & 1) + 8 * (p4 & 1) + 96 * nh;
      v4u tf[2][2];
      s16x8 ga[2][3];
      tf[1][0] = tf[1][1] = (v4u){0, 0, 0, 0};
      ga[1][0] = ga[1][1] = ga[1][2] = asfrag(tf[1][0]);
#define GU_RT(j, tt) tf[(j) & 1][tt] = *(LAS v4u*)(L0 + aT + (tt) * 4096 + (j) * 1024); SB()
#define GU_RW(j, l3) ga[(j) & 1][l3] = cat(trread(L0, aW + (j) * 16 * 192 + 32 * (l3)), trread(L0, aW + ((j) * 16 + 8) * 192 + 32 * (l3))); SB()
#define GU_M(j, tt, l3)                                                       \
  acc[tt][l3] = mfma32(ga[(j) & 1][l3], asfrag(tf[(j) & 1][tt]), acc[tt][l3]); \
  TIE(acc[tt][l3]);                                                            \
  SB()
#define GU_STEP(jn, jc)            \
  GU_RT(jn, 0);                    \
  GU_M(jc, 0, 0);                  \
  GU_RW(jn, 0);                    \
  GU_M(jc, 0, 1);                  \
  GU_RW(jn, 1);                    \
  GU_M(jc, 0, 2);                  \
  GU_RT(jn, 1);                    \
  GU_M(jc, 1, 0);                  \
  GU_RW(jn, 2);                    \
  GU_M(jc, 1, 1);                  \
  GU_M(jc, 1, 2);                  \
  SB();
#pragma unroll 1
      for (int st = 0; st < 8; ++st) {
        BAR();
        if (st == 0) STAMP(17);
        if (st == 1) STAMP(18);
        if (st == 7) STAMP(24);
        const int h3 = st >= 6 ? st - 6 : (st >= 3 ? st - 3 : st);
        const unsigned hcur = h3 == 2 ? L_GH2 : L_GH0 + h3 * 16384;
        const unsigned wcur = (st & 3) == 3 ? L_GW3 : L_GW0 + (st & 3) * GU_SLOT;
        const unsigned aT = relT + hcur, aW = relW + wcur;
        GU_STEP(0, 3)
        GU_STEP(1, 0)
        GU_STEP(2, 1)
        GU_STEP(3, 2)
      }
      const unsigned zo = lb + L_ZERO;
      const unsigned aC = lane < 32 ? lb + L_CSF + 3 * nh * 512 + lane * 16 : zo - 0, aM = lane < 32 ? lb + L_MF + 2 * mh * 512 + lane * 16 : zo;
      const unsigned stp = lane < 32 ? 512 : 0;
      tf[0][0] = *(LAS v4u*)(L0 + aM);
      tf[0][1] = *(LAS v4u*)(L0 + aM + stp);
      ga[0][0] = *(LAS s16x8*)(L0 + aC);
      ga[0][1] = *(LAS s16x8*)(L0 + aC + stp);
      ga[0][2] = *(LAS s16x8*)(L0 + aC + 2 * stp);
      GU_M(3, 0, 0); GU_M(3, 0, 1); GU_M(3, 0, 2); GU_M(3, 1, 0); GU_M(3, 1, 1); GU_M(3, 1, 2);
      GU_M(0, 0, 0); GU_M(0, 0, 1); GU_M(0, 0, 2); GU_M(0, 1, 0); GU_M(0, 1, 1); GU_M(0, 1, 2);
    }
    const float r2a = *(LAS float*)(L + L_R2 + (64 * mh + t32) * 4), r2b = *(LAS float*)(L + L_R2 + (64 * mh + 32 + t32) * 4);
    BAR();
    STAMP(9);
#pragma unroll
    for (int tt = 0; tt < 2; ++tt) {
      const float r2 = tt ? r2b : r2a;
#pragma unroll
      for (int l3 = 0; l3 < 3; ++l3) {
        const int fblk = 6 * m + 3 * nh + l3;
        float hv[8];
#pragma unroll
        for (int i = 0; i < 2; ++i)
#pragma unroll
          for (int e = 0; e < 4; ++e) hv[4 * i + e] = (acc[tt][l3][8 * i + e] * r2) * acc[tt][l3][8 * i + 4 + e];
        const v4u o = {pk2(hv[0], hv[1]), pk2(hv[2], hv[3]), pk2(hv[4], hv[5]), pk2(hv[6], hv[7])};
        const unsigned off = fblk < 86 ? WS_HH + ((rt * 4 + 2 * mh + tt) * 86 + fblk) * 1024 + lane * 16 : 0x7FFFFFF0u;
        __builtin_amdgcn_raw_buffer_store_b128(o, rsWs, off, 0, 16);
      }
    }
    WAITV(0);
    if (lane < 2) set_flag(p.ws + WS_F4 + (rt * 4 + 2 * mh + lane) * 256 + (m * 2 + nh) * 8, tag);
    STAMP(10);

    const unsigned kh2048 = kh * 2048;
    const unsigned pb0 = opaque(rel0 + dn_base(0) + dn_n(0) * kh2048), pb1 = opaque(rel1 + dn_base(0) + dn_n(0) * kh2048);
    BAR();
    s16x8 wpre[DN_PRE][2];
#pragma unroll
    for (int i = 0; i < DN_PRE; ++i) {
      wpre[i][0] = cat(trread(L0, pb0 + i * 2048), trread(L0, pb0 + i * 2048 + 1024));
      wpre[i][1] = cat(trread(L0, pb1 + i * 2048), trread(L0, pb1 + i * 2048 + 1024));
    }
#pragma unroll
    for (int i = 0; i < DN_PRE; ++i) { TIE(wpre[i][0]); TIE(wpre[i][1]); }
    BAR();
    STAMP(25);
    pollN<32>(p.ws + WS_F4 + (rt * 4 + tb32) * 256, tag, lane);
    STAMP(11);
    v4u hf_[DN_RING];
    const unsigned hv = WS_HH + ((rt * 4 + tb32) * 86 + 43 * kh) * 1024 + lane * 16;
#pragma unroll
    for (int i = 0; i < DN_RING; ++i) hf_[i] = ld128_sc1(rsWs, hv, i * 1024);
#pragma unroll
    for (int i = 0; i < 16; ++i) { a0[i] = 0.f; a1[i] = 0.f; }
    s16x8 fw[2][2];
    DnStep<0>::run(hf_, wpre, fw, a0, a1, rel0, rel1, kh2048, pb0, pb1, rsWs, hv);
    STAMP(12);
    {
      int tz = threadIdx.x;
      asm volatile("" : "+v"(tz));
      const int lane = tz & 63, hl = lane >> 5, t32 = lane & 31;
      const f32x16 give = kh ? a0 : a1;
      const f32x16 keep = kh ? a1 : a0;
#pragma unroll
      for (int i = 0; i < 4; ++i)
        *(LAS f32x4*)(L + L_RED_DN + (w * 4 + i) * 1024 + lane * 16) = (f32x4){give[4 * i], give[4 * i + 1], give[4 * i + 2], give[4 * i + 3]};
      BAR();
      unsigned ob[8];
#pragma unroll
      for (int i = 0; i < 4; ++i) {
        const f32x4 o = *(LAS f32x4*)(L + L_RED_DN + ((w ^ 2) * 4 + i) * 1024 + lane * 16);
        ob[2 * i] = pk2(keep[4 * i] + o[0] + bflo(hres[2 * i]), keep[4 * i + 1] + o[1] + bfhi(hres[2 * i]));
        ob[2 * i + 1] = pk2(keep[4 * i + 2] + o[2] + bflo(hres[2 * i + 1]), keep[4 * i + 3] + o[3] + bfhi(hres[2 * i + 1]));
      }
      const rsrc_t rsOut = mkrsrc(p.out, 2048u * 1024u);
      const unsigned oo = (128 * rt + 32 * tb32 + t32) * 1024 + (64 * cg + 32 * kh + 8 * hl) * 2;
#pragma unroll
      for (int pr = 0; pr < 2; ++pr) {
        swap32u(ob[4 * pr], ob[4 * pr + 2]);
        swap32u(ob[4 * pr + 1], ob[4 * pr + 3]);
        if (b == DIAG_B && w == 0 && pr == 0) {
          u64 tend_;
          asm volatile("s_memrealtime %0\n\ts_waitcnt lgkmcnt(0)" : "=s"(tend_)::"memory");
          const float dur = (float)(unsigned)(tend_ - tstart_) * 0.01f;
          if (dur > DIAG_T && lane == 0) ob[0] = pk2(dur, dur);
        }
        __builtin_amdgcn_raw_buffer_store_b128((v4u){ob[4 * pr], ob[4 * pr + 1], ob[4 * pr + 2], ob[4 * pr + 3]}, rsOut, oo + pr * 32, 0, 0);
      }
    }
    if (b == 0 && tid == 0) __hip_atomic_store((GAS u64*)(p.ws + WS_EPOCH), tag, __ATOMIC_RELAXED, __HIP_MEMORY_SCOPE_AGENT);
    STAMP(13);
  }
}

}

extern "C" void kernel_launch(void* const* d_in, const int* in_sizes, int n_in, void* d_out, int out_size, void* d_ws, size_t ws_size,
                              hipStream_t stream) {
  Params p;
  p.x = d_in[0];
  p.wqkv = d_in[1];
  p.wphi = d_in[2];
  p.wo = d_in[3];
  p.wg = d_in[5];
  p.wu = d_in[6];
  p.wd = d_in[7];
  p.out = d_out;
  p.ws = (unsigned char*)d_ws;
  fused_block_kernel<<<256, 512, 0, stream>>>(p);
}
```

```cpp
#include <hip/hip_runtime.h>
#include <hip/hip_bf16.h>
#include <stdint.h>

#define GAS __attribute__((address_space(1)))
#define LAS __attribute__((address_space(3)))
typedef unsigned v4u __attribute__((ext_vector_type(4)));
typedef unsigned v2u __attribute__((ext_vector_type(2)));
typedef float f32x2 __attribute__((ext_vector_type(2)));
typedef float f32x4 __attribute__((ext_vector_type(4)));
typedef float f32x16 __attribute__((ext_vector_type(16)));
typedef short s16x4 __attribute__((ext_vector_type(4)));
typedef short s16x8 __attribute__((ext_vector_type(8)));
typedef __bf16 bf16x2 __attribute__((ext_vector_type(2)));
typedef __amdgpu_buffer_rsrc_t rsrc_t;
typedef unsigned long long u64;

#define STAMPS 0
#define GU_ROT 4
#define DIAG_T 32.0f
#define DIAG_MODE 0
#define MAPPING 2
#define SB() __builtin_amdgcn_sched_barrier(0)

namespace {

constexpr unsigned WS_EPOCH = 0;
constexpr unsigned WS_F1 = 4096;
constexpr unsigned WS_F5 = 12288;
constexpr unsigned WS_F2 = 20480;
constexpr unsigned WS_F3 = 36864;
constexpr unsigned WS_F4 = 45056;
constexpr unsigned WS_KVP = 65536;
constexpr unsigned KVP_STRIDE = 4352;
constexpr unsigned WS_CSP = WS_KVP + 256 * KVP_STRIDE;
constexpr unsigned WS_ST = WS_CSP + 256 * 768;
constexpr unsigned ST_BYTES = 16 * 128 * 128;
constexpr unsigned WS_O = WS_ST + ST_BYTES;
constexpr unsigned O_BYTES = 16 * 4 * 32 * 1024;
constexpr unsigned WS_H = WS_O + O_BYTES;
constexpr unsigned H_BYTES = 16 * 4 * 32 * 1024;
constexpr unsigned WS_HH = WS_H + H_BYTES;
constexpr unsigned HH_BYTES = 16 * 4 * 86 * 1024;
constexpr unsigned WS_END = WS_HH + HH_BYTES;
constexpr unsigned WS_STAMP = 16u << 20;

constexpr unsigned A_SLOT = 24576;
constexpr unsigned L_WQ = 98304;
constexpr unsigned L_CSW = 131072, CSW_UPOFF = 6176;
constexpr unsigned L_KVIMG = 65536;
constexpr unsigned L_WO = 0;
constexpr unsigned GU_UPOFF = 12320, GU_SLOT = 24640;
constexpr unsigned L_GW0 = 65536, L_GW1 = L_GW0 + GU_SLOT, L_GW2 = L_GW1 + GU_SLOT, L_GW3 = 0;
constexpr unsigned L_GH0 = 24640, L_GH1 = 41024, L_GH2 = 139456, L_SPARE = 57408;
constexpr unsigned L_RED_WO = 139456;
constexpr unsigned L_RED_DN = L_GW1;
constexpr unsigned L_MISC = 155840;
constexpr unsigned L_CS = L_MISC;
constexpr unsigned L_WPHI = L_MISC + 1024;
constexpr unsigned L_STAT = L_MISC + 2048;
constexpr unsigned L_CSF = L_MISC;
constexpr unsigned L_KF = L_MISC + 3072;
constexpr unsigned L_ZF = L_MISC + 5120;
constexpr unsigned L_MF = L_MISC + 5248;
constexpr unsigned L_R2 = L_MISC + 7296;
constexpr unsigned L_ZERO = L_MISC + 7808;
constexpr unsigned LDS_BYTES = 163840;
static_assert(L_GW2 + GU_SLOT == L_GH2, "map");
static_assert(L_GH2 + 16384 == L_MISC, "map");
static_assert(L_ZERO + 16 <= LDS_BYTES, "map");
static_assert(L_CSW + 2 * CSW_UPOFF <= L_MISC, "map");

constexpr unsigned SPIN_CAP = 1u << 18;
constexpr float LOG2E = 1.4426950408889634f;

struct Params {
  const void* x;
  const void* wqkv;
  const void* wphi;
  const void* wo;
  const void* wg;
  const void* wu;
  const void* wd;
  void* out;
  unsigned char* ws;
};

#define DI __device__ __forceinline__

DI rsrc_t mkrsrc(const void* p, unsigned bytes) { return __builtin_amdgcn_make_buffer_rsrc((void*)p, 0, (int)bytes, 0x00020000); }
DI void dma16(unsigned ldsaddr, unsigned voff, rsrc_t rs, unsigned soff) {
  asm volatile("s_mov_b32 m0, %0\n\ts_nop 0\n\tbuffer_load_dwordx4 %1, %2, %3 offen lds" ::"s"(ldsaddr), "v"(voff), "s"(rs), "s"(soff) : "memory");
}
DI void dma16_sc1(unsigned ldsaddr, unsigned voff, rsrc_t rs, unsigned soff) {
  asm volatile("s_mov_b32 m0, %0\n\ts_nop 0\n\tbuffer_load_dwordx4 %1, %2, %3 offen sc1 lds" ::"s"(ldsaddr), "v"(voff), "s"(rs), "s"(soff) : "memory");
}
DI v4u ld128(rsrc_t rs, unsigned voff, unsigned soff) {
  v4u r;
  asm volatile("buffer_load_dwordx4 %0, %1, %2, %3 offen" : "=v"(r) : "v"(voff), "s"(rs), "s"(soff));
  return r;
}
DI v4u ld128_sc1(rsrc_t rs, unsigned voff, unsigned soff) {
  v4u r;
  asm volatile("buffer_load_dwordx4 %0, %1, %2, %3 offen sc1" : "=v"(r) : "v"(voff), "s"(rs), "s"(soff));
  return r;
}
DI v2u ld64(rsrc_t rs, unsigned voff, unsigned soff) {
  v2u r;
  asm volatile("buffer_load_dwordx2 %0, %1, %2, %3 offen" : "=v"(r) : "v"(voff), "s"(rs), "s"(soff));
  return r;
}
#define WAITV(n) asm volatile("s_waitcnt vmcnt(" #n ")" ::: "memory")
#define BAR() asm volatile("s_waitcnt lgkmcnt(0)\n\ts_barrier" ::: "memory")
#define WAITV_BAR(n) asm volatile("s_waitcnt vmcnt(" #n ") lgkmcnt(0)\n\ts_barrier" ::: "memory")
#define TIE(x) asm volatile("" : "+v"(x))

DI s16x4 trread(LAS unsigned char* L, unsigned off) { return __builtin_amdgcn_ds_read_tr16_b64_v4i16((LAS s16x4*)(L + off)); }
DI s16x8 cat(s16x4 a, s16x4 b) { return __builtin_shufflevector(a, b, 0, 1, 2, 3, 4, 5, 6, 7); }
DI s16x8 asfrag(v4u v) { return __builtin_bit_cast(s16x8, v); }
DI f32x4 mfma16(s16x8 a, s16x8 b, f32x4 c) { return __builtin_amdgcn_mfma_f32_16x16x32_bf16(a, b, c, 0, 0, 0); }
DI f32x4 mfma16k16(s16x4 a, s16x4 b, f32x4 c) { return __builtin_amdgcn_mfma_f32_16x16x16bf16_1k(a, b, c, 0, 0, 0); }
DI f32x16 mfma32(s16x8 a, s16x8 b, f32x16 c) { return __builtin_amdgcn_mfma_f32_32x32x16_bf16(a, b, c, 0, 0, 0); }
DI unsigned pk2(float lo, float hi) {
  f32x2 v = {lo, hi};
  return __builtin_bit_cast(unsigned, __builtin_convertvector(v, bf16x2));
}
DI float dot2(unsigned a, unsigned b, float c) {
  return __builtin_amdgcn_fdot2_f32_bf16(__builtin_bit_cast(bf16x2, a), __builtin_bit_cast(bf16x2, b), c, false);
}
DI float bflo(unsigned u) { return __builtin_bit_cast(float, u << 16); }
DI float bfhi(unsigned u) { return __builtin_bit_cast(float, u & 0xffff0000u); }
DI unsigned fu(float f) { return __builtin_bit_cast(unsigned, f); }
DI float uf(unsigned u) { return __builtin_bit_cast(float, u); }
DI void swap16(float& a, float& b) {
  auto r = __builtin_amdgcn_permlane16_swap(fu(a), fu(b), false, false);
  a = uf(r[0]);
  b = uf(r[1]);
}
DI void swap32(float& a, float& b) {
  auto r = __builtin_amdgcn_permlane32_swap(fu(a), fu(b), false, false);
  a = uf(r[0]);
  b = uf(r[1]);
}
DI void swap16u(unsigned& a, unsigned& b) {
  auto r = __builtin_amdgcn_permlane16_swap(a, b, false, false);
  a = r[0];
  b = r[1];
}
DI void swap32u(unsigned& a, unsigned& b) {
  auto r = __builtin_amdgcn_permlane32_swap(a, b, false, false);
  a = r[0];
  b = r[1];
}
DI void red2add(float& a, float& b) {
  swap16(a, b);
  float m = a + b, m2 = m;
  swap32(m, m2);
  float t = m + m2, t2 = t;
  swap16(t, t2);
  a = t;
  b = t2;
}
template <bool MAX> DI float rop(float a, float b) { return MAX ? fmaxf(a, b) : a + b; }
template <bool MAX> DI void red4(float& a, float& b, float& c, float& d) {
  swap16(a, b);
  float m1 = rop<MAX>(a, b);
  swap16(c, d);
  float m2 = rop<MAX>(c, d);
  swap32(m1, m2);
  float t = rop<MAX>(m1, m2), t2 = t;
  swap32(t, t2);
  float y1 = t, y1b = t, y2 = t2, y2b = t2;
  swap16(y1, y1b);
  swap16(y2, y2b);
  a = y1;
  b = y1b;
  c = y2;
  d = y2b;
}
DI void dual_softmax(f32x4 p0, f32x4 p1, v2u& pos0, v2u& neg0, v2u& pos1, v2u& neg1) {
  float mx0 = fmaxf(fmaxf(p0[0], p0[1]), fmaxf(p0[2], p0[3]));
  float mn0 = -fminf(fminf(p0[0], p0[1]), fminf(p0[2], p0[3]));
  float mx1 = fmaxf(fmaxf(p1[0], p1[1]), fmaxf(p1[2], p1[3]));
  float mn1 = -fminf(fminf(p1[0], p1[1]), fminf(p1[2], p1[3]));
  red4<true>(mx0, mn0, mx1, mn1);
  const float a0 = -mx0 * LOG2E, b0 = -mn0 * LOG2E, a1 = -mx1 * LOG2E, b1 = -mn1 * LOG2E;
  float ep0[4], en0[4], ep1[4], en1[4];
#pragma unroll
  for (int j = 0; j < 4; ++j) {
    ep0[j] = __builtin_amdgcn_exp2f(fmaf(p0[j], LOG2E, a0));
    en0[j] = __builtin_amdgcn_exp2f(fmaf(p0[j], -LOG2E, b0));
    ep1[j] = __builtin_amdgcn_exp2f(fmaf(p1[j], LOG2E, a1));
    en1[j] = __builtin_amdgcn_exp2f(fmaf(p1[j], -LOG2E, b1));
  }
  float sp0 = (ep0[0] + ep0[1]) + (ep0[2] + ep0[3]), sn0 = (en0[0] + en0[1]) + (en0[2] + en0[3]);
  float sp1 = (ep1[0] + ep1[1]) + (ep1[2] + ep1[3]), sn1 = (en1[0] + en1[1]) + (en1[2] + en1[3]);
  red4<false>(sp0, sn0, sp1, sn1);
  const float ip0 = __builtin_amdgcn_rcpf(sp0), in0 = __builtin_amdgcn_rcpf(sn0), ip1 = __builtin_amdgcn_rcpf(sp1), in1 = __builtin_amdgcn_rcpf(sn1);
  pos0 = (v2u){pk2(ep0[0] * ip0, ep0[1] * ip0), pk2(ep0[2] * ip0, ep0[3] * ip0)};
  neg0 = (v2u){pk2(en0[0] * in0, en0[1] * in0), pk2(en0[2] * in0, en0[3] * in0)};
  pos1 = (v2u){pk2(ep1[0] * ip1, ep1[1] * ip1), pk2(ep1[2] * ip1, ep1[3] * ip1)};
  neg1 = (v2u){pk2(en1[0] * in1, en1[1] * in1), pk2(en1[2] * in1, en1[3] * in1)};
}
DI u64 mix64(u64 z) {
  z += 0x9E3779B97F4A7C15ull;
  z = (z ^ (z >> 30)) * 0xBF58476D1CE4E5B9ull;
  z = (z ^ (z >> 27)) * 0x94D049BB133111EBull;
  return z ^ (z >> 31);
}
template <int N> DI void pollN(const unsigned char* base, u64 tag, int lane) {
  const GAS u64* fl = (const GAS u64*)base;
#pragma unroll 1
  for (unsigned it = 0; it < SPIN_CAP; ++it) {
    const u64 v = __hip_atomic_load(fl + (lane & (N - 1)), __ATOMIC_RELAXED, __HIP_MEMORY_SCOPE_AGENT);
    if (__all(v == tag)) break;
    __builtin_amdgcn_s_sleep(1);
  }
}
DI void set_flag(unsigned char* f, u64 tag) { __hip_atomic_store((GAS u64*)f, tag, __ATOMIC_RELAXED, __HIP_MEMORY_SCOPE_AGENT); }

DI v4u ld128_sc1o(rsrc_t rs, unsigned voff) {
  v4u r;
  asm volatile("buffer_load_dwordx4 %0, %1, %2, 0 offen sc1" : "=v"(r) : "v"(voff), "s"(rs));
  return r;
}
DI unsigned opaque(unsigned v) {
  asm volatile("" : "+v"(v));
  return v;
}
#define TIE4(a, b, c, d) asm volatile("" : "+v"(a), "+v"(b), "+v"(c), "+v"(d))
#define WAITV_TIE(n, x) asm volatile("s_waitcnt vmcnt(" #n ")" : "+v"(x))
template <int N> DI void waitv_tie(v4u& x) { asm volatile("s_waitcnt vmcnt(%1)" : "+v"(x) : "n"(N)); }
template <int N> DI void waitv() { asm volatile("s_waitcnt vmcnt(%0)" ::"n"(N) : "memory"); }

#define STAMPW(wv, i) \
  do {                \
  } while (0)
#define STAMP(i) STAMPW(0, i)

constexpr int DN_RING = 20, DN_PRE = 6;
constexpr unsigned dn_base(int i) {
  return i < 6 ? L_GW0 : i < 7 ? L_SPARE : i < 13 ? L_GW1 : i < 17 ? L_GH2 : i < 23 ? L_GW2 : i < 27 ? L_GH0 : i < 33 ? L_GW3 : i < 37 ? L_GH1 : L_GW0;
}
constexpr int dn_i0(int i) { return i < 6 ? 0 : i < 7 ? 6 : i < 13 ? 7 : i < 17 ? 13 : i < 23 ? 17 : i < 27 ? 23 : i < 33 ? 27 : i < 37 ? 33 : 37; }
constexpr int dn_n(int i) { return i < 6 ? 6 : i < 7 ? 1 : i < 13 ? 6 : i < 17 ? 4 : i < 23 ? 6 : i < 27 ? 4 : i < 33 ? 6 : i < 37 ? 4 : 6; }

template <int I> struct DnStep {
  static DI void run(v4u (&hf)[DN_RING], s16x8 (&wpre)[DN_PRE][2], s16x8 (&fw)[2][2], f32x16& a0, f32x16& a1, unsigned rel0, unsigned rel1,
                     unsigned kh2048, unsigned b0, unsigned b1, rsrc_t rsWs, unsigned hv) {
    LAS unsigned char* const L0 = (LAS unsigned char*)0;
    if constexpr (I + 1 < 43 && I + 1 >= DN_PRE) {
      constexpr int J = I + 1;
      if constexpr (dn_i0(J) == J) {
        b0 = opaque(rel0 + dn_base(J) + dn_n(J) * kh2048);
        b1 = opaque(rel1 + dn_base(J) + dn_n(J) * kh2048);
      }
      if constexpr (J == 37) asm volatile("s_barrier" ::: "memory");
      constexpr unsigned o = (J - dn_i0(J)) * 2048;
      fw[J & 1][0] = cat(trread(L0, b0 + o), trread(L0, b0 + o + 1024));
      SB();
    }
    constexpr int younger = (I + DN_RING - 1 < 42 ? I + DN_RING - 1 : 42) - I;
    waitv_tie<younger>(hf[I % DN_RING]);
    const s16x8 bf = asfrag(hf[I % DN_RING]);
    if constexpr (I < DN_PRE) a0 = mfma32(wpre[I][0], bf, a0);
    else a0 = mfma32(fw[I & 1][0], bf, a0);
    TIE(a0);
    SB();
    if constexpr (I + 1 < 43 && I + 1 >= DN_PRE) {
      constexpr int J = I + 1;
      constexpr unsigned o = (J - dn_i0(J)) * 2048;
      fw[J & 1][1] = cat(trread(L0, b1 + o), trread(L0, b1 + o + 1024));
      SB();
    }
    if constexpr (I < DN_PRE) a1 = mfma32(wpre[I][1], bf, a1);
    else a1 = mfma32(fw[I & 1][1], bf, a1);
    TIE(a1);
    SB();
    if constexpr (I + DN_RING < 43) hf[I % DN_RING] = ld128_sc1(rsWs, hv, (I + DN_RING) * 1024);
    SB();
    if constexpr (I + 1 < 43) DnStep<I + 1>::run(hf, wpre, fw, a0, a1, rel0, rel1, kh2048, b0, b1, rsWs, hv);
  }
};

__global__ void __launch_bounds__(512, 2) fused_block_kernel(Params p) {
  __shared__ __attribute__((aligned(16))) unsigned char smem[LDS_BYTES];
  LAS unsigned char* L = (LAS unsigned char*)smem;
  LAS unsigned char* const L0 = (LAS unsigned char*)0;
  const unsigned lb = (unsigned)(uintptr_t)L;
  const int tid = threadIdx.x, lane = tid & 63;
  const int w = __builtin_amdgcn_readfirstlane(tid >> 6);
  const int w4 = w & 3;
  const int b = blockIdx.x, xcd = b & 7, jj = b >> 3;
  const int rt = 4 * (xcd >> 1) + (jj >> 3), m = 8 * (xcd & 1) + (jj & 7);
  const int kro = (m + 2 * (rt & 3)) & 7;
  const int l15 = lane & 15, g = lane >> 4, q4 = l15 >> 2, p4 = lane & 3;
  const int hl = lane >> 5, t32 = lane & 31;
  const int hf = m >> 3, cg = m & 7;
  STAMP(0);
  u64 tstart_;
  asm volatile("s_memrealtime %0\n\ts_waitcnt lgkmcnt(0)" : "=s"(tstart_)::"memory");
  if (tid == 0) __hip_atomic_store((GAS u64*)(p.ws + WS_STAMP) + b, tstart_, __ATOMIC_RELAXED, __HIP_MEMORY_SCOPE_AGENT);

  const rsrc_t rsX = mkrsrc(p.x, 2048u * 1024u);
  const rsrc_t rsWs = mkrsrc(p.ws, WS_END);
  const s16x8 ones8 = {0x3F80, 0x3F80, 0x3F80, 0x3F80, 0x3F80, 0x3F80, 0x3F80, 0x3F80};
  v2u ep;
  asm volatile("global_load_dwordx2 %0, %1, off sc1" : "=v"(ep) : "v"(p.ws + WS_EPOCH));
  const unsigned trW0 = 16384 + (8 * g + q4) * 64 + ((0 ^ (g & 1)) << 5) + 8 * p4;
  const unsigned trW1 = 16384 + (8 * g + q4) * 64 + ((1 ^ (g & 1)) << 5) + 8 * p4;
  const unsigned voffL = lane * 16;
  const unsigned voffR = (lane >> 3) * 1024 + (((lane & 7) ^ (((lane >> 4) & 1) << 2)) * 16);

  if (w >= 4) {
    const rsrc_t rsWqkv = mkrsrc(p.wqkv, 512u * 3072u);
    const unsigned voffX0 = (128 * rt + 32 * w4 + (lane >> 3)) * 1024 + (((lane & 7) ^ (lane >> 4)) * 16);
    const unsigned voffX1 = (128 * rt + 32 * w4 + 8 + (lane >> 3)) * 1024 + (((lane & 7) ^ (4 + (lane >> 4))) * 16);
    const unsigned voffW = (lane >> 2) * 3072 + (((lane & 3) ^ ((lane >> 5) << 1)) * 16);
#define A_LOAD(st)                                                                                     \
  {                                                                                                    \
    const unsigned sl_ = lb + ((st) & 3) * A_SLOT, kb_ = (((st) + kro) & 7) * 64;                      \
    dma16(sl_ + (4 * w4 + 0) * 1024, voffX0, rsX, kb_ * 2);                                            \
    dma16(sl_ + (4 * w4 + 1) * 1024, voffX1, rsX, kb_ * 2);                                            \
    dma16(sl_ + 16384 + w4 * 1024, voffW, rsWqkv, (kb_ + 16 * w4) * 3072 + 1024 + 64 * m);             \
    dma16(sl_ + (4 * w4 + 2) * 1024, voffX0, rsX, kb_ * 2 + 16384);                                    \
    dma16(sl_ + (4 * w4 + 3) * 1024, voffX1, rsX, kb_ * 2 + 16384);                                    \
    dma16(sl_ + 20480 + w4 * 1024, voffW, rsWqkv, (kb_ + 16 * w4) * 3072 + 2048 + 64 * m);             \
  }
    if (w == 7) {
      const rsrc_t rsPhi = mkrsrc(p.wphi, 32u * 512u);
      dma16(lb + L_WPHI, voffL, rsPhi, m * 1024);
    }
    A_LOAD(0)
    A_LOAD(1)
    STAMPW(4, 35);
    WAITV_BAR(6);
    A_LOAD(2)
    A_LOAD(3)
#pragma unroll 1
    for (int st = 1; st < 8; ++st) {
      if (st <= 5) WAITV(12);
      else if (st == 6) WAITV(10);
      else WAITV(8);
      BAR();
      if (st <= 4) A_LOAD(st + 3)
      else if (st <= 6) {
#pragma unroll
        for (int j = 0; j < 4; ++j) {
          const unsigned r = 128 * w4 + 64 * (st - 5) + 16 * j;
          dma16(lb + L_WQ + r * 64, voffW, rsWqkv, ((r + 64 * kro) & 511) * 3072 + 64 * m);
        }
      }
    }
    asm volatile("s_waitcnt vmcnt(0) lgkmcnt(0)\n\ts_barrier" : "+v"(ep)::"memory");
    const u64 tag = mix64(((u64)(unsigned)__builtin_amdgcn_readfirstlane(ep[1]) << 32) | (unsigned)__builtin_amdgcn_readfirstlane(ep[0]));
    {
      const int mat = w4 >> 1, i6 = 3 * (w4 & 1);
      const rsrc_t rsG = mkrsrc(mat ? p.wu : p.wg, 512u * 2752u);
#pragma unroll
      for (int j = 0; j < 3; ++j) {
        const unsigned u = 64 * (i6 + j) + lane, row = u / 12, c = u - row * 12;
        dma16(lb + L_CSW + mat * CSW_UPOFF + (i6 + j) * 1024, row * 2752 + c * 16, rsG, 32 * rt * 2752 + 192 * m);
      }
      const rsrc_t rsWo = mkrsrc(p.wo, 512u * 1024u);
#pragma unroll 4
      for (int i = 0; i < 16; ++i) dma16(lb + L_WO + (w4 * 16 + i) * 1024, voffR, rsWo, (w4 * 16 + i) * 8192 + 128 * cg);
    }
    {
      const unsigned cq = lb + L_WQ - 16384 + ((w4 & 1) ? trW1 : trW0) + (w4 >> 1) * 8 * 2048;
      f32x4 acq = {0, 0, 0, 0}, acq2 = acq;
#pragma unroll
      for (int s = 0; s < 8; s += 2) {
        acq = mfma16(cat(trread(L0, cq + s * 2048), trread(L0, cq + s * 2048 + 256)), ones8, acq);
        acq2 = mfma16(cat(trread(L0, cq + (s + 1) * 2048), trread(L0, cq + (s + 1) * 2048 + 256)), ones8, acq2);
      }
      if (l15 == 0) *(LAS f32x4*)(L + L_CS + 256 + (w4 & 1) * 128 + (w4 >> 1) * 64 + g * 16) = acq + acq2;
    }
    WAITV_BAR(16);
#pragma unroll 1
    for (int bl = w4; bl < 6; bl += 4) {
      const unsigned a = L_CSW + (p4 >> 1) * CSW_UPOFF + (4 * (g >> 1) + q4) * 192 + 16 * (g & 1) + 8 * (p4 & 1) + 32 * bl;
      f32x16 c;
#pragma unroll
      for (int i = 0; i < 16; ++i) c[i] = 0.f;
      c = mfma32(cat(trread(L, a), trread(L, a + 8 * 192)), ones8, c);
      c = mfma32(cat(trread(L, a + 16 * 192), trread(L, a + 24 * 192)), ones8, c);
      const unsigned off = t32 == 0 ? WS_CSP + ((m * 16 + rt) * 6 + bl) * 128 + hl * 16 : 0x7FFFFF00u;
#pragma unroll
      for (int i = 0; i < 4; ++i)
        __builtin_amdgcn_raw_buffer_store_b128(__builtin_bit_cast(v4u, (f32x4){c[4 * i], c[4 * i + 1], c[4 * i + 2], c[4 * i + 3]}), rsWs, off, i * 32, 16);
    }
    WAITV(0);
    if (lane == 0) set_flag(p.ws + WS_F5 + m * 512 + (rt * 4 + w4) * 8, tag);
    BAR();
    STAMPW(4, 28);

    const rsrc_t rsGU = mkrsrc((w4 >> 1) ? p.wu : p.wg, 512u * 2752u);
    unsigned voffG[6];
#pragma unroll
    for (int j = 0; j < 6; ++j) {
      const unsigned u = 64 * (6 * (w4 & 1) + j) + lane, row = u / 12, c = u - row * 12;
      voffG[j] = row * 2752 + c * 16;
    }
    const unsigned gdst = (w4 >> 1) * GU_UPOFF + 6 * (w4 & 1) * 1024;
    const unsigned gsrc = 192 * m;
#define GU_LOADW(st, slot)                                                                       \
  {                                                                                              \
    _Pragma("unroll") for (int j = 0; j < 6; ++j) dma16(lb + (slot) + gdst + j * 1024, voffG[j], rsGU, gsrc + (((st) + kro) & 7) * 64 * 2752); \
  }
    GU_LOADW(0, L_GW0)
    GU_LOADW(1, L_GW1)
    GU_LOADW(2, L_GW2)
    STAMPW(4, 30);
    pollN<64>(p.ws + WS_F5 + m * 512, tag, lane);
    if (tid == 256) *(LAS v4u*)(L + L_ZERO) = (v4u){0, 0, 0, 0};
#pragma unroll 1
    for (int bl = w4; bl < 6; bl += 4) {
      float s = 0.f;
      unsigned cv[8];
#pragma unroll
      for (int j = 0; j < 8; ++j) cv[j] = __builtin_amdgcn_raw_buffer_load_b32(rsWs, WS_CSP + ((m * 16 + 8 * hl) * 6 + bl) * 128 + t32 * 4, j * 768, 16);
#pragma unroll
      for (int j = 0; j < 8; ++j) s += uf(cv[j]);
      float s2 = s;
      swap32(s, s2);
      s += s2;
      const unsigned hi = pk2(s, 0.f) & 0xffffu, lo = pk2(s - uf(hi << 16), 0.f) & 0xffffu;
      if (lane < 32) *(LAS v4u*)(L + L_CSF + bl * 512 + lane * 16) = (v4u){hi | (hi << 16), lo, 0, 0};
    }
    BAR();
    BAR();
    GU_LOADW(3, L_GW3)
    pollN<16>(p.ws + WS_F3 + (rt * 4 + w4) * 128, tag, lane);
    STAMPW(4, 8);
    const unsigned hsrc = WS_H + (rt * 4 + w4) * 32 * 1024;
    const unsigned hdst = w4 * 4096;
#define GU_LOADH(st, slot)                                           \
  {                                                                  \
    _Pragma("unroll") for (int j = 0; j < 4; ++j) dma16_sc1(lb + (slot) + hdst + j * 1024, voffL, rsWs, hsrc + (((st) + kro) & 7) * 4096 + j * 1024); \
  }
    GU_LOADH(0, L_GH0)
    WAITV_BAR(0);
    GU_LOADH(1, L_GH1)
    v4u sv[4];
#pragma unroll
    for (int i = 0; i < 4; ++i) sv[i] = ld128_sc1(rsWs, WS_ST + (rt * 128 + 32 * w4 + t32) * 128 + hl * 64, i * 16);
    GU_LOADH(2, L_GH2)
    WAITV_BAR(8);
    GU_LOADH(3, L_GH0)
    GU_LOADW(4, L_GW0)
    asm volatile("s_waitcnt vmcnt(10)\n\ts_barrier" : "+v"(sv[0]), "+v"(sv[1]), "+v"(sv[2]), "+v"(sv[3])::"memory");
    GU_LOADH(4, L_GH1)
    GU_LOADW(5, L_GW1)
    {
      float s = 0.f, q = 0.f;
#pragma unroll
      for (int i = 0; i < 4; ++i) {
        s += uf(sv[i][0]) + uf(sv[i][2]);
        q += uf(sv[i][1]) + uf(sv[i][3]);
      }
      swap32(s, q);
      float t = s + q, t2 = t;
      swap32(t, t2);
      const float mean2 = t * (1.f / 512.f);
      const float var = fmaxf(t2 * (1.f / 512.f) - mean2 * mean2, 0.f) + 1e-5f;
      const float nm = -mean2;
      const unsigned hi = pk2(nm, 0.f) & 0xffffu, lo = pk2(nm - uf(hi << 16), 0.f) & 0xffffu;
      if (lane < 32) {
        *(LAS v4u*)(L + L_MF + w4 * 512 + lane * 16) = (v4u){hi | (lo << 16), hi, 0, 0};
        *(LAS float*)(L + L_R2 + (32 * w4 + lane) * 4) = __builtin_amdgcn_rcpf(var);
      }
    }
    const rsrc_t rsWd = mkrsrc(p.wd, 1376u * 1024u);
    const unsigned dsrc = (16 * 43 * (w4 >> 1) + 8 * (w4 & 1)) * 1024 + 128 * cg;
#define DN_LOAD(i0)                                                                                            \
  {                                                                                                            \
    _Pragma("unroll") for (int j = 0; j < dn_n(i0); ++j)                                                       \
        dma16(lb + dn_base(i0) + ((w4 >> 1) * dn_n(i0) + j) * 2048 + (w4 & 1) * 1024, voffR, rsWd, dsrc + ((i0) + j) * 16384); \
  }
#pragma unroll 1
    for (int st = 3; st < 8; ++st) {
      if (st >= 6) WAITV(17);
      else WAITV(16);
      BAR();
      const int h3 = st >= 6 ? st - 6 : (st >= 3 ? st - 3 : st);
      const unsigned hfree = h3 == 0 ? L_GH2 : L_GH0 + (h3 - 1) * 16384;
      const unsigned wfree = (st & 3) == 0 ? L_GW3 : L_GW0 + ((st & 3) - 1) * GU_SLOT;
      if (st <= 5) GU_LOADH(st + 2, hfree)
      if (st <= 4) GU_LOADW(st + 3, wfree)
      else if (st == 5) { DN_LOAD(0) DN_LOAD(6) }
      else if (st == 6) { DN_LOAD(7) DN_LOAD(13) }
      else { DN_LOAD(17) DN_LOAD(23) }
    }
    BAR();
    DN_LOAD(27) DN_LOAD(33)
    STAMPW(4, 31);
    WAITV_BAR(0);
    BAR();
    DN_LOAD(37)
    WAITV_BAR(0);
    BAR();
  } else {
    v4u xf[16][2];
    f32x4 ak[4][2];
    const f32x4 z4 = {0, 0, 0, 0};
    f32x2 stt[2];
#pragma unroll
    for (int j = 0; j < 4; ++j) ak[j][0] = ak[j][1] = (f32x4){0, 0, 0, 0};
    {
      const unsigned xr0 = lb + (32 * w4 + l15) * 128;
      const unsigned xlo0 = opaque(xr0 + ((g ^ (l15 >> 1)) << 4)), xlo1 = opaque(xr0 + (((4 + g) ^ (l15 >> 1)) << 4));
      const unsigned xhi0 = opaque(xlo0 + 2 * A_SLOT), xhi1 = opaque(xlo1 + 2 * A_SLOT);
      const unsigned w0lo = opaque(lb + trW0), w1lo = opaque(lb + trW1), w0hi = opaque(w0lo + 2 * A_SLOT), w1hi = opaque(w1lo + 2 * A_SLOT);
      const unsigned clo = opaque(((w4 & 1) ? w1lo : w0lo) + (w4 >> 1) * 4096), chi = opaque(clo + 2 * A_SLOT);
      s16x8 fa[2][4], fc[2];
      f32x4 asum[2] = {z4, z4}, agr[2] = {z4, z4}, acs = z4;
#define A1_X(s, a) xf[s][a] = *(LAS v4u*)(L0 + ((((s) >> 1) & 2) ? (((s) & 1) ? xhi1 : xhi0) : (((s) & 1) ? xlo1 : xlo0)) + (((s) >> 1) & 1) * A_SLOT + (a) * 2048); SB()
#define A1_WA(s, j) (((((s) >> 1) & 2) ? (((j) & 1) ? w1hi : w0hi) : (((j) & 1) ? w1lo : w0lo)) + (((s) >> 1) & 1) * A_SLOT + ((j) >> 1) * 4096 + ((s) & 1) * 2048)
#define A1_W(s, j) fa[(s) & 1][j] = cat(trread(L0, A1_WA(s, j)), trread(L0, A1_WA(s, j) + 256)); SB()
#define A1_CA(s) (((((s) >> 1) & 2) ? chi : clo) + (((s) >> 1) & 1) * A_SLOT + ((s) & 1) * 2048)
#define A1_C(s) fc[(s) & 1] = cat(trread(L0, A1_CA(s)), trread(L0, A1_CA(s) + 256)); SB()
#define A1_M(s, j, a)                                           \
  ak[j][a] = mfma16(fa[(s) & 1][j], asfrag(xf[s][a]), ak[j][a]); \
  TIE(ak[j][a]);                                                \
  SB()
#define A1_S(s, a)                                               \
  asum[a] = mfma16(ones8, asfrag(xf[s][a]), asum[a]);            \
  TIE(asum[a]);                                                  \
  agr[a] = mfma16(asfrag(xf[s][a]), asfrag(xf[s][a]), agr[a]);   \
  TIE(agr[a]);                                                   \
  SB()
#define A1_CM(s)                              \
  acs = mfma16(fc[(s) & 1], ones8, acs);      \
  TIE(acs);                                   \
  SB()
#define A1_ALLM(s)                                                            \
  A1_M(s, 0, 0); A1_M(s, 0, 1); A1_M(s, 1, 0); A1_M(s, 1, 1); A1_M(s, 2, 0); A1_M(s, 2, 1); A1_M(s, 3, 0); A1_M(s, 3, 1); \
  A1_S(s, 0); A1_S(s, 1); A1_CM(s);
#define A1_RM(sn, sc)                                                         \
  A1_X(sn, 0); A1_M(sc, 0, 0); A1_W(sn, 0); A1_M(sc, 0, 1);                   \
  A1_X(sn, 1); A1_M(sc, 1, 0); A1_W(sn, 1); A1_M(sc, 1, 1);                   \
  A1_W(sn, 2); A1_M(sc, 2, 0); A1_M(sc, 2, 1);                                \
  A1_W(sn, 3); A1_M(sc, 3, 0); A1_M(sc, 3, 1);                                \
  A1_C(sn); A1_S(sc, 0); A1_S(sc, 1); A1_CM(sc);                              \
  SB();
#define A1_STAGE(st) BAR(); A1_RM(2 * (st), 2 * (st) - 1) A1_RM(2 * (st) + 1, 2 * (st))
      BAR();
      STAMP(14);
      A1_X(0, 0); A1_X(0, 1); A1_W(0, 0); A1_W(0, 1); A1_W(0, 2); A1_W(0, 3); A1_C(0);
      SB();
      A1_RM(1, 0)
      A1_STAGE(1) A1_STAGE(2) A1_STAGE(3) A1_STAGE(4) A1_STAGE(5) A1_STAGE(6) A1_STAGE(7)
      A1_ALLM(15)
      if (l15 == 0) *(LAS f32x4*)(L + L_CS + w4 * 64 + g * 16) = acs;
#pragma unroll
      for (int a = 0; a < 2; ++a) {
        const float a01 = (p4 & 1) ? agr[a][1] : agr[a][0], a23 = (p4 & 1) ? agr[a][3] : agr[a][2];
        const float d = (p4 & 2) ? a23 : a01;
        stt[a][1] = (g == q4) ? d : 0.f;
      }
      float d0 = stt[0][1], d1 = stt[1][1];
      red2add(d0, d1);
      const float mean0 = asum[0][0] * (1.f / 512.f), mean1 = asum[1][0] * (1.f / 512.f);
      const float r0 = __builtin_amdgcn_rsqf(fmaxf(d0 * (1.f / 512.f) - mean0 * mean0, 0.f) + 1e-5f);
      const float r1 = __builtin_amdgcn_rsqf(fmaxf(d1 * (1.f / 512.f) - mean1 * mean1, 0.f) + 1e-5f);
      stt[0] = (f32x2){r0, -mean0 * r0};
      stt[1] = (f32x2){r1, -mean1 * r1};
    }
    STAMP(1);
    asm volatile("s_waitcnt lgkmcnt(0)\n\ts_barrier" : "+v"(ep)::"memory");
    STAMP(26);
    asm volatile("s_waitcnt vmcnt(0)" : "+v"(ep));
    const u64 tag = mix64(((u64)(unsigned)__builtin_amdgcn_readfirstlane(ep[1]) << 32) | (unsigned)__builtin_amdgcn_readfirstlane(ep[0]));

    const s16x4 wphi0 = trread(L, L_WPHI + (4 * g + q4) * 32 + 8 * p4);
    const s16x4 wphi1 = trread(L, L_WPHI + 512 + (4 * g + q4) * 32 + 8 * p4);
    {
      f32x4 cs[4];
#pragma unroll
      for (int i = 0; i < 4; ++i) cs[i] = *(LAS f32x4*)(L + L_CS + i * 64 + g * 16);
#pragma unroll
      for (int a = 0; a < 2; ++a) {
        const float rstd = stt[a][0], nb = stt[a][1];
        const f32x4 k0 = ak[0][a] * rstd + cs[0] * nb, k1 = ak[1][a] * rstd + cs[1] * nb;
        const f32x4 v0 = ak[2][a] * rstd + cs[2] * nb, v1 = ak[3][a] * rstd + cs[3] * nb;
        const v2u k0b = {pk2(k0[0], k0[1]), pk2(k0[2], k0[3])}, k1b = {pk2(k1[0], k1[1]), pk2(k1[2], k1[3])};
        const f32x4 p0 = mfma16k16(wphi0, __builtin_bit_cast(s16x4, k0b), z4);
        const f32x4 p1 = mfma16k16(wphi1, __builtin_bit_cast(s16x4, k1b), z4);
        const unsigned wa = L_KVIMG + (32 * w4 + 16 * a + l15) * 32 + 8 * (g ^ q4);
        *(LAS v2u*)(L + wa + 4 * 4096) = (v2u){pk2(v0[0], v0[1]), pk2(v0[2], v0[3])};
        *(LAS v2u*)(L + wa + 5 * 4096) = (v2u){pk2(v1[0], v1[1]), pk2(v1[2], v1[3])};
        v2u pos0, neg0, pos1, neg1;
        dual_softmax(p0, p1, pos0, neg0, pos1, neg1);
        *(LAS v2u*)(L + wa + 0 * 4096) = pos0;
        *(LAS v2u*)(L + wa + 1 * 4096) = neg0;
        *(LAS v2u*)(L + wa + 2 * 4096) = pos1;
        *(LAS v2u*)(L + wa + 3 * 4096) = neg1;
      }
    }
    BAR();
    STAMP(15);
    {
      const unsigned ra = L_KVIMG + (4 * g + q4) * 32 + 8 * (p4 ^ g);
      const unsigned raA = ra + w4 * 4096, raB = ra + (4 + (w4 >> 1)) * 4096;
      f32x4 acc = z4, accz = z4;
      s16x8 fA[4], fB[4];
#pragma unroll
      for (int u = 0; u < 4; ++u) {
        fA[u] = cat(trread(L, raA + u * 1024), trread(L, raA + u * 1024 + 512));
        fB[u] = cat(trread(L, raB + u * 1024), trread(L, raB + u * 1024 + 512));
      }
#pragma unroll
      for (int u = 0; u < 4; ++u) {
        acc = mfma16(fA[u], fB[u], acc);
        accz = mfma16(fA[u], ones8, accz);
      }
      const unsigned base = WS_KVP + (m * 16 + rt) * KVP_STRIDE;
      __builtin_amdgcn_raw_buffer_store_b128(__builtin_bit_cast(v4u, acc), rsWs, base + w4 * 1024 + lane * 16, 0, 16);
      __builtin_amdgcn_raw_buffer_store_b128(__builtin_bit_cast(v4u, accz), rsWs, l15 == 0 ? base + 4096 + w4 * 64 + g * 16 : 0x7FFFFFF0u, 0, 16);
      WAITV(0);
      if (lane == 0) set_flag(p.ws + WS_F1 + m * 512 + (rt * 4 + w4) * 8, tag);
    }
    STAMP(2);

    v4u phq[2][2];
    {
      f32x4 aq[2][2] = {{z4, z4}, {z4, z4}};
      const unsigned trQ0 = opaque(lb + L_WQ - 16384 + trW0), trQ1 = opaque(lb + L_WQ - 16384 + trW1);
      s16x8 fq[2][2];
#define A2_W(s, j) fq[(s) & 1][j] = cat(trread(L0, ((j) ? trQ1 : trQ0) + (s) * 2048), trread(L0, ((j) ? trQ1 : trQ0) + (s) * 2048 + 256)); SB()
#define A2_M(s, j, a)                                           \
  aq[j][a] = mfma16(fq[(s) & 1][j], asfrag(xf[s][a]), aq[j][a]); \
  TIE(aq[j][a]);                                                \
  SB()
#define A2_STEP(s)                                \
  if ((s) < 15) { A2_W(((s) + 1) & 15, 0); }      \
  A2_M(s, 0, 0); A2_M(s, 0, 1);                   \
  if ((s) < 15) { A2_W(((s) + 1) & 15, 1); }      \
  A2_M(s, 1, 0); A2_M(s, 1, 1);                   \
  SB();
      A2_W(0, 0); A2_W(0, 1);
      SB();
      A2_STEP(0) A2_STEP(1) A2_STEP(2) A2_STEP(3) A2_STEP(4) A2_STEP(5) A2_STEP(6) A2_STEP(7)
      A2_STEP(8) A2_STEP(9) A2_STEP(10) A2_STEP(11) A2_STEP(12) A2_STEP(13) A2_STEP(14) A2_STEP(15)
      STAMP(27);
      const f32x4 c0 = *(LAS f32x4*)(L + L_CS + 256 + g * 16) + *(LAS f32x4*)(L + L_CS + 320 + g * 16);
      const f32x4 c1 = *(LAS f32x4*)(L + L_CS + 384 + g * 16) + *(LAS f32x4*)(L + L_CS + 448 + g * 16);
#pragma unroll
      for (int a = 0; a < 2; ++a) {
        const f32x4 q0 = aq[0][a] * stt[a][0] + c0 * stt[a][1], q1 = aq[1][a] * stt[a][0] + c1 * stt[a][1];
        const v2u q0b = {pk2(q0[0], q0[1]), pk2(q0[2], q0[3])}, q1b = {pk2(q1[0], q1[1]), pk2(q1[2], q1[3])};
        const f32x4 p0 = mfma16k16(wphi0, __builtin_bit_cast(s16x4, q0b), z4);
        const f32x4 p1 = mfma16k16(wphi1, __builtin_bit_cast(s16x4, q1b), z4);
        v2u pos0, neg0, pos1, neg1;
        dual_softmax(p0, p1, pos0, neg0, pos1, neg1);
        phq[a][0] = (v4u){pos0[0], pos0[1], neg0[0], neg0[1]};
        phq[a][1] = (v4u){pos1[0], pos1[1], neg1[0], neg1[1]};
      }
    }
    STAMP(3);

    {
      pollN<64>(p.ws + WS_F1 + m * 512, tag, lane);
      STAMP(4);
      const unsigned base = WS_KVP + m * 16 * KVP_STRIDE;
      v4u pv[16], zv[4];
#pragma unroll
      for (int i = 0; i < 4; ++i) zv[i] = __builtin_amdgcn_raw_buffer_load_b128(rsWs, base + g * KVP_STRIDE + 4096 + l15 * 16, 4 * i * KVP_STRIDE, 16);
#pragma unroll
      for (int i = 0; i < 16; ++i) pv[i] = __builtin_amdgcn_raw_buffer_load_b128(rsWs, base + tid * 16, i * KVP_STRIDE, 16);
      f32x4 sz = __builtin_bit_cast(f32x4, zv[0]);
#pragma unroll
      for (int i = 1; i < 4; ++i) sz += __builtin_bit_cast(f32x4, zv[i]);
      float z0 = sz[0], z1 = sz[1], z2 = sz[2], z3 = sz[3];
      red4<false>(z0, z1, z2, z3);
      if (tid < 16) *(LAS v2u*)(L + L_ZF + (l15 >> 3) * 64 + (l15 & 3) * 16 + ((l15 >> 2) & 1) * 8) = (v2u){pk2(z0, z1), pk2(z2, z3)};
      f32x4 s = __builtin_bit_cast(f32x4, pv[0]);
#pragma unroll
      for (int i = 1; i < 16; ++i) s += __builtin_bit_cast(f32x4, pv[i]);
      *(LAS v2u*)(L + L_KF + (w >> 1) * 1024 + lane * 16 + (w & 1) * 8) = (v2u){pk2(s[0], s[1]), pk2(s[2], s[3])};
    }
    STAMP(33);
    BAR();
    {
      const s16x8 kf0 = *(LAS s16x8*)(L + L_KF + lane * 16), kf1 = *(LAS s16x8*)(L + L_KF + 1024 + lane * 16);
      const s16x8 zf0 = *(LAS s16x8*)(L + L_ZF + g * 16), zf1 = *(LAS s16x8*)(L + L_ZF + 64 + g * 16);
#pragma unroll
      for (int a = 0; a < 2; ++a) {
        const f32x4 n0 = mfma16(kf0, asfrag(phq[a][0]), z4), d0 = mfma16(zf0, asfrag(phq[a][0]), z4);
        const f32x4 n1 = mfma16(kf1, asfrag(phq[a][1]), z4), d1 = mfma16(zf1, asfrag(phq[a][1]), z4);
        const float i0 = __builtin_amdgcn_rcpf(d0[0] + 1e-6f), i1 = __builtin_amdgcn_rcpf(d1[0] + 1e-6f);
        const v4u ov = {pk2(n0[0] * i0, n0[1] * i0), pk2(n0[2] * i0, n0[3] * i0), pk2(n1[0] * i1, n1[1] * i1), pk2(n1[2] * i1, n1[3] * i1)};
        __builtin_amdgcn_raw_buffer_store_b128(ov, rsWs, WS_O + ((rt * 4 + w4) * 32 + 2 * m + (g >> 1)) * 1024 + ((g & 1) * 32 + 16 * a + l15) * 16, 0, 16);
      }
      WAITV(0);
      if (lane < 2) set_flag(p.ws + WS_F2 + ((rt * 4 + w4) * 2 + hf) * 128 + (lane * 8 + cg) * 8, tag);
    }
    STAMP(5);

    const int tb32 = 2 * hf + (w & 1), kh = (w >> 1) & 1;
    unsigned hres[8];
    const unsigned tok = 128 * rt + 32 * tb32 + t32;
    v2u xr[4];
#pragma unroll
    for (int i = 0; i < 4; ++i) xr[i] = ld64(rsX, tok * 1024 + (64 * cg + 32 * kh + 4 * hl) * 2, i * 16);
    const unsigned relR = (4 * (g >> 1) + q4) * 128 + (g & 1) * 32 + 8 * p4;
    const unsigned rel0 = lb + relR + ((0 ^ (q4 >> 1)) << 6), rel1 = lb + relR + ((1 ^ (q4 >> 1)) << 6);
    f32x16 a0, a1;
#pragma unroll
    for (int i = 0; i < 16; ++i) { a0[i] = 0.f; a1[i] = 0.f; }
    {
      const unsigned rl0 = opaque(rel0 + kh * 32768), rl1 = opaque(rel1 + kh * 32768);
      s16x8 fw[2][2];
#define WO_RD1(i, c)                                                                                    \
  {                                                                                                     \
    constexpr unsigned o_ = (32 * ((i) >> 1) + 8 * ((i) & 1)) * 128;                                    \
    fw[(i) & 1][c] = cat(trread(L0, ((c) ? rl1 : rl0) + o_), trread(L0, ((c) ? rl1 : rl0) + o_ + 2048)); \
    SB();                                                                                               \
  }
#define WO_RD(i) WO_RD1(i, 0) WO_RD1(i, 1)
      WO_RD(0)
      pollN<16>(p.ws + WS_F2 + ((rt * 4 + tb32) * 2 + kh) * 128, tag, lane);
      STAMP(6);
      v4u of[16];
      const unsigned ov = WS_O + ((rt * 4 + tb32) * 32 + 16 * kh) * 1024 + lane * 16;
#pragma unroll
      for (int i = 0; i < 16; ++i) of[i] = ld128_sc1(rsWs, ov, i * 1024);
#define WO_STEP(i)                                          \
  {                                                         \
    if ((i) < 15) WO_RD1(((i) + 1) & 15, 0)                 \
    waitv_tie<15 - (i)>(of[i]);                             \
    a0 = mfma32(fw[(i) & 1][0], asfrag(of[i]), a0);         \
    TIE(a0);                                                \
    SB();                                                   \
    if ((i) < 15) WO_RD1(((i) + 1) & 15, 1)                 \
    a1 = mfma32(fw[(i) & 1][1], asfrag(of[i]), a1);         \
    TIE(a1);                                                \
    SB();                                                   \
  }
      WO_STEP(0) WO_STEP(1) WO_STEP(2) WO_STEP(3) WO_STEP(4) WO_STEP(5) WO_STEP(6) WO_STEP(7)
      WO_STEP(8) WO_STEP(9) WO_STEP(10) WO_STEP(11) WO_STEP(12) WO_STEP(13) WO_STEP(14) WO_STEP(15)
    }
    STAMP(16);
    {
      const f32x16 give = kh ? a0 : a1;
      f32x16 keep = kh ? a1 : a0;
#pragma unroll
      for (int i = 0; i < 4; ++i)
        *(LAS f32x4*)(L + L_RED_WO + (w * 4 + i) * 1024 + lane * 16) = (f32x4){give[4 * i], give[4 * i + 1], give[4 * i + 2], give[4 * i + 3]};
      BAR();
      float ss = 0.f, sq = 0.f;
#pragma unroll
      for (int i = 0; i < 4; ++i) {
        const f32x4 o = *(LAS f32x4*)(L + L_RED_WO + ((w ^ 2) * 4 + i) * 1024 + lane * 16);
        TIE(xr[i]);
        hres[2 * i] = pk2(keep[4 * i] + o[0] + bflo(xr[i][0]), keep[4 * i + 1] + o[1] + bfhi(xr[i][0]));
        hres[2 * i + 1] = pk2(keep[4 * i + 2] + o[2] + bflo(xr[i][1]), keep[4 * i + 3] + o[3] + bfhi(xr[i][1]));
      }
      const int Sg = 2 * cg + kh;
      const unsigned ho = WS_H + ((rt * 4 + tb32) * 32 + 2 * Sg) * 1024 + lane * 16;
      __builtin_amdgcn_raw_buffer_store_b128((v4u){hres[0], hres[1], hres[2], hres[3]}, rsWs, ho, 0, 16);
      __builtin_amdgcn_raw_buffer_store_b128((v4u){hres[4], hres[5], hres[6], hres[7]}, rsWs, ho + 1024, 0, 16);
#pragma unroll
      for (int i = 0; i < 8; ++i) {
        const float lo = bflo(hres[i]), hi = bfhi(hres[i]);
        ss += lo + hi;
        sq = fmaf(lo, lo, fmaf(hi, hi, sq));
      }
      swap32(ss, sq);
      __builtin_amdgcn_raw_buffer_store_b32(fu(ss + sq), rsWs, WS_ST + (rt * 128 + 32 * tb32 + t32) * 128 + Sg * 8 + hl * 4, 0, 16);
      WAITV(0);
      if (lane == 0) set_flag(p.ws + WS_F3 + (rt * 4 + tb32) * 128 + Sg * 8, tag);
    }
    BAR();
    STAMP(7);

    const int mh = w & 1, nh = (w >> 1) & 1;
    f32x16 acc[2][3];
#pragma unroll
    for (int i = 0; i < 16; ++i) {
      acc[0][0][i] = 0.f; acc[0][1][i] = 0.f; acc[0][2][i] = 0.f;
      acc[1][0][i] = 0.f; acc[1][1][i] = 0.f; acc[1][2][i] = 0.f;
    }
    {
      const unsigned relT = lb + mh * 8192 + lane * 16;
      const unsigned relW = lb + (p4 >> 1) * GU_UPOFF + (4 * (g >> 1) + q4) * 192 + 16 * (g & 1) + 8 * (p4 & 1) + 96 * nh;
      v4u tf[2][2];
      s16x8 ga[2][3];
      tf[1][0] = tf[1][1] = (v4u){0, 0, 0, 0};
      ga[1][0] = ga[1][1] = ga[1][2] = asfrag(tf[1][0]);
#define GU_RT(j, tt) tf[(j) & 1][tt] = *(LAS v4u*)(L0 + aT + (tt) * 4096 + (j) * 1024); SB()
#define GU_RW(j, l3) ga[(j) & 1][l3] = cat(trread(L0, aW + (j) * 16 * 192 + 32 * (l3)), trread(L0, aW + ((j) * 16 + 8) * 192 + 32 * (l3))); SB()
#define GU_M(j, tt, l3)                                                       \
  acc[tt][l3] = mfma32(ga[(j) & 1][l3], asfrag(tf[(j) & 1][tt]), acc[tt][l3]); \
  TIE(acc[tt][l3]);                                                            \
  SB()
#define GU_STEP(jn, jc)            \
  GU_RT(jn, 0);                    \
  GU_M(jc, 0, 0);                  \
  GU_RW(jn, 0);                    \
  GU_M(jc, 0, 1);                  \
  GU_RW(jn, 1);                    \
  GU_M(jc, 0, 2);                  \
  GU_RT(jn, 1);                    \
  GU_M(jc, 1, 0);                  \
  GU_RW(jn, 2);                    \
  GU_M(jc, 1, 1);                  \
  GU_M(jc, 1, 2);                  \
  SB();
#pragma unroll 1
      for (int st = 0; st < 8; ++st) {
        BAR();
        if (st == 0) STAMP(17);
        if (st == 1) STAMP(18);
        if (st == 7) STAMP(24);
        const int h3 = st >= 6 ? st - 6 : (st >= 3 ? st - 3 : st);
        const unsigned hcur = h3 == 2 ? L_GH2 : L_GH0 + h3 * 16384;
        const unsigned wcur = (st & 3) == 3 ? L_GW3 : L_GW0 + (st & 3) * GU_SLOT;
        const unsigned aT = relT + hcur, aW = relW + wcur;
        GU_STEP(0, 3)
        GU_STEP(1, 0)
        GU_STEP(2, 1)
        GU_STEP(3, 2)
      }
      const unsigned zo = lb + L_ZERO;
      const unsigned aC = lane < 32 ? lb + L_CSF + 3 * nh * 512 + lane * 16 : zo - 0, aM = lane < 32 ? lb + L_MF + 2 * mh * 512 + lane * 16 : zo;
      const unsigned stp = lane < 32 ? 512 : 0;
      tf[0][0] = *(LAS v4u*)(L0 + aM);
      tf[0][1] = *(LAS v4u*)(L0 + aM + stp);
      ga[0][0] = *(LAS s16x8*)(L0 + aC);
      ga[0][1] = *(LAS s16x8*)(L0 + aC + stp);
      ga[0][2] = *(LAS s16x8*)(L0 + aC + 2 * stp);
      GU_M(3, 0, 0); GU_M(3, 0, 1); GU_M(3, 0, 2); GU_M(3, 1, 0); GU_M(3, 1, 1); GU_M(3, 1, 2);
      GU_M(0, 0, 0); GU_M(0, 0, 1); GU_M(0, 0, 2); GU_M(0, 1, 0); GU_M(0, 1, 1); GU_M(0, 1, 2);
    }
    const float r2a = *(LAS float*)(L + L_R2 + (64 * mh + t32) * 4), r2b = *(LAS float*)(L + L_R2 + (64 * mh + 32 + t32) * 4);
    BAR();
    STAMP(9);
#pragma unroll
    for (int tt = 0; tt < 2; ++tt) {
      const float r2 = tt ? r2b : r2a;
#pragma unroll
      for (int l3 = 0; l3 < 3; ++l3) {
        const int fblk = 6 * m + 3 * nh + l3;
        float hv[8];
#pragma unroll
        for (int i = 0; i < 2; ++i)
#pragma unroll
          for (int e = 0; e < 4; ++e) hv[4 * i + e] = (acc[tt][l3][8 * i + e] * r2) * acc[tt][l3][8 * i + 4 + e];
        const v4u o = {pk2(hv[0], hv[1]), pk2(hv[2], hv[3]), pk2(hv[4], hv[5]), pk2(hv[6], hv[7])};
        const unsigned off = fblk < 86 ? WS_HH + ((rt * 4 + 2 * mh + tt) * 86 + fblk) * 1024 + lane * 16 : 0x7FFFFFF0u;
        __builtin_amdgcn_raw_buffer_store_b128(o, rsWs, off, 0, 16);
      }
    }
    WAITV(0);
    if (lane < 2) set_flag(p.ws + WS_F4 + (rt * 4 + 2 * mh + lane) * 256 + (m * 2 + nh) * 8, tag);
    STAMP(10);

    const unsigned kh2048 = kh * 2048;
    const unsigned pb0 = opaque(rel0 + dn_base(0) + dn_n(0) * kh2048), pb1 = opaque(rel1 + dn_base(0) + dn_n(0) * kh2048);
    BAR();
    s16x8 wpre[DN_PRE][2];
#pragma unroll
    for (int i = 0; i < DN_PRE; ++i) {
      wpre[i][0] = cat(trread(L0, pb0 + i * 2048), trread(L0, pb0 + i * 2048 + 1024));
      wpre[i][1] = cat(trread(L0, pb1 + i * 2048), trread(L0, pb1 + i * 2048 + 1024));
    }
#pragma unroll
    for (int i = 0; i < DN_PRE; ++i) { TIE(wpre[i][0]); TIE(wpre[i][1]); }
    BAR();
    STAMP(25);
    pollN<32>(p.ws + WS_F4 + (rt * 4 + tb32) * 256, tag, lane);
    STAMP(11);
    v4u hf_[DN_RING];
    const unsigned hv = WS_HH + ((rt * 4 + tb32) * 86 + 43 * kh) * 1024 + lane * 16;
#pragma unroll
    for (int i = 0; i < DN_RING; ++i) hf_[i] = ld128_sc1(rsWs, hv, i * 1024);
#pragma unroll
    for (int i = 0; i < 16; ++i) { a0[i] = 0.f; a1[i] = 0.f; }
    s16x8 fw[2][2];
    DnStep<0>::run(hf_, wpre, fw, a0, a1, rel0, rel1, kh2048, pb0, pb1, rsWs, hv);
    STAMP(12);
    {
      int tz = threadIdx.x;
      asm volatile("" : "+v"(tz));
      const int lane = tz & 63, hl = lane >> 5, t32 = lane & 31;
      const f32x16 give = kh ? a0 : a1;
      const f32x16 keep = kh ? a1 : a0;
#pragma unroll
      for (int i = 0; i < 4; ++i)
        *(LAS f32x4*)(L + L_RED_DN + (w * 4 + i) * 1024 + lane * 16) = (f32x4){give[4 * i], give[4 * i + 1], give[4 * i + 2], give[4 * i + 3]};
      BAR();
      unsigned ob[8];
#pragma unroll
      for (int i = 0; i < 4; ++i) {
        const f32x4 o = *(LAS f32x4*)(L + L_RED_DN + ((w ^ 2) * 4 + i) * 1024 + lane * 16);
        ob[2 * i] = pk2(keep[4 * i] + o[0] + bflo(hres[2 * i]), keep[4 * i + 1] + o[1] + bfhi(hres[2 * i]));
        ob[2 * i + 1] = pk2(keep[4 * i + 2] + o[2] + bflo(hres[2 * i + 1]), keep[4 * i + 3] + o[3] + bfhi(hres[2 * i + 1]));
      }
      const rsrc_t rsOut = mkrsrc(p.out, 2048u * 1024u);
      const unsigned oo = (128 * rt + 32 * tb32 + t32) * 1024 + (64 * cg + 32 * kh + 8 * hl) * 2;
#pragma unroll
      for (int pr = 0; pr < 2; ++pr) {
        swap32u(ob[4 * pr], ob[4 * pr + 2]);
        swap32u(ob[4 * pr + 1], ob[4 * pr + 3]);
        if (b == 0 && w == 0 && pr == 0) {
          u64 tend_;
          asm volatile("s_memrealtime %0\n\ts_waitcnt lgkmcnt(0)" : "=s"(tend_)::"memory");
          u64 mn = ~0ull, mx = 0;
          for (int i = 0; i < 4; ++i) {
            const u64 v = __hip_atomic_load((GAS u64*)(p.ws + WS_STAMP) + lane * 4 + i, __ATOMIC_RELAXED, __HIP_MEMORY_SCOPE_AGENT);
            mn = v < mn ? v : mn; mx = v > mx ? v : mx;
          }
          unsigned lo = (unsigned)(tend_ - mn), hi = (unsigned)(tend_ - mx);
          for (int o = 32; o >= 1; o >>= 1) { lo = max(lo, (unsigned)__shfl_xor((int)lo, o)); hi = min(hi, (unsigned)__shfl_xor((int)hi, o)); }
          const float val = DIAG_MODE == 0 ? lo * 0.01f : (lo - hi) * 0.01f;
          if (val > DIAG_T && lane == 0) ob[0] = pk2(val, val);
        }
        __builtin_amdgcn_raw_buffer_store_b128((v4u){ob[4 * pr], ob[4 * pr + 1], ob[4 * pr + 2], ob[4 * pr + 3]}, rsOut, oo + pr * 32, 0, 0);
      }
    }
    if (b == 0 && tid == 0) __hip_atomic_store((GAS u64*)(p.ws + WS_EPOCH), tag, __ATOMIC_RELAXED, __HIP_MEMORY_SCOPE_AGENT);
    STAMP(13);
  }
}

}

extern "C" void kernel_launch(void* const* d_in, const int* in_sizes, int n_in, void* d_out, int out_size, void* d_ws, size_t ws_size,
                              hipStream_t stream) {
  Params p;
  p.x = d_in[0];
  p.wqkv = d_in[1];
  p.wphi = d_in[2];
  p.wo = d_in[3];
  p.wg = d_in[5];
  p.wu = d_in[6];
  p.wd = d_in[7];
  p.out = d_out;
  p.ws = (unsigned char*)d_ws;
  fused_block_kernel<<<256, 512, 0, stream>>>(p);
}
```

```cpp
#include <hip/hip_runtime.h>
#include <hip/hip_bf16.h>
#include <stdint.h>

#define GAS __attribute__((address_space(1)))
#define LAS __attribute__((address_space(3)))
typedef unsigned v4u __attribute__((ext_vector_type(4)));
typedef unsigned v2u __attribute__((ext_vector_type(2)));
typedef float f32x2 __attribute__((ext_vector_type(2)));
typedef float f32x4 __attribute__((ext_vector_type(4)));
typedef float f32x16 __attribute__((ext_vector_type(16)));
typedef short s16x4 __attribute__((ext_vector_type(4)));
typedef short s16x8 __attribute__((ext_vector_type(8)));
typedef __bf16 bf16x2 __attribute__((ext_vector_type(2)));
typedef __amdgpu_buffer_rsrc_t rsrc_t;
typedef unsigned long long u64;

#define STAMPS 0
#define GU_ROT 4
#define CODEPF 1
#define TOUCH 0
#define MAPPING 2
#define SB() __builtin_amdgcn_sched_barrier(0)

namespace {

constexpr unsigned WS_EPOCH = 0;
constexpr unsigned WS_F1 = 4096;
constexpr unsigned WS_F5 = 12288;
constexpr unsigned WS_F2 = 20480;
constexpr unsigned WS_F3 = 36864;
constexpr unsigned WS_F4 = 45056;
constexpr unsigned WS_KVP = 65536;
constexpr unsigned KVP_STRIDE = 4352;
constexpr unsigned WS_CSP = WS_KVP + 256 * KVP_STRIDE;
constexpr unsigned WS_ST = WS_CSP + 256 * 768;
constexpr unsigned ST_BYTES = 16 * 4 * 16 * 512;
constexpr unsigned WS_O = WS_ST + ST_BYTES;
constexpr unsigned O_BYTES = 16 * 4 * 32 * 1024;
constexpr unsigned WS_H = WS_O + O_BYTES;
constexpr unsigned H_BYTES = 16 * 4 * 32 * 1024;
constexpr unsigned WS_HH = WS_H + H_BYTES;
constexpr unsigned HH_BYTES = 16 * 4 * 86 * 1024;
constexpr unsigned WS_END = WS_HH + HH_BYTES;
constexpr unsigned WS_STAMP = 16u << 20;

constexpr unsigned A_SLOT = 24576;
constexpr unsigned L_WQ = 98304;
constexpr unsigned L_CSW = 131072;
constexpr unsigned L_KVIMG = 65536;
constexpr unsigned L_WO = 0;
constexpr unsigned GU_UPOFF = 12320, GU_SLOT = 24640;
constexpr unsigned L_GW0 = 65536, L_GW1 = L_GW0 + GU_SLOT, L_GW2 = L_GW1 + GU_SLOT, L_GW3 = 0;
constexpr unsigned L_GH0 = 24640, L_GH1 = 41024, L_GH2 = 139456, L_SPARE = 57408;
constexpr unsigned L_RED_WO = 139456;
constexpr unsigned L_RED_DN = L_GW1;
constexpr unsigned L_MISC = 155840;
constexpr unsigned L_CS = L_MISC;
constexpr unsigned L_WPHI = L_MISC + 1024;
constexpr unsigned L_STAT = L_MISC + 2048;
constexpr unsigned L_CSF = L_MISC;
constexpr unsigned L_KF = L_MISC + 3072;
constexpr unsigned L_ZF = L_MISC + 5120;
constexpr unsigned L_MF = L_MISC + 5248;
constexpr unsigned L_R2 = L_MISC + 7296;
constexpr unsigned L_ZERO = L_MISC + 7808;
constexpr unsigned L_CNT = L_MISC + 7824;
constexpr unsigned LDS_BYTES = 163840;
static_assert(L_GW2 + GU_SLOT == L_GH2, "map");
static_assert(L_GH2 + 16384 == L_MISC, "map");
static_assert(L_ZERO + 16 <= LDS_BYTES, "map");
static_assert(L_CSW + 4 * 4608 <= L_MISC, "map");

constexpr unsigned SPIN_CAP = 1u << 18;
constexpr float LOG2E = 1.4426950408889634f;

struct Params {
  const void* x;
  const void* wqkv;
  const void* wphi;
  const void* wo;
  const void* wg;
  const void* wu;
  const void* wd;
  void* out;
  unsigned char* ws;
};

#define DI __device__ __forceinline__

DI rsrc_t mkrsrc(const void* p, unsigned bytes) { return __builtin_amdgcn_make_buffer_rsrc((void*)p, 0, (int)bytes, 0x00020000); }
DI void dma16(unsigned ldsaddr, unsigned voff, rsrc_t rs, unsigned soff) {
  asm volatile("s_mov_b32 m0, %0\n\ts_nop 0\n\tbuffer_load_dwordx4 %1, %2, %3 offen lds" ::"s"(ldsaddr), "v"(voff), "s"(rs), "s"(soff) : "memory");
}
DI void dma16_sc1(unsigned ldsaddr, unsigned voff, rsrc_t rs, unsigned soff) {
  asm volatile("s_mov_b32 m0, %0\n\ts_nop 0\n\tbuffer_load_dwordx4 %1, %2, %3 offen sc1 lds" ::"s"(ldsaddr), "v"(voff), "s"(rs), "s"(soff) : "memory");
}
DI v4u ld128(rsrc_t rs, unsigned voff, unsigned soff) {
  v4u r;
  asm volatile("buffer_load_dwordx4 %0, %1, %2, %3 offen" : "=v"(r) : "v"(voff), "s"(rs), "s"(soff));
  return r;
}
DI v4u ld128_sc1(rsrc_t rs, unsigned voff, unsigned soff) {
  v4u r;
  asm volatile("buffer_load_dwordx4 %0, %1, %2, %3 offen sc1" : "=v"(r) : "v"(voff), "s"(rs), "s"(soff));
  return r;
}
DI v2u ld64(rsrc_t rs, unsigned voff, unsigned soff) {
  v2u r;
  asm volatile("buffer_load_dwordx2 %0, %1, %2, %3 offen" : "=v"(r) : "v"(voff), "s"(rs), "s"(soff));
  return r;
}
#define WAITV(n) asm volatile("s_waitcnt vmcnt(" #n ")" ::: "memory")
#define BAR() asm volatile("s_waitcnt lgkmcnt(0)\n\ts_barrier" ::: "memory")
#define WAITV_BAR(n) asm volatile("s_waitcnt vmcnt(" #n ") lgkmcnt(0)\n\ts_barrier" ::: "memory")
#define TIE(x) asm volatile("" : "+v"(x))

DI s16x4 trread(LAS unsigned char* L, unsigned off) { return __builtin_amdgcn_ds_read_tr16_b64_v4i16((LAS s16x4*)(L + off)); }
DI s16x8 cat(s16x4 a, s16x4 b) { return __builtin_shufflevector(a, b, 0, 1, 2, 3, 4, 5, 6, 7); }
DI s16x8 asfrag(v4u v) { return __builtin_bit_cast(s16x8, v); }
DI f32x4 mfma16(s16x8 a, s16x8 b, f32x4 c) { return __builtin_amdgcn_mfma_f32_16x16x32_bf16(a, b, c, 0, 0, 0); }
DI f32x4 mfma16k16(s16x4 a, s16x4 b, f32x4 c) { return __builtin_amdgcn_mfma_f32_16x16x16bf16_1k(a, b, c, 0, 0, 0); }
DI f32x16 mfma32(s16x8 a, s16x8 b, f32x16 c) { return __builtin_amdgcn_mfma_f32_32x32x16_bf16(a, b, c, 0, 0, 0); }
DI unsigned pk2(float lo, float hi) {
  f32x2 v = {lo, hi};
  return __builtin_bit_cast(unsigned, __builtin_convertvector(v, bf16x2));
}
DI float dot2(unsigned a, unsigned b, float c) {
  return __builtin_amdgcn_fdot2_f32_bf16(__builtin_bit_cast(bf16x2, a), __builtin_bit_cast(bf16x2, b), c, false);
}
DI float bflo(unsigned u) { return __builtin_bit_cast(float, u << 16); }
DI float bfhi(unsigned u) { return __builtin_bit_cast(float, u & 0xffff0000u); }
DI unsigned fu(float f) { return __builtin_bit_cast(unsigned, f); }
DI float uf(unsigned u) { return __builtin_bit_cast(float, u); }
DI void swap16(float& a, float& b) {
  auto r = __builtin_amdgcn_permlane16_swap(fu(a), fu(b), false, false);
  a = uf(r[0]);
  b = uf(r[1]);
}
DI void swap32(float& a, float& b) {
  auto r = __builtin_amdgcn_permlane32_swap(fu(a), fu(b), false, false);
  a = uf(r[0]);
  b = uf(r[1]);
}
DI void swap16u(unsigned& a, unsigned& b) {
  auto r = __builtin_amdgcn_permlane16_swap(a, b, false, false);
  a = r[0];
  b = r[1];
}
DI void swap32u(unsigned& a, unsigned& b) {
  auto r = __builtin_amdgcn_permlane32_swap(a, b, false, false);
  a = r[0];
  b = r[1];
}
DI void red2add(float& a, float& b) {
  swap16(a, b);
  float m = a + b, m2 = m;
  swap32(m, m2);
  float t = m + m2, t2 = t;
  swap16(t, t2);
  a = t;
  b = t2;
}
template <bool MAX> DI float rop(float a, float b) { return MAX ? fmaxf(a, b) : a + b; }
template <bool MAX> DI void red4(float& a, float& b, float& c, float& d) {
  swap16(a, b);
  float m1 = rop<MAX>(a, b);
  swap16(c, d);
  float m2 = rop<MAX>(c, d);
  swap32(m1, m2);
  float t = rop<MAX>(m1, m2), t2 = t;
  swap32(t, t2);
  float y1 = t, y1b = t, y2 = t2, y2b = t2;
  swap16(y1, y1b);
  swap16(y2, y2b);
  a = y1;
  b = y1b;
  c = y2;
  d = y2b;
}
DI void dual_softmax(f32x4 p0, f32x4 p1, v2u& pos0, v2u& neg0, v2u& pos1, v2u& neg1) {
  float mx0 = fmaxf(fmaxf(p0[0], p0[1]), fmaxf(p0[2], p0[3]));
  float mn0 = -fminf(fminf(p0[0], p0[1]), fminf(p0[2], p0[3]));
  float mx1 = fmaxf(fmaxf(p1[0], p1[1]), fmaxf(p1[2], p1[3]));
  float mn1 = -fminf(fminf(p1[0], p1[1]), fminf(p1[2], p1[3]));
  red4<true>(mx0, mn0, mx1, mn1);
  const float a0 = -mx0 * LOG2E, b0 = -mn0 * LOG2E, a1 = -mx1 * LOG2E, b1 = -mn1 * LOG2E;
  float ep0[4], en0[4], ep1[4], en1[4];
#pragma unroll
  for (int j = 0; j < 4; ++j) {
    ep0[j] = __builtin_amdgcn_exp2f(fmaf(p0[j], LOG2E, a0));
    en0[j] = __builtin_amdgcn_exp2f(fmaf(p0[j], -LOG2E, b0));
    ep1[j] = __builtin_amdgcn_exp2f(fmaf(p1[j], LOG2E, a1));
    en1[j] = __builtin_amdgcn_exp2f(fmaf(p1[j], -LOG2E, b1));
  }
  float sp0 = (ep0[0] + ep0[1]) + (ep0[2] + ep0[3]), sn0 = (en0[0] + en0[1]) + (en0[2] + en0[3]);
  float sp1 = (ep1[0] + ep1[1]) + (ep1[2] + ep1[3]), sn1 = (en1[0] + en1[1]) + (en1[2] + en1[3]);
  red4<false>(sp0, sn0, sp1, sn1);
  const float ip0 = __builtin_amdgcn_rcpf(sp0), in0 = __builtin_amdgcn_rcpf(sn0), ip1 = __builtin_amdgcn_rcpf(sp1), in1 = __builtin_amdgcn_rcpf(sn1);
  pos0 = (v2u){pk2(ep0[0] * ip0, ep0[1] * ip0), pk2(ep0[2] * ip0, ep0[3] * ip0)};
  neg0 = (v2u){pk2(en0[0] * in0, en0[1] * in0), pk2(en0[2] * in0, en0[3] * in0)};
  pos1 = (v2u){pk2(ep1[0] * ip1, ep1[1] * ip1), pk2(ep1[2] * ip1, ep1[3] * ip1)};
  neg1 = (v2u){pk2(en1[0] * in1, en1[1] * in1), pk2(en1[2] * in1, en1[3] * in1)};
}
DI u64 mix64(u64 z) {
  z += 0x9E3779B97F4A7C15ull;
  z = (z ^ (z >> 30)) * 0xBF58476D1CE4E5B9ull;
  z = (z ^ (z >> 27)) * 0x94D049BB133111EBull;
  return z ^ (z >> 31);
}
template <int N> DI void pollN(const unsigned char* base, u64 tag, int lane) {
  const GAS u64* fl = (const GAS u64*)base;
#pragma unroll 1
  for (unsigned it = 0; it < SPIN_CAP; ++it) {
    const u64 v = __hip_atomic_load(fl + (lane & (N - 1)), __ATOMIC_RELAXED, __HIP_MEMORY_SCOPE_AGENT);
    if (__all(v == tag)) break;
    __builtin_amdgcn_s_sleep(1);
  }
}
DI void set_flag(unsigned char* f, u64 tag) { __hip_atomic_store((GAS u64*)f, tag, __ATOMIC_RELAXED, __HIP_MEMORY_SCOPE_AGENT); }

DI v4u ld128_sc1o(rsrc_t rs, unsigned voff) {
  v4u r;
  asm volatile("buffer_load_dwordx4 %0, %1, %2, 0 offen sc1" : "=v"(r) : "v"(voff), "s"(rs));
  return r;
}
DI unsigned opaque(unsigned v) {
  asm volatile("" : "+v"(v));
  return v;
}
#define TIE4(a, b, c, d) asm volatile("" : "+v"(a), "+v"(b), "+v"(c), "+v"(d))
#define WAITV_TIE(n, x) asm volatile("s_waitcnt vmcnt(" #n ")" : "+v"(x))
template <int N> DI void waitv_tie(v4u& x) { asm volatile("s_waitcnt vmcnt(%1)" : "+v"(x) : "n"(N)); }
template <int N> DI void waitv() { asm volatile("s_waitcnt vmcnt(%0)" ::"n"(N) : "memory"); }

DI void lds_arrive(LAS unsigned char* c, int lane) {
  asm volatile("s_waitcnt lgkmcnt(0)" ::: "memory");
  if (lane == 0) __hip_atomic_fetch_add((LAS unsigned*)c, 1u, __ATOMIC_RELAXED, __HIP_MEMORY_SCOPE_WORKGROUP);
}
DI void lds_wait(LAS unsigned char* c, unsigned n) {
#pragma unroll 1
  for (unsigned it = 0; it < SPIN_CAP; ++it) {
    const unsigned v = __hip_atomic_load((LAS unsigned*)c, __ATOMIC_RELAXED, __HIP_MEMORY_SCOPE_WORKGROUP);
    if (__builtin_amdgcn_readfirstlane(v) >= n) break;
  }
  asm volatile("" ::: "memory");
}

#define STAMPW(wv, i) \
  do {                \
  } while (0)
#define STAMP(i) STAMPW(0, i)
#define CSTAMP(i) do {} while (0)

constexpr int DN_RING = 20, DN_PRE = 6;
constexpr unsigned dn_base(int i) {
  return i < 6 ? L_GW0 : i < 7 ? L_SPARE : i < 13 ? L_GW1 : i < 17 ? L_GH2 : i < 23 ? L_GW2 : i < 27 ? L_GH0 : i < 33 ? L_GW3 : i < 37 ? L_GH1 : L_GW0;
}
constexpr int dn_i0(int i) { return i < 6 ? 0 : i < 7 ? 6 : i < 13 ? 7 : i < 17 ? 13 : i < 23 ? 17 : i < 27 ? 23 : i < 33 ? 27 : i < 37 ? 33 : 37; }
constexpr int dn_n(int i) { return i < 6 ? 6 : i < 7 ? 1 : i < 13 ? 6 : i < 17 ? 4 : i < 23 ? 6 : i < 27 ? 4 : i < 33 ? 6 : i < 37 ? 4 : 6; }

template <int I> struct DnStep {
  static DI void run(v4u (&hf)[DN_RING], s16x8 (&wpre)[DN_PRE][2], s16x8 (&fw)[2][2], f32x16& a0, f32x16& a1, unsigned rel0, unsigned rel1,
                     unsigned kh2048, unsigned b0, unsigned b1, rsrc_t rsWs, unsigned hv) {
    LAS unsigned char* const L0 = (LAS unsigned char*)0;
    if constexpr (I + 1 < 43 && I + 1 >= DN_PRE) {
      constexpr int J = I + 1;
      if constexpr (dn_i0(J) == J) {
        b0 = opaque(rel0 + dn_base(J) + dn_n(J) * kh2048);
        b1 = opaque(rel1 + dn_base(J) + dn_n(J) * kh2048);
      }
      if constexpr (J == 37) asm volatile("s_barrier" ::: "memory");
      constexpr unsigned o = (J - dn_i0(J)) * 2048;
      fw[J & 1][0] = cat(trread(L0, b0 + o), trread(L0, b0 + o + 1024));
      SB();
    }
    constexpr int younger = (I + DN_RING - 1 < 42 ? I + DN_RING - 1 : 42) - I;
    waitv_tie<younger>(hf[I % DN_RING]);
    const s16x8 bf = asfrag(hf[I % DN_RING]);
    if constexpr (I < DN_PRE) a0 = mfma32(wpre[I][0], bf, a0);
    else a0 = mfma32(fw[I & 1][0], bf, a0);
    TIE(a0);
    SB();
    if constexpr (I + 1 < 43 && I + 1 >= DN_PRE) {
      constexpr int J = I + 1;
      constexpr unsigned o = (J - dn_i0(J)) * 2048;
      fw[J & 1][1] = cat(trread(L0, b1 + o), trread(L0, b1 + o + 1024));
      SB();
    }
    if constexpr (I < DN_PRE) a1 = mfma32(wpre[I][1], bf, a1);
    else a1 = mfma32(fw[I & 1][1], bf, a1);
    TIE(a1);
    SB();
    if constexpr (I + DN_RING < 43) hf[I % DN_RING] = ld128_sc1(rsWs, hv, (I + DN_RING) * 1024);
    SB();
    if constexpr (I + 1 < 43) DnStep<I + 1>::run(hf, wpre, fw, a0, a1, rel0, rel1, kh2048, b0, b1, rsWs, hv);
  }
};

__global__ void __launch_bounds__(512, 2) fused_block_kernel(Params p) {
  __shared__ __attribute__((aligned(16))) unsigned char smem[LDS_BYTES];
  LAS unsigned char* L = (LAS unsigned char*)smem;
  LAS unsigned char* const L0 = (LAS unsigned char*)0;
  const unsigned lb = (unsigned)(uintptr_t)L;
  const int tid = threadIdx.x, lane = tid & 63;
  const int w = __builtin_amdgcn_readfirstlane(tid >> 6);
  const int w4 = w & 3;
  const int b = blockIdx.x, xcd = b & 7, jj = b >> 3;
  const int rt = 4 * (xcd >> 1) + (jj >> 3), m = 8 * (xcd & 1) + (jj & 7);
  const int kro = (m + 2 * (rt & 3)) & 7;
  const int l15 = lane & 15, g = lane >> 4, q4 = l15 >> 2, p4 = lane & 3;
  const int hl = lane >> 5, t32 = lane & 31;
  const int hf = m >> 3, cg = m & 7;
  STAMP(0);

  const rsrc_t rsX = mkrsrc(p.x, 2048u * 1024u);
  const rsrc_t rsWs = mkrsrc(p.ws, WS_END);
  const s16x8 ones8 = {0x3F80, 0x3F80, 0x3F80, 0x3F80, 0x3F80, 0x3F80, 0x3F80, 0x3F80};
  v2u ep;
  asm volatile("global_load_dwordx2 %0, %1, off sc1" : "=v"(ep) : "v"(p.ws + WS_EPOCH));
  const unsigned trW0 = 16384 + (8 * g + q4) * 64 + ((0 ^ (g & 1)) << 5) + 8 * p4;
  const unsigned trW1 = 16384 + (8 * g + q4) * 64 + ((1 ^ (g & 1)) << 5) + 8 * p4;
  const unsigned voffL = lane * 16;
  const unsigned voffR = (lane >> 3) * 1024 + (((lane & 7) ^ (((lane >> 4) & 1) << 2)) * 16);

  if (w >= 4) {
    const rsrc_t rsWqkv = mkrsrc(p.wqkv, 512u * 3072u);
    const unsigned voffX0 = (128 * rt + 32 * w4 + (lane >> 3)) * 1024 + (((lane & 7) ^ (lane >> 4)) * 16);
    const unsigned voffX1 = (128 * rt + 32 * w4 + 8 + (lane >> 3)) * 1024 + (((lane & 7) ^ (4 + (lane >> 4))) * 16);
    const unsigned voffW = (lane >> 2) * 3072 + (((lane & 3) ^ ((lane >> 5) << 1)) * 16);
#define A_LOAD(st)                                                                                     \
  {                                                                                                    \
    const unsigned sl_ = lb + ((st) & 3) * A_SLOT, kb_ = (((st) + kro) & 7) * 64;                      \
    dma16(sl_ + (4 * w4 + 0) * 1024, voffX0, rsX, kb_ * 2);                                            \
    dma16(sl_ + (4 * w4 + 1) * 1024, voffX1, rsX, kb_ * 2);                                            \
    dma16(sl_ + 16384 + w4 * 1024, voffW, rsWqkv, (kb_ + 16 * w4) * 3072 + 1024 + 64 * m);             \
    dma16(sl_ + (4 * w4 + 2) * 1024, voffX0, rsX, kb_ * 2 + 16384);                                    \
    dma16(sl_ + (4 * w4 + 3) * 1024, voffX1, rsX, kb_ * 2 + 16384);                                    \
    dma16(sl_ + 20480 + w4 * 1024, voffW, rsWqkv, (kb_ + 16 * w4) * 3072 + 2048 + 64 * m);             \
  }
    if (w == 7) {
      const rsrc_t rsPhi = mkrsrc(p.wphi, 32u * 512u);
      dma16(lb + L_WPHI, voffL, rsPhi, m * 1024);
    }
    A_LOAD(0)
    A_LOAD(1)
    STAMPW(4, 35);
    WAITV_BAR(6);
    A_LOAD(2)
    A_LOAD(3)
#pragma unroll 1
    for (int st = 1; st < 8; ++st) {
      if (st <= 5) WAITV(12);
      else if (st == 6) WAITV(10);
      else WAITV(8);
      BAR();
      if (st <= 4) A_LOAD(st + 3)
      else if (st <= 6) {
#pragma unroll
        for (int j = 0; j < 4; ++j) {
          const unsigned r = 128 * w4 + 64 * (st - 5) + 16 * j;
          dma16(lb + L_WQ + r * 64, voffW, rsWqkv, ((r + 64 * kro) & 511) * 3072 + 64 * m);
        }
      }
    }
    asm volatile("s_waitcnt vmcnt(0) lgkmcnt(0)\n\ts_barrier" : "+v"(ep)::"memory");
    const u64 tag = mix64(((u64)(unsigned)__builtin_amdgcn_readfirstlane(ep[1]) << 32) | (unsigned)__builtin_amdgcn_readfirstlane(ep[0]));
    unsigned dummy = 0;
    {
      const rsrc_t rsG = mkrsrc(p.wg, 512u * 2752u), rsU = mkrsrc(p.wu, 512u * 2752u);
      const unsigned vo = (lane >> 1) * 2752 + (lane & 1) * 16, so = 32 * rt * 2752 + 192 * m;
      const unsigned dst = lb + L_CSW + w4 * 4608;
      if (w4 < 2) {
        dma16(dst + 2304, vo, rsG, so + 32 * (w4 + 4));
        dma16(dst + 2304 + 1152, vo, rsU, so + 32 * (w4 + 4));
      }
      dma16(dst, vo, rsG, so + 32 * w4);
      dma16(dst + 1152, vo, rsU, so + 32 * w4);
      const rsrc_t rsWo = mkrsrc(p.wo, 512u * 1024u);
#pragma unroll 4
      for (int i = 0; i < 16; ++i) dma16(lb + L_WO + (w4 * 16 + i) * 1024, voffR, rsWo, (w4 * 16 + i) * 8192 + 128 * cg);
    }
    {
      const unsigned cq = lb + L_WQ - 16384 + ((w4 & 1) ? trW1 : trW0) + (w4 >> 1) * 8 * 2048;
      f32x4 acq = {0, 0, 0, 0}, acq2 = acq;
#pragma unroll
      for (int s = 0; s < 8; s += 2) {
        acq = mfma16(cat(trread(L0, cq + s * 2048), trread(L0, cq + s * 2048 + 256)), ones8, acq);
        acq2 = mfma16(cat(trread(L0, cq + (s + 1) * 2048), trread(L0, cq + (s + 1) * 2048 + 256)), ones8, acq2);
      }
      if (l15 == 0) *(LAS f32x4*)(L + L_CS + 256 + (w4 & 1) * 128 + (w4 >> 1) * 64 + g * 16) = acq + acq2;
      lds_arrive(L + L_CNT + 4, lane);
    }
    asm volatile("s_waitcnt vmcnt(16)" : "+v"(dummy)::"memory");
#pragma unroll 1
    for (int bl = w4, k = 0; bl < 6; bl += 4, ++k) {
      const unsigned a = L_CSW + w4 * 4608 + k * 2304 + (p4 >> 1) * 1152 + (4 * (g >> 1) + q4) * 32 + 16 * (g & 1) + 8 * (p4 & 1);
      f32x16 c;
#pragma unroll
      for (int i = 0; i < 16; ++i) c[i] = 0.f;
      c = mfma32(cat(trread(L, a), trread(L, a + 8 * 32)), ones8, c);
      c = mfma32(cat(trread(L, a + 16 * 32), trread(L, a + 24 * 32)), ones8, c);
      const unsigned off = t32 == 0 ? WS_CSP + ((m * 16 + rt) * 6 + bl) * 128 + hl * 16 : 0x7FFFFF00u;
#pragma unroll
      for (int i = 0; i < 4; ++i)
        __builtin_amdgcn_raw_buffer_store_b128(__builtin_bit_cast(v4u, (f32x4){c[4 * i], c[4 * i + 1], c[4 * i + 2], c[4 * i + 3]}), rsWs, off, i * 32, 16);
    }
    lds_arrive(L + L_CNT + 12, lane);
    WAITV(0);
    if (lane == 0) set_flag(p.ws + WS_F5 + m * 512 + (rt * 4 + w4) * 8, tag);

    const rsrc_t rsGU = mkrsrc((w4 >> 1) ? p.wu : p.wg, 512u * 2752u);
    unsigned voffG[6];
#pragma unroll
    for (int j = 0; j < 6; ++j) {
      const unsigned u = 64 * (6 * (w4 & 1) + j) + lane, row = u / 12, c = u - row * 12;
      voffG[j] = row * 2752 + c * 16;
    }
    const unsigned gdst = (w4 >> 1) * GU_UPOFF + 6 * (w4 & 1) * 1024;
    const unsigned gsrc = 192 * m;
#define GU_LOADW(st, slot)                                                                       \
  {                                                                                              \
    _Pragma("unroll") for (int j = 0; j < 6; ++j) dma16(lb + (slot) + gdst + j * 1024, voffG[j], rsGU, gsrc + (((st) + kro) & 7) * 64 * 2752); \
  }
    lds_wait(L + L_CNT + 8, 4);
    GU_LOADW(0, L_GW0)
    lds_wait(L + L_CNT + 12, 8);
    GU_LOADW(1, L_GW1)
    GU_LOADW(2, L_GW2)
    STAMPW(4, 30);
    BAR();
    STAMPW(4, 28);
    pollN<64>(p.ws + WS_F5 + m * 512, tag, lane);
    if (tid == 256) *(LAS v4u*)(L + L_ZERO) = (v4u){0, 0, 0, 0};
#pragma unroll 1
    for (int bl = w4; bl < 6; bl += 4) {
      float s = 0.f;
      unsigned cv[8];
#pragma unroll
      for (int j = 0; j < 8; ++j) cv[j] = __builtin_amdgcn_raw_buffer_load_b32(rsWs, WS_CSP + ((m * 16 + 8 * hl) * 6 + bl) * 128 + t32 * 4, j * 768, 16);
#pragma unroll
      for (int j = 0; j < 8; ++j) s += uf(cv[j]);
      float s2 = s;
      swap32(s, s2);
      s += s2;
      const unsigned hi = pk2(s, 0.f) & 0xffffu, lo = pk2(s - uf(hi << 16), 0.f) & 0xffffu;
      if (lane < 32) *(LAS v4u*)(L + L_CSF + bl * 512 + lane * 16) = (v4u){hi | (hi << 16), lo, 0, 0};
    }
    BAR();
    BAR();
    GU_LOADW(3, L_GW3)
    pollN<16>(p.ws + WS_F3 + (rt * 4 + w4) * 128, tag, lane);
    STAMPW(4, 8);
    const unsigned hsrc = WS_H + (rt * 4 + w4) * 32 * 1024;
    const unsigned hdst = w4 * 4096;
#define GU_LOADH(st, slot)                                           \
  {                                                                  \
    _Pragma("unroll") for (int j = 0; j < 4; ++j) dma16_sc1(lb + (slot) + hdst + j * 1024, voffL, rsWs, hsrc + (((st) + kro) & 7) * 4096 + j * 1024); \
  }
    GU_LOADH(0, L_GH0)
    WAITV_BAR(0);
    GU_LOADH(1, L_GH1)
    GU_LOADH(2, L_GH2)
    WAITV_BAR(4);
    GU_LOADH(3, L_GH0)
    v4u sv[8];
    const unsigned svo = WS_ST + ((rt * 4 + w4) * 16 + 8 * hl) * 512 + t32 * 16;
#pragma unroll
    for (int i = 0; i < 8; ++i) sv[i] = ld128_sc1(rsWs, svo, i * 512);
    GU_LOADW(4, L_GW0)
    WAITV_BAR(18);
    GU_LOADH(4, L_GH1)
    GU_LOADW(5, L_GW1)
    asm volatile("s_waitcnt vmcnt(16)" : "+v"(sv[0]), "+v"(sv[1]), "+v"(sv[2]), "+v"(sv[3]), "+v"(sv[4]), "+v"(sv[5]), "+v"(sv[6]), "+v"(sv[7])::"memory");
    {
      bool ok = true;
#pragma unroll
      for (int i = 0; i < 8; ++i) ok &= (sv[i][1] == (unsigned)tag) & (sv[i][3] == (unsigned)tag);
#pragma unroll 1
      for (unsigned it = 0; !__all(ok) && it < SPIN_CAP; ++it) {
        ok = true;
#pragma unroll
        for (int i = 0; i < 8; ++i) {
          sv[i] = __builtin_amdgcn_raw_buffer_load_b128(rsWs, svo, i * 512, 16);
          ok &= (sv[i][1] == (unsigned)tag) & (sv[i][3] == (unsigned)tag);
        }
      }
      float s = 0.f, q = 0.f;
#pragma unroll
      for (int i = 0; i < 8; ++i) {
        s += uf(sv[i][0]);
        q += uf(sv[i][2]);
      }
      swap32(s, q);
      float t = s + q, t2 = t;
      swap32(t, t2);
      const float mean2 = t * (1.f / 512.f);
      const float var = fmaxf(t2 * (1.f / 512.f) - mean2 * mean2, 0.f) + 1e-5f;
      const float nm = -mean2;
      const unsigned hi = pk2(nm, 0.f) & 0xffffu, lo = pk2(nm - uf(hi << 16), 0.f) & 0xffffu;
      if (lane < 32) {
        *(LAS v4u*)(L + L_MF + w4 * 512 + lane * 16) = (v4u){hi | (lo << 16), hi, 0, 0};
        *(LAS float*)(L + L_R2 + (32 * w4 + lane) * 4) = __builtin_amdgcn_rcpf(var);
      }
    }
    const rsrc_t rsWd = mkrsrc(p.wd, 1376u * 1024u);
    const unsigned dsrc = (16 * 43 * (w4 >> 1) + 8 * (w4 & 1)) * 1024 + 128 * cg;
#define DN_LOAD(i0)                                                                                            \
  {                                                                                                            \
    _Pragma("unroll") for (int j = 0; j < dn_n(i0); ++j)                                                       \
        dma16(lb + dn_base(i0) + ((w4 >> 1) * dn_n(i0) + j) * 2048 + (w4 & 1) * 1024, voffR, rsWd, dsrc + ((i0) + j) * 16384); \
  }
#pragma unroll 1
    for (int st = 3; st < 8; ++st) {
      if (st >= 6) WAITV(17);
      else WAITV(16);
      BAR();
      const int h3 = st >= 6 ? st - 6 : (st >= 3 ? st - 3 : st);
      const unsigned hfree = h3 == 0 ? L_GH2 : L_GH0 + (h3 - 1) * 16384;
      const unsigned wfree = (st & 3) == 0 ? L_GW3 : L_GW0 + ((st & 3) - 1) * GU_SLOT;
      if (st <= 5) GU_LOADH(st + 2, hfree)
      if (st <= 4) GU_LOADW(st + 3, wfree)
      else if (st == 5) { DN_LOAD(0) DN_LOAD(6) }
      else if (st == 6) { DN_LOAD(7) DN_LOAD(13) }
      else { DN_LOAD(17) DN_LOAD(23) }
    }
    BAR();
    BAR();
    DN_LOAD(27) DN_LOAD(33)
    STAMPW(4, 31);
    WAITV_BAR(0);
    BAR();
    DN_LOAD(37)
    WAITV_BAR(0);
    BAR();
    asm volatile(".Lfb_end:");
  } else {
    if (tid < 4) *(LAS unsigned*)(L + L_CNT + tid * 4) = 0;
    unsigned cdummy = 0;
    {
      u64 pc;
      unsigned len;
      asm volatile("s_getpc_b64 %0\n.Lfb_here:\n\ts_mov_b32 %1, .Lfb_end-.Lfb_here" : "=s"(pc), "=s"(len));
      const unsigned o = (jj * 14 + (lane < 14 ? lane : 0)) * 128;
      const unsigned char* q = (const unsigned char*)pc + (o + 128 <= len ? o : 0);
      asm volatile("global_load_dword %0, %1, off" : "=v"(cdummy) : "v"(q));
    }
    v4u xf[16][2];
    f32x4 ak[4][2];
    const f32x4 z4 = {0, 0, 0, 0};
    f32x2 stt[2];
#pragma unroll
    for (int j = 0; j < 4; ++j) ak[j][0] = ak[j][1] = (f32x4){0, 0, 0, 0};
    {
      const unsigned xr0 = lb + (32 * w4 + l15) * 128;
      const unsigned xlo0 = opaque(xr0 + ((g ^ (l15 >> 1)) << 4)), xlo1 = opaque(xr0 + (((4 + g) ^ (l15 >> 1)) << 4));
      const unsigned xhi0 = opaque(xlo0 + 2 * A_SLOT), xhi1 = opaque(xlo1 + 2 * A_SLOT);
      const unsigned w0lo = opaque(lb + trW0), w1lo = opaque(lb + trW1), w0hi = opaque(w0lo + 2 * A_SLOT), w1hi = opaque(w1lo + 2 * A_SLOT);
      const unsigned clo = opaque(((w4 & 1) ? w1lo : w0lo) + (w4 >> 1) * 4096), chi = opaque(clo + 2 * A_SLOT);
      s16x8 fa[2][4], fc[2];
      f32x4 asum[2] = {z4, z4}, agr[2] = {z4, z4}, acs = z4;
#define A1_X(s, a) xf[s][a] = *(LAS v4u*)(L0 + ((((s) >> 1) & 2) ? (((s) & 1) ? xhi1 : xhi0) : (((s) & 1) ? xlo1 : xlo0)) + (((s) >> 1) & 1) * A_SLOT + (a) * 2048); SB()
#define A1_WA(s, j) (((((s) >> 1) & 2) ? (((j) & 1) ? w1hi : w0hi) : (((j) & 1) ? w1lo : w0lo)) + (((s) >> 1) & 1) * A_SLOT + ((j) >> 1) * 4096 + ((s) & 1) * 2048)
#define A1_W(s, j) fa[(s) & 1][j] = cat(trread(L0, A1_WA(s, j)), trread(L0, A1_WA(s, j) + 256)); SB()
#define A1_CA(s) (((((s) >> 1) & 2) ? chi : clo) + (((s) >> 1) & 1) * A_SLOT + ((s) & 1) * 2048)
#define A1_C(s) fc[(s) & 1] = cat(trread(L0, A1_CA(s)), trread(L0, A1_CA(s) + 256)); SB()
#define A1_M(s, j, a)                                           \
  ak[j][a] = mfma16(fa[(s) & 1][j], asfrag(xf[s][a]), ak[j][a]); \
  TIE(ak[j][a]);                                                \
  SB()
#define A1_S(s, a)                                               \
  asum[a] = mfma16(ones8, asfrag(xf[s][a]), asum[a]);            \
  TIE(asum[a]);                                                  \
  agr[a] = mfma16(asfrag(xf[s][a]), asfrag(xf[s][a]), agr[a]);   \
  TIE(agr[a]);                                                   \
  SB()
#define A1_CM(s)                              \
  acs = mfma16(fc[(s) & 1], ones8, acs);      \
  TIE(acs);                                   \
  SB()
#define A1_ALLM(s)                                                            \
  A1_M(s, 0, 0); A1_M(s, 0, 1); A1_M(s, 1, 0); A1_M(s, 1, 1); A1_M(s, 2, 0); A1_M(s, 2, 1); A1_M(s, 3, 0); A1_M(s, 3, 1); \
  A1_S(s, 0); A1_S(s, 1); A1_CM(s);
#define A1_RM(sn, sc)                                                         \
  A1_X(sn, 0); A1_M(sc, 0, 0); A1_W(sn, 0); A1_M(sc, 0, 1);                   \
  A1_X(sn, 1); A1_M(sc, 1, 0); A1_W(sn, 1); A1_M(sc, 1, 1);                   \
  A1_W(sn, 2); A1_M(sc, 2, 0); A1_M(sc, 2, 1);                                \
  A1_W(sn, 3); A1_M(sc, 3, 0); A1_M(sc, 3, 1);                                \
  A1_C(sn); A1_S(sc, 0); A1_S(sc, 1); A1_CM(sc);                              \
  SB();
#define A1_STAGE(st) BAR(); A1_RM(2 * (st), 2 * (st) - 1) A1_RM(2 * (st) + 1, 2 * (st))
      BAR();
      STAMP(14); CSTAMP(52);
      A1_X(0, 0); A1_X(0, 1); A1_W(0, 0); A1_W(0, 1); A1_W(0, 2); A1_W(0, 3); A1_C(0);
      SB();
      A1_RM(1, 0)
      A1_STAGE(1)
      A1_STAGE(2) A1_STAGE(3) A1_STAGE(4) A1_STAGE(5) A1_STAGE(6) A1_STAGE(7)
      A1_ALLM(15)
      if (l15 == 0) *(LAS f32x4*)(L + L_CS + w4 * 64 + g * 16) = acs;
#pragma unroll
      for (int a = 0; a < 2; ++a) {
        const float a01 = (p4 & 1) ? agr[a][1] : agr[a][0], a23 = (p4 & 1) ? agr[a][3] : agr[a][2];
        const float d = (p4 & 2) ? a23 : a01;
        stt[a][1] = (g == q4) ? d : 0.f;
      }
      float d0 = stt[0][1], d1 = stt[1][1];
      red2add(d0, d1);
      const float mean0 = asum[0][0] * (1.f / 512.f), mean1 = asum[1][0] * (1.f / 512.f);
      const float r0 = __builtin_amdgcn_rsqf(fmaxf(d0 * (1.f / 512.f) - mean0 * mean0, 0.f) + 1e-5f);
      const float r1 = __builtin_amdgcn_rsqf(fmaxf(d1 * (1.f / 512.f) - mean1 * mean1, 0.f) + 1e-5f);
      stt[0] = (f32x2){r0, -mean0 * r0};
      stt[1] = (f32x2){r1, -mean1 * r1};
    }
    STAMP(1); CSTAMP(53);
    asm volatile("s_waitcnt lgkmcnt(0)\n\ts_barrier" : "+v"(ep)::"memory");
    STAMP(26); CSTAMP(54);
    asm volatile("s_waitcnt vmcnt(0)" : "+v"(ep), "+v"(cdummy));
    const u64 tag = mix64(((u64)(unsigned)__builtin_amdgcn_readfirstlane(ep[1]) << 32) | (unsigned)__builtin_amdgcn_readfirstlane(ep[0]));

    const s16x4 wphi0 = trread(L, L_WPHI + (4 * g + q4) * 32 + 8 * p4);
    const s16x4 wphi1 = trread(L, L_WPHI + 512 + (4 * g + q4) * 32 + 8 * p4);
    {
      f32x4 cs[4];
#pragma unroll
      for (int i = 0; i < 4; ++i) cs[i] = *(LAS f32x4*)(L + L_CS + i * 64 + g * 16);
#pragma unroll
      for (int a = 0; a < 2; ++a) {
        const float rstd = stt[a][0], nb = stt[a][1];
        const f32x4 k0 = ak[0][a] * rstd + cs[0] * nb, k1 = ak[1][a] * rstd + cs[1] * nb;
        const f32x4 v0 = ak[2][a] * rstd + cs[2] * nb, v1 = ak[3][a] * rstd + cs[3] * nb;
        const v2u k0b = {pk2(k0[0], k0[1]), pk2(k0[2], k0[3])}, k1b = {pk2(k1[0], k1[1]), pk2(k1[2], k1[3])};
        const f32x4 p0 = mfma16k16(wphi0, __builtin_bit_cast(s16x4, k0b), z4);
        const f32x4 p1 = mfma16k16(wphi1, __builtin_bit_cast(s16x4, k1b), z4);
        const unsigned wa = L_KVIMG + (32 * w4 + 16 * a + l15) * 32 + 8 * (g ^ q4);
        *(LAS v2u*)(L + wa + 4 * 4096) = (v2u){pk2(v0[0], v0[1]), pk2(v0[2], v0[3])};
        *(LAS v2u*)(L + wa + 5 * 4096) = (v2u){pk2(v1[0], v1[1]), pk2(v1[2], v1[3])};
        v2u pos0, neg0, pos1, neg1;
        dual_softmax(p0, p1, pos0, neg0, pos1, neg1);
        *(LAS v2u*)(L + wa + 0 * 4096) = pos0;
        *(LAS v2u*)(L + wa + 1 * 4096) = neg0;
        *(LAS v2u*)(L + wa + 2 * 4096) = pos1;
        *(LAS v2u*)(L + wa + 3 * 4096) = neg1;
      }
    }
    lds_arrive(L + L_CNT, lane);
    lds_wait(L + L_CNT, 4);
    STAMP(15); CSTAMP(55);
    {
      const unsigned ra = L_KVIMG + (4 * g + q4) * 32 + 8 * (p4 ^ g);
      const unsigned raA = ra + w4 * 4096, raB = ra + (4 + (w4 >> 1)) * 4096;
      f32x4 acc = z4, accz = z4;
      s16x8 fA[4], fB[4];
#pragma unroll
      for (int u = 0; u < 4; ++u) {
        fA[u] = cat(trread(L, raA + u * 1024), trread(L, raA + u * 1024 + 512));
        fB[u] = cat(trread(L, raB + u * 1024), trread(L, raB + u * 1024 + 512));
      }
#pragma unroll
      for (int u = 0; u < 4; ++u) { TIE(fA[u]); TIE(fB[u]); }
      lds_arrive(L + L_CNT + 8, lane);
#pragma unroll
      for (int u = 0; u < 4; ++u) {
        acc = mfma16(fA[u], fB[u], acc);
        accz = mfma16(fA[u], ones8, accz);
      }
      const unsigned base = WS_KVP + (m * 16 + rt) * KVP_STRIDE;
      __builtin_amdgcn_raw_buffer_store_b128(__builtin_bit_cast(v4u, acc), rsWs, base + w4 * 1024 + lane * 16, 0, 16);
      __builtin_amdgcn_raw_buffer_store_b128(__builtin_bit_cast(v4u, accz), rsWs, l15 == 0 ? base + 4096 + w4 * 64 + g * 16 : 0x7FFFFFF0u, 0, 16);
      WAITV(0);
      if (lane == 0) set_flag(p.ws + WS_F1 + m * 512 + (rt * 4 + w4) * 8, tag);
    }
    STAMP(2);

    v4u phq[2][2];
    {
      f32x4 aq[2][2] = {{z4, z4}, {z4, z4}};
      const unsigned trQ0 = opaque(lb + L_WQ - 16384 + trW0), trQ1 = opaque(lb + L_WQ - 16384 + trW1);
      s16x8 fq[2][2];
#define A2_W(s, j) fq[(s) & 1][j] = cat(trread(L0, ((j) ? trQ1 : trQ0) + (s) * 2048), trread(L0, ((j) ? trQ1 : trQ0) + (s) * 2048 + 256)); SB()
#define A2_M(s, j, a)                                           \
  aq[j][a] = mfma16(fq[(s) & 1][j], asfrag(xf[s][a]), aq[j][a]); \
  TIE(aq[j][a]);                                                \
  SB()
#define A2_STEP(s)                                \
  if ((s) < 15) { A2_W(((s) + 1) & 15, 0); }      \
  A2_M(s, 0, 0); A2_M(s, 0, 1);                   \
  if ((s) < 15) { A2_W(((s) + 1) & 15, 1); }      \
  A2_M(s, 1, 0); A2_M(s, 1, 1);                   \
  SB();
      A2_W(0, 0); A2_W(0, 1);
      SB();
      A2_STEP(0) A2_STEP(1) A2_STEP(2) A2_STEP(3) A2_STEP(4) A2_STEP(5) A2_STEP(6) A2_STEP(7)
      A2_STEP(8) A2_STEP(9) A2_STEP(10) A2_STEP(11) A2_STEP(12) A2_STEP(13) A2_STEP(14) A2_STEP(15)
      lds_arrive(L + L_CNT + 12, lane);
      STAMP(27);
      lds_wait(L + L_CNT + 4, 4);
      const f32x4 c0 = *(LAS f32x4*)(L + L_CS + 256 + g * 16) + *(LAS f32x4*)(L + L_CS + 320 + g * 16);
      const f32x4 c1 = *(LAS f32x4*)(L + L_CS + 384 + g * 16) + *(LAS f32x4*)(L + L_CS + 448 + g * 16);
#pragma unroll
      for (int a = 0; a < 2; ++a) {
        const f32x4 q0 = aq[0][a] * stt[a][0] + c0 * stt[a][1], q1 = aq[1][a] * stt[a][0] + c1 * stt[a][1];
        const v2u q0b = {pk2(q0[0], q0[1]), pk2(q0[2], q0[3])}, q1b = {pk2(q1[0], q1[1]), pk2(q1[2], q1[3])};
        const f32x4 p0 = mfma16k16(wphi0, __builtin_bit_cast(s16x4, q0b), z4);
        const f32x4 p1 = mfma16k16(wphi1, __builtin_bit_cast(s16x4, q1b), z4);
        v2u pos0, neg0, pos1, neg1;
        dual_softmax(p0, p1, pos0, neg0, pos1, neg1);
        phq[a][0] = (v4u){pos0[0], pos0[1], neg0[0], neg0[1]};
        phq[a][1] = (v4u){pos1[0], pos1[1], neg1[0], neg1[1]};
      }
    }
    STAMP(3);

    {
      pollN<64>(p.ws + WS_F1 + m * 512, tag, lane);
      STAMP(4);
      const unsigned base = WS_KVP + m * 16 * KVP_STRIDE;
      v4u pv[16], zv[4];
#pragma unroll
      for (int i = 0; i < 4; ++i) zv[i] = __builtin_amdgcn_raw_buffer_load_b128(rsWs, base + g * KVP_STRIDE + 4096 + l15 * 16, 4 * i * KVP_STRIDE, 16);
#pragma unroll
      for (int i = 0; i < 16; ++i) pv[i] = __builtin_amdgcn_raw_buffer_load_b128(rsWs, base + tid * 16, i * KVP_STRIDE, 16);
      f32x4 sz = __builtin_bit_cast(f32x4, zv[0]);
#pragma unroll
      for (int i = 1; i < 4; ++i) sz += __builtin_bit_cast(f32x4, zv[i]);
      float z0 = sz[0], z1 = sz[1], z2 = sz[2], z3 = sz[3];
      red4<false>(z0, z1, z2, z3);
      if (tid < 16) *(LAS v2u*)(L + L_ZF + (l15 >> 3) * 64 + (l15 & 3) * 16 + ((l15 >> 2) & 1) * 8) = (v2u){pk2(z0, z1), pk2(z2, z3)};
      f32x4 s = __builtin_bit_cast(f32x4, pv[0]);
#pragma unroll
      for (int i = 1; i < 16; ++i) s += __builtin_bit_cast(f32x4, pv[i]);
      *(LAS v2u*)(L + L_KF + (w >> 1) * 1024 + lane * 16 + (w & 1) * 8) = (v2u){pk2(s[0], s[1]), pk2(s[2], s[3])};
    }
    STAMP(33);
    BAR();
    {
      const s16x8 kf0 = *(LAS s16x8*)(L + L_KF + lane * 16), kf1 = *(LAS s16x8*)(L + L_KF + 1024 + lane * 16);
      const s16x8 zf0 = *(LAS s16x8*)(L + L_ZF + g * 16), zf1 = *(LAS s16x8*)(L + L_ZF + 64 + g * 16);
#pragma unroll
      for (int a = 0; a < 2; ++a) {
        const f32x4 n0 = mfma16(kf0, asfrag(phq[a][0]), z4), d0 = mfma16(zf0, asfrag(phq[a][0]), z4);
        const f32x4 n1 = mfma16(kf1, asfrag(phq[a][1]), z4), d1 = mfma16(zf1, asfrag(phq[a][1]), z4);
        const float i0 = __builtin_amdgcn_rcpf(d0[0] + 1e-6f), i1 = __builtin_amdgcn_rcpf(d1[0] + 1e-6f);
        const v4u ov = {pk2(n0[0] * i0, n0[1] * i0), pk2(n0[2] * i0, n0[3] * i0), pk2(n1[0] * i1, n1[1] * i1), pk2(n1[2] * i1, n1[3] * i1)};
        __builtin_amdgcn_raw_buffer_store_b128(ov, rsWs, WS_O + ((rt * 4 + w4) * 32 + 2 * m + (g >> 1)) * 1024 + ((g & 1) * 32 + 16 * a + l15) * 16, 0, 16);
      }
      WAITV(0);
      if (lane < 2) set_flag(p.ws + WS_F2 + ((rt * 4 + w4) * 2 + hf) * 128 + (lane * 8 + cg) * 8, tag);
    }
    STAMP(5);

    const int tb32 = 2 * hf + (w & 1), kh = (w >> 1) & 1;
    unsigned hres[8];
    const unsigned tok = 128 * rt + 32 * tb32 + t32;
    v2u xr[4];
#pragma unroll
    for (int i = 0; i < 4; ++i) xr[i] = ld64(rsX, tok * 1024 + (64 * cg + 32 * kh + 4 * hl) * 2, i * 16);
    const unsigned relR = (4 * (g >> 1) + q4) * 128 + (g & 1) * 32 + 8 * p4;
    const unsigned rel0 = lb + relR + ((0 ^ (q4 >> 1)) << 6), rel1 = lb + relR + ((1 ^ (q4 >> 1)) << 6);
    f32x16 a0, a1;
#pragma unroll
    for (int i = 0; i < 16; ++i) { a0[i] = 0.f; a1[i] = 0.f; }
    {
      const unsigned rl0 = opaque(rel0 + kh * 32768), rl1 = opaque(rel1 + kh * 32768);
      s16x8 fw[2][2];
#define WO_RD1(i, c)                                                                                    \
  {                                                                                                     \
    constexpr unsigned o_ = (32 * ((i) >> 1) + 8 * ((i) & 1)) * 128;                                    \
    fw[(i) & 1][c] = cat(trread(L0, ((c) ? rl1 : rl0) + o_), trread(L0, ((c) ? rl1 : rl0) + o_ + 2048)); \
    SB();                                                                                               \
  }
#define WO_RD(i) WO_RD1(i, 0) WO_RD1(i, 1)
      WO_RD(0)
      pollN<16>(p.ws + WS_F2 + ((rt * 4 + tb32) * 2 + kh) * 128, tag, lane);
      STAMP(6);
      v4u of[16];
      const unsigned ov = WS_O + ((rt * 4 + tb32) * 32 + 16 * kh) * 1024 + lane * 16;
#pragma unroll
      for (int i = 0; i < 16; ++i) of[i] = ld128_sc1(rsWs, ov, i * 1024);
#define WO_STEP(i)                                          \
  {                                                         \
    if ((i) < 15) WO_RD1(((i) + 1) & 15, 0)                 \
    waitv_tie<15 - (i)>(of[i]);                             \
    a0 = mfma32(fw[(i) & 1][0], asfrag(of[i]), a0);         \
    TIE(a0);                                                \
    SB();                                                   \
    if ((i) < 15) WO_RD1(((i) + 1) & 15, 1)                 \
    a1 = mfma32(fw[(i) & 1][1], asfrag(of[i]), a1);         \
    TIE(a1);                                                \
    SB();                                                   \
  }
      WO_STEP(0) WO_STEP(1) WO_STEP(2) WO_STEP(3) WO_STEP(4) WO_STEP(5) WO_STEP(6) WO_STEP(7)
      WO_STEP(8) WO_STEP(9) WO_STEP(10) WO_STEP(11) WO_STEP(12) WO_STEP(13) WO_STEP(14) WO_STEP(15)
    }
    STAMP(16);
    {
      const f32x16 give = kh ? a0 : a1;
      f32x16 keep = kh ? a1 : a0;
#pragma unroll
      for (int i = 0; i < 4; ++i)
        *(LAS f32x4*)(L + L_RED_WO + (w * 4 + i) * 1024 + lane * 16) = (f32x4){give[4 * i], give[4 * i + 1], give[4 * i + 2], give[4 * i + 3]};
      BAR();
      float ss = 0.f, sq = 0.f;
#pragma unroll
      for (int i = 0; i < 4; ++i) {
        const f32x4 o = *(LAS f32x4*)(L + L_RED_WO + ((w ^ 2) * 4 + i) * 1024 + lane * 16);
        TIE(xr[i]);
        hres[2 * i] = pk2(keep[4 * i] + o[0] + bflo(xr[i][0]), keep[4 * i + 1] + o[1] + bfhi(xr[i][0]));
        hres[2 * i + 1] = pk2(keep[4 * i + 2] + o[2] + bflo(xr[i][1]), keep[4 * i + 3] + o[3] + bfhi(xr[i][1]));
      }
      const int Sg = 2 * cg + kh;
      const unsigned ho = WS_H + ((rt * 4 + tb32) * 32 + 2 * Sg) * 1024 + lane * 16;
      __builtin_amdgcn_raw_buffer_store_b128((v4u){hres[0], hres[1], hres[2], hres[3]}, rsWs, ho, 0, 16);
      __builtin_amdgcn_raw_buffer_store_b128((v4u){hres[4], hres[5], hres[6], hres[7]}, rsWs, ho + 1024, 0, 16);
      WAITV(0);
      if (lane == 0) set_flag(p.ws + WS_F3 + (rt * 4 + tb32) * 128 + Sg * 8, tag);
      STAMP(36);
#pragma unroll
      for (int i = 0; i < 8; ++i) {
        const float lo = bflo(hres[i]), hi = bfhi(hres[i]);
        ss += lo + hi;
        sq = fmaf(lo, lo, fmaf(hi, hi, sq));
      }
      swap32(ss, sq);
      __builtin_amdgcn_raw_buffer_store_b64((v2u){fu(ss + sq), (unsigned)tag}, rsWs, WS_ST + ((rt * 4 + tb32) * 16 + Sg) * 512 + t32 * 16 + hl * 8, 0, 16);
    }
    BAR();
    STAMP(7);

    const int mh = w & 1, nh = (w >> 1) & 1;
    f32x16 acc[2][3];
#pragma unroll
    for (int i = 0; i < 16; ++i) {
      acc[0][0][i] = 0.f; acc[0][1][i] = 0.f; acc[0][2][i] = 0.f;
      acc[1][0][i] = 0.f; acc[1][1][i] = 0.f; acc[1][2][i] = 0.f;
    }
    {
      const unsigned relT = lb + mh * 8192 + lane * 16;
      const unsigned relW = lb + (p4 >> 1) * GU_UPOFF + (4 * (g >> 1) + q4) * 192 + 16 * (g & 1) + 8 * (p4 & 1) + 96 * nh;
      v4u tf[2][2];
      s16x8 ga[2][3];
      tf[1][0] = tf[1][1] = (v4u){0, 0, 0, 0};
      ga[1][0] = ga[1][1] = ga[1][2] = asfrag(tf[1][0]);
#define GU_RT(j, tt) tf[(j) & 1][tt] = *(LAS v4u*)(L0 + aT + (tt) * 4096 + (j) * 1024); SB()
#define GU_RW(j, l3) ga[(j) & 1][l3] = cat(trread(L0, aW + (j) * 16 * 192 + 32 * (l3)), trread(L0, aW + ((j) * 16 + 8) * 192 + 32 * (l3))); SB()
#define GU_M(j, tt, l3)                                                       \
  acc[tt][l3] = mfma32(ga[(j) & 1][l3], asfrag(tf[(j) & 1][tt]), acc[tt][l3]); \
  TIE(acc[tt][l3]);                                                            \
  SB()
#define GU_STEP(jn, jc)            \
  GU_RT(jn, 0);                    \
  GU_M(jc, 0, 0);                  \
  GU_RW(jn, 0);                    \
  GU_M(jc, 0, 1);                  \
  GU_RW(jn, 1);                    \
  GU_M(jc, 0, 2);                  \
  GU_RT(jn, 1);                    \
  GU_M(jc, 1, 0);                  \
  GU_RW(jn, 2);                    \
  GU_M(jc, 1, 1);                  \
  GU_M(jc, 1, 2);                  \
  SB();
#pragma unroll 1
      for (int st = 0; st < 8; ++st) {
        BAR();
        if (st == 0) STAMP(17);
        if (st == 1) { STAMP(18); CSTAMP(50); }
        if (st == 7) { STAMP(24); CSTAMP(51); }
        const int h3 = st >= 6 ? st - 6 : (st >= 3 ? st - 3 : st);
        const unsigned hcur = h3 == 2 ? L_GH2 : L_GH0 + h3 * 16384;
        const unsigned wcur = (st & 3) == 3 ? L_GW3 : L_GW0 + (st & 3) * GU_SLOT;
        const unsigned aT = relT + hcur, aW = relW + wcur;
        if (6 * m + 3 * nh < 86) {
          GU_STEP(0, 3)
          GU_STEP(1, 0)
          GU_STEP(2, 1)
          GU_STEP(3, 2)
        }
      }
      const unsigned zo = lb + L_ZERO;
      const unsigned aC = lane < 32 ? lb + L_CSF + 3 * nh * 512 + lane * 16 : zo - 0, aM = lane < 32 ? lb + L_MF + 2 * mh * 512 + lane * 16 : zo;
      const unsigned stp = lane < 32 ? 512 : 0;
      tf[0][0] = *(LAS v4u*)(L0 + aM);
      tf[0][1] = *(LAS v4u*)(L0 + aM + stp);
      ga[0][0] = *(LAS s16x8*)(L0 + aC);
      ga[0][1] = *(LAS s16x8*)(L0 + aC + stp);
      ga[0][2] = *(LAS s16x8*)(L0 + aC + 2 * stp);
      GU_M(3, 0, 0); GU_M(3, 0, 1); GU_M(3, 0, 2); GU_M(3, 1, 0); GU_M(3, 1, 1); GU_M(3, 1, 2);
      GU_M(0, 0, 0); GU_M(0, 0, 1); GU_M(0, 0, 2); GU_M(0, 1, 0); GU_M(0, 1, 1); GU_M(0, 1, 2);
    }
    const float r2a = *(LAS float*)(L + L_R2 + (64 * mh + t32) * 4), r2b = *(LAS float*)(L + L_R2 + (64 * mh + 32 + t32) * 4);
    BAR();
    STAMP(9);
#pragma unroll
    for (int tt = 0; tt < 2; ++tt) {
      const float r2 = tt ? r2b : r2a;
#pragma unroll
      for (int l3 = 0; l3 < 3; ++l3) {
        const int fblk = 6 * m + 3 * nh + l3;
        float hv[8];
#pragma unroll
        for (int i = 0; i < 2; ++i)
#pragma unroll
          for (int e = 0; e < 4; ++e) hv[4 * i + e] = (acc[tt][l3][8 * i + e] * r2) * acc[tt][l3][8 * i + 4 + e];
        const v4u o = {pk2(hv[0], hv[1]), pk2(hv[2], hv[3]), pk2(hv[4], hv[5]), pk2(hv[6], hv[7])};
        const unsigned off = fblk < 86 ? WS_HH + ((rt * 4 + 2 * mh + tt) * 86 + fblk) * 1024 + lane * 16 : 0x7FFFFFF0u;
        __builtin_amdgcn_raw_buffer_store_b128(o, rsWs, off, 0, 16);
      }
    }
    asm volatile("s_barrier" ::: "memory");
    WAITV(0);
    if (lane < 2) set_flag(p.ws + WS_F4 + (rt * 4 + 2 * mh + lane) * 256 + (m * 2 + nh) * 8, tag);
    STAMP(10);

    const unsigned kh2048 = kh * 2048;
    const unsigned pb0 = opaque(rel0 + dn_base(0) + dn_n(0) * kh2048), pb1 = opaque(rel1 + dn_base(0) + dn_n(0) * kh2048);
    BAR();
    s16x8 wpre[DN_PRE][2];
#pragma unroll
    for (int i = 0; i < DN_PRE; ++i) {
      wpre[i][0] = cat(trread(L0, pb0 + i * 2048), trread(L0, pb0 + i * 2048 + 1024));
      wpre[i][1] = cat(trread(L0, pb1 + i * 2048), trread(L0, pb1 + i * 2048 + 1024));
    }
#pragma unroll
    for (int i = 0; i < DN_PRE; ++i) { TIE(wpre[i][0]); TIE(wpre[i][1]); }
    BAR();
    STAMP(25);
    pollN<32>(p.ws + WS_F4 + (rt * 4 + tb32) * 256, tag, lane);
    STAMP(11); CSTAMP(56);
    v4u hf_[DN_RING];
    const unsigned hv = WS_HH + ((rt * 4 + tb32) * 86 + 43 * kh) * 1024 + lane * 16;
#pragma unroll
    for (int i = 0; i < DN_RING; ++i) hf_[i] = ld128_sc1(rsWs, hv, i * 1024);
#pragma unroll
    for (int i = 0; i < 16; ++i) { a0[i] = 0.f; a1[i] = 0.f; }
    s16x8 fw[2][2];
    DnStep<0>::run(hf_, wpre, fw, a0, a1, rel0, rel1, kh2048, pb0, pb1, rsWs, hv);
    STAMP(12); CSTAMP(57);
    {
      int tz = threadIdx.x;
      asm volatile("" : "+v"(tz));
      const int lane = tz & 63, hl = lane >> 5, t32 = lane & 31;
      const f32x16 give = kh ? a0 : a1;
      const f32x16 keep = kh ? a1 : a0;
#pragma unroll
      for (int i = 0; i < 4; ++i)
        *(LAS f32x4*)(L + L_RED_DN + (w * 4 + i) * 1024 + lane * 16) = (f32x4){give[4 * i], give[4 * i + 1], give[4 * i + 2], give[4 * i + 3]};
      BAR();
      unsigned ob[8];
#pragma unroll
      for (int i = 0; i < 4; ++i) {
        const f32x4 o = *(LAS f32x4*)(L + L_RED_DN + ((w ^ 2) * 4 + i) * 1024 + lane * 16);
        ob[2 * i] = pk2(keep[4 * i] + o[0] + bflo(hres[2 * i]), keep[4 * i + 1] + o[1] + bfhi(hres[2 * i]));
        ob[2 * i + 1] = pk2(keep[4 * i + 2] + o[2] + bflo(hres[2 * i + 1]), keep[4 * i + 3] + o[3] + bfhi(hres[2 * i + 1]));
      }
      const rsrc_t rsOut = mkrsrc(p.out, 2048u * 1024u);
      const unsigned oo = (128 * rt + 32 * tb32 + t32) * 1024 + (64 * cg + 32 * kh + 8 * hl) * 2;
#pragma unroll
      for (int pr = 0; pr < 2; ++pr) {
        swap32u(ob[4 * pr], ob[4 * pr + 2]);
        swap32u(ob[4 * pr + 1], ob[4 * pr + 3]);
        __builtin_amdgcn_raw_buffer_store_b128((v4u){ob[4 * pr], ob[4 * pr + 1], ob[4 * pr + 2], ob[4 * pr + 3]}, rsOut, oo + pr * 32, 0, 0);
      }
    }
    if (b == 0 && tid == 0) __hip_atomic_store((GAS u64*)(p.ws + WS_EPOCH), tag, __ATOMIC_RELAXED, __HIP_MEMORY_SCOPE_AGENT);
    STAMP(13);
  }
}

}

extern "C" void kernel_launch(void* const* d_in, const int* in_sizes, int n_in, void* d_out, int out_size, void* d_ws, size_t ws_size,
                              hipStream_t stream) {
  Params p;
  p.x = d_in[0];
  p.wqkv = d_in[1];
  p.wphi = d_in[2];
  p.wo = d_in[3];
  p.wg = d_in[5];
  p.wu = d_in[6];
  p.wd = d_in[7];
  p.out = d_out;
  p.ws = (unsigned char*)d_ws;
  fused_block_kernel<<<256, 512, 0, stream>>>(p);
}
```

```cpp
#include <hip/hip_runtime.h>
#include <hip/hip_bf16.h>
#include <stdint.h>

#define GAS __attribute__((address_space(1)))
#define LAS __attribute__((address_space(3)))
typedef unsigned v4u __attribute__((ext_vector_type(4)));
typedef unsigned v2u __attribute__((ext_vector_type(2)));
typedef float f32x2 __attribute__((ext_vector_type(2)));
typedef float f32x4 __attribute__((ext_vector_type(4)));
typedef float f32x16 __attribute__((ext_vector_type(16)));
typedef short s16x4 __attribute__((ext_vector_type(4)));
typedef short s16x8 __attribute__((ext_vector_type(8)));
typedef __bf16 bf16x2 __attribute__((ext_vector_type(2)));
typedef __amdgpu_buffer_rsrc_t rsrc_t;
typedef unsigned long long u64;

#define STAMPS 0
#define GU_ROT 4
#define CODEPF 1
#define TOUCH 1
#define DIAG_T 30.0f
#define MAPPING 2
#define SB() __builtin_amdgcn_sched_barrier(0)

namespace {

constexpr unsigned WS_EPOCH = 0;
constexpr unsigned WS_F1 = 4096;
constexpr unsigned WS_F5 = 12288;
constexpr unsigned WS_F2 = 20480;
constexpr unsigned WS_F3 = 36864;
constexpr unsigned WS_F4 = 45056;
constexpr unsigned WS_KVP = 65536;
constexpr unsigned KVP_STRIDE = 4352;
constexpr unsigned WS_CSP = WS_KVP + 256 * KVP_STRIDE;
constexpr unsigned WS_ST = WS_CSP + 256 * 768;
constexpr unsigned ST_BYTES = 16 * 4 * 16 * 512;
constexpr unsigned WS_O = WS_ST + ST_BYTES;
constexpr unsigned O_BYTES = 16 * 4 * 32 * 1024;
constexpr unsigned WS_H = WS_O + O_BYTES;
constexpr unsigned H_BYTES = 16 * 4 * 32 * 1024;
constexpr unsigned WS_HH = WS_H + H_BYTES;
constexpr unsigned HH_BYTES = 16 * 4 * 86 * 1024;
constexpr unsigned WS_END = WS_HH + HH_BYTES;
constexpr unsigned WS_STAMP = 16u << 20;

constexpr unsigned A_SLOT = 24576;
constexpr unsigned L_WQ = 98304;
constexpr unsigned L_CSW = 131072;
constexpr unsigned L_KVIMG = 65536;
constexpr unsigned L_WO = 0;
constexpr unsigned GU_UPOFF = 12320, GU_SLOT = 24640;
constexpr unsigned L_GW0 = 65536, L_GW1 = L_GW0 + GU_SLOT, L_GW2 = L_GW1 + GU_SLOT, L_GW3 = 0;
constexpr unsigned L_GH0 = 24640, L_GH1 = 41024, L_GH2 = 139456, L_SPARE = 57408;
constexpr unsigned L_RED_WO = 139456;
constexpr unsigned L_RED_DN = L_GW1;
constexpr unsigned L_MISC = 155840;
constexpr unsigned L_CS = L_MISC;
constexpr unsigned L_WPHI = L_MISC + 1024;
constexpr unsigned L_STAT = L_MISC + 2048;
constexpr unsigned L_CSF = L_MISC;
constexpr unsigned L_KF = L_MISC + 3072;
constexpr unsigned L_ZF = L_MISC + 5120;
constexpr unsigned L_MF = L_MISC + 5248;
constexpr unsigned L_R2 = L_MISC + 7296;
constexpr unsigned L_ZERO = L_MISC + 7808;
constexpr unsigned L_CNT = L_MISC + 7824;
constexpr unsigned LDS_BYTES = 163840;
static_assert(L_GW2 + GU_SLOT == L_GH2, "map");
static_assert(L_GH2 + 16384 == L_MISC, "map");
static_assert(L_ZERO + 16 <= LDS_BYTES, "map");
static_assert(L_CSW + 4 * 4608 <= L_MISC, "map");

constexpr unsigned SPIN_CAP = 1u << 18;
constexpr float LOG2E = 1.4426950408889634f;

struct Params {
  const void* x;
  const void* wqkv;
  const void* wphi;
  const void* wo;
  const void* wg;
  const void* wu;
  const void* wd;
  void* out;
  unsigned char* ws;
};

#define DI __device__ __forceinline__

DI rsrc_t mkrsrc(const void* p, unsigned bytes) { return __builtin_amdgcn_make_buffer_rsrc((void*)p, 0, (int)bytes, 0x00020000); }
DI void dma16(unsigned ldsaddr, unsigned voff, rsrc_t rs, unsigned soff) {
  asm volatile("s_mov_b32 m0, %0\n\ts_nop 0\n\tbuffer_load_dwordx4 %1, %2, %3 offen lds" ::"s"(ldsaddr), "v"(voff), "s"(rs), "s"(soff) : "memory");
}
DI void dma16_sc1(unsigned ldsaddr, unsigned voff, rsrc_t rs, unsigned soff) {
  asm volatile("s_mov_b32 m0, %0\n\ts_nop 0\n\tbuffer_load_dwordx4 %1, %2, %3 offen sc1 lds" ::"s"(ldsaddr), "v"(voff), "s"(rs), "s"(soff) : "memory");
}
DI v4u ld128(rsrc_t rs, unsigned voff, unsigned soff) {
  v4u r;
  asm volatile("buffer_load_dwordx4 %0, %1, %2, %3 offen" : "=v"(r) : "v"(voff), "s"(rs), "s"(soff));
  return r;
}
DI v4u ld128_sc1(rsrc_t rs, unsigned voff, unsigned soff) {
  v4u r;
  asm volatile("buffer_load_dwordx4 %0, %1, %2, %3 offen sc1" : "=v"(r) : "v"(voff), "s"(rs), "s"(soff));
  return r;
}
DI v2u ld64(rsrc_t rs, unsigned voff, unsigned soff) {
  v2u r;
  asm volatile("buffer_load_dwordx2 %0, %1, %2, %3 offen" : "=v"(r) : "v"(voff), "s"(rs), "s"(soff));
  return r;
}
#define WAITV(n) asm volatile("s_waitcnt vmcnt(" #n ")" ::: "memory")
#define BAR() asm volatile("s_waitcnt lgkmcnt(0)\n\ts_barrier" ::: "memory")
#define WAITV_BAR(n) asm volatile("s_waitcnt vmcnt(" #n ") lgkmcnt(0)\n\ts_barrier" ::: "memory")
#define TIE(x) asm volatile("" : "+v"(x))

DI s16x4 trread(LAS unsigned char* L, unsigned off) { return __builtin_amdgcn_ds_read_tr16_b64_v4i16((LAS s16x4*)(L + off)); }
DI s16x8 cat(s16x4 a, s16x4 b) { return __builtin_shufflevector(a, b, 0, 1, 2, 3, 4, 5, 6, 7); }
DI s16x8 asfrag(v4u v) { return __builtin_bit_cast(s16x8, v); }
DI f32x4 mfma16(s16x8 a, s16x8 b, f32x4 c) { return __builtin_amdgcn_mfma_f32_16x16x32_bf16(a, b, c, 0, 0, 0); }
DI f32x4 mfma16k16(s16x4 a, s16x4 b, f32x4 c) { return __builtin_amdgcn_mfma_f32_16x16x16bf16_1k(a, b, c, 0, 0, 0); }
DI f32x16 mfma32(s16x8 a, s16x8 b, f32x16 c) { return __builtin_amdgcn_mfma_f32_32x32x16_bf16(a, b, c, 0, 0, 0); }
DI unsigned pk2(float lo, float hi) {
  f32x2 v = {lo, hi};
  return __builtin_bit_cast(unsigned, __builtin_convertvector(v, bf16x2));
}
DI float dot2(unsigned a, unsigned b, float c) {
  return __builtin_amdgcn_fdot2_f32_bf16(__builtin_bit_cast(bf16x2, a), __builtin_bit_cast(bf16x2, b), c, false);
}
DI float bflo(unsigned u) { return __builtin_bit_cast(float, u << 16); }
DI float bfhi(unsigned u) { return __builtin_bit_cast(float, u & 0xffff0000u); }
DI unsigned fu(float f) { return __builtin_bit_cast(unsigned, f); }
DI float uf(unsigned u) { return __builtin_bit_cast(float, u); }
DI void swap16(float& a, float& b) {
  auto r = __builtin_amdgcn_permlane16_swap(fu(a), fu(b), false, false);
  a = uf(r[0]);
  b = uf(r[1]);
}
DI void swap32(float& a, float& b) {
  auto r = __builtin_amdgcn_permlane32_swap(fu(a), fu(b), false, false);
  a = uf(r[0]);
  b = uf(r[1]);
}
DI void swap16u(unsigned& a, unsigned& b) {
  auto r = __builtin_amdgcn_permlane16_swap(a, b, false, false);
  a = r[0];
  b = r[1];
}
DI void swap32u(unsigned& a, unsigned& b) {
  auto r = __builtin_amdgcn_permlane32_swap(a, b, false, false);
  a = r[0];
  b = r[1];
}
DI void red2add(float& a, float& b) {
  swap16(a, b);
  float m = a + b, m2 = m;
  swap32(m, m2);
  float t = m + m2, t2 = t;
  swap16(t, t2);
  a = t;
  b = t2;
}
template <bool MAX> DI float rop(float a, float b) { return MAX ? fmaxf(a, b) : a + b; }
template <bool MAX> DI void red4(float& a, float& b, float& c, float& d) {
  swap16(a, b);
  float m1 = rop<MAX>(a, b);
  swap16(c, d);
  float m2 = rop<MAX>(c, d);
  swap32(m1, m2);
  float t = rop<MAX>(m1, m2), t2 = t;
  swap32(t, t2);
  float y1 = t, y1b = t, y2 = t2, y2b = t2;
  swap16(y1, y1b);
  swap16(y2, y2b);
  a = y1;
  b = y1b;
  c = y2;
  d = y2b;
}
DI void dual_softmax(f32x4 p0, f32x4 p1, v2u& pos0, v2u& neg0, v2u& pos1, v2u& neg1) {
  float mx0 = fmaxf(fmaxf(p0[0], p0[1]), fmaxf(p0[2], p0[3]));
  float mn0 = -fminf(fminf(p0[0], p0[1]), fminf(p0[2], p0[3]));
  float mx1 = fmaxf(fmaxf(p1[0], p1[1]), fmaxf(p1[2], p1[3]));
  float mn1 = -fminf(fminf(p1[0], p1[1]), fminf(p1[2], p1[3]));
  red4<true>(mx0, mn0, mx1, mn1);
  const float a0 = -mx0 * LOG2E, b0 = -mn0 * LOG2E, a1 = -mx1 * LOG2E, b1 = -mn1 * LOG2E;
  float ep0[4], en0[4], ep1[4], en1[4];
#pragma unroll
  for (int j = 0; j < 4; ++j) {
    ep0[j] = __builtin_amdgcn_exp2f(fmaf(p0[j], LOG2E, a0));
    en0[j] = __builtin_amdgcn_exp2f(fmaf(p0[j], -LOG2E, b0));
    ep1[j] = __builtin_amdgcn_exp2f(fmaf(p1[j], LOG2E, a1));
    en1[j] = __builtin_amdgcn_exp2f(fmaf(p1[j], -LOG2E, b1));
  }
  float sp0 = (ep0[0] + ep0[1]) + (ep0[2] + ep0[3]), sn0 = (en0[0] + en0[1]) + (en0[2] + en0[3]);
  float sp1 = (ep1[0] + ep1[1]) + (ep1[2] + ep1[3]), sn1 = (en1[0] + en1[1]) + (en1[2] + en1[3]);
  red4<false>(sp0, sn0, sp1, sn1);
  const float ip0 = __builtin_amdgcn_rcpf(sp0), in0 = __builtin_amdgcn_rcpf(sn0), ip1 = __builtin_amdgcn_rcpf(sp1), in1 = __builtin_amdgcn_rcpf(sn1);
  pos0 = (v2u){pk2(ep0[0] * ip0, ep0[1] * ip0), pk2(ep0[2] * ip0, ep0[3] * ip0)};
  neg0 = (v2u){pk2(en0[0] * in0, en0[1] * in0), pk2(en0[2] * in0, en0[3] * in0)};
  pos1 = (v2u){pk2(ep1[0] * ip1, ep1[1] * ip1), pk2(ep1[2] * ip1, ep1[3] * ip1)};
  neg1 = (v2u){pk2(en1[0] * in1, en1[1] * in1), pk2(en1[2] * in1, en1[3] * in1)};
}
DI u64 mix64(u64 z) {
  z += 0x9E3779B97F4A7C15ull;
  z = (z ^ (z >> 30)) * 0xBF58476D1CE4E5B9ull;
  z = (z ^ (z >> 27)) * 0x94D049BB133111EBull;
  return z ^ (z >> 31);
}
template <int N> DI void pollN(const unsigned char* base, u64 tag, int lane) {
  const GAS u64* fl = (const GAS u64*)base;
#pragma unroll 1
  for (unsigned it = 0; it < SPIN_CAP; ++it) {
    const u64 v = __hip_atomic_load(fl + (lane & (N - 1)), __ATOMIC_RELAXED, __HIP_MEMORY_SCOPE_AGENT);
    if (__all(v == tag)) break;
    __builtin_amdgcn_s_sleep(1);
  }
}
DI void set_flag(unsigned char* f, u64 tag) { __hip_atomic_store((GAS u64*)f, tag, __ATOMIC_RELAXED, __HIP_MEMORY_SCOPE_AGENT); }

DI v4u ld128_sc1o(rsrc_t rs, unsigned voff) {
  v4u r;
  asm volatile("buffer_load_dwordx4 %0, %1, %2, 0 offen sc1" : "=v"(r) : "v"(voff), "s"(rs));
  return r;
}
DI unsigned opaque(unsigned v) {
  asm volatile("" : "+v"(v));
  return v;
}
#define TIE4(a, b, c, d) asm volatile("" : "+v"(a), "+v"(b), "+v"(c), "+v"(d))
#define WAITV_TIE(n, x) asm volatile("s_waitcnt vmcnt(" #n ")" : "+v"(x))
template <int N> DI void waitv_tie(v4u& x) { asm volatile("s_waitcnt vmcnt(%1)" : "+v"(x) : "n"(N)); }
template <int N> DI void waitv() { asm volatile("s_waitcnt vmcnt(%0)" ::"n"(N) : "memory"); }

DI void lds_arrive(LAS unsigned char* c, int lane) {
  asm volatile("s_waitcnt lgkmcnt(0)" ::: "memory");
  if (lane == 0) __hip_atomic_fetch_add((LAS unsigned*)c, 1u, __ATOMIC_RELAXED, __HIP_MEMORY_SCOPE_WORKGROUP);
}
DI void lds_wait(LAS unsigned char* c, unsigned n) {
#pragma unroll 1
  for (unsigned it = 0; it < SPIN_CAP; ++it) {
    const unsigned v = __hip_atomic_load((LAS unsigned*)c, __ATOMIC_RELAXED, __HIP_MEMORY_SCOPE_WORKGROUP);
    if (__builtin_amdgcn_readfirstlane(v) >= n) break;
  }
  asm volatile("" ::: "memory");
}

#define STAMPW(wv, i) \
  do {                \
  } while (0)
#define STAMP(i) STAMPW(0, i)
#define CSTAMP(i) do {} while (0)

constexpr int DN_RING = 20, DN_PRE = 6;
constexpr unsigned dn_base(int i) {
  return i < 6 ? L_GW0 : i < 7 ? L_SPARE : i < 13 ? L_GW1 : i < 17 ? L_GH2 : i < 23 ? L_GW2 : i < 27 ? L_GH0 : i < 33 ? L_GW3 : i < 37 ? L_GH1 : L_GW0;
}
constexpr int dn_i0(int i) { return i < 6 ? 0 : i < 7 ? 6 : i < 13 ? 7 : i < 17 ? 13 : i < 23 ? 17 : i < 27 ? 23 : i < 33 ? 27 : i < 37 ? 33 : 37; }
constexpr int dn_n(int i) { return i < 6 ? 6 : i < 7 ? 1 : i < 13 ? 6 : i < 17 ? 4 : i < 23 ? 6 : i < 27 ? 4 : i < 33 ? 6 : i < 37 ? 4 : 6; }

template <int I> struct DnStep {
  static DI void run(v4u (&hf)[DN_RING], s16x8 (&wpre)[DN_PRE][2], s16x8 (&fw)[2][2], f32x16& a0, f32x16& a1, unsigned rel0, unsigned rel1,
                     unsigned kh2048, unsigned b0, unsigned b1, rsrc_t rsWs, unsigned hv) {
    LAS unsigned char* const L0 = (LAS unsigned char*)0;
    if constexpr (I + 1 < 43 && I + 1 >= DN_PRE) {
      constexpr int J = I + 1;
      if constexpr (dn_i0(J) == J) {
        b0 = opaque(rel0 + dn_base(J) + dn_n(J) * kh2048);
        b1 = opaque(rel1 + dn_base(J) + dn_n(J) * kh2048);
      }
      if constexpr (J == 37) asm volatile("s_barrier" ::: "memory");
      constexpr unsigned o = (J - dn_i0(J)) * 2048;
      fw[J & 1][0] = cat(trread(L0, b0 + o), trread(L0, b0 + o + 1024));
      SB();
    }
    constexpr int younger = (I + DN_RING - 1 < 42 ? I + DN_RING - 1 : 42) - I;
    waitv_tie<younger>(hf[I % DN_RING]);
    const s16x8 bf = asfrag(hf[I % DN_RING]);
    if constexpr (I < DN_PRE) a0 = mfma32(wpre[I][0], bf, a0);
    else a0 = mfma32(fw[I & 1][0], bf, a0);
    TIE(a0);
    SB();
    if constexpr (I + 1 < 43 && I + 1 >= DN_PRE) {
      constexpr int J = I + 1;
      constexpr unsigned o = (J - dn_i0(J)) * 2048;
      fw[J & 1][1] = cat(trread(L0, b1 + o), trread(L0, b1 + o + 1024));
      SB();
    }
    if constexpr (I < DN_PRE) a1 = mfma32(wpre[I][1], bf, a1);
    else a1 = mfma32(fw[I & 1][1], bf, a1);
    TIE(a1);
    SB();
    if constexpr (I + DN_RING < 43) hf[I % DN_RING] = ld128_sc1(rsWs, hv, (I + DN_RING) * 1024);
    SB();
    if constexpr (I + 1 < 43) DnStep<I + 1>::run(hf, wpre, fw, a0, a1, rel0, rel1, kh2048, b0, b1, rsWs, hv);
  }
};

__global__ void __launch_bounds__(512, 2) fused_block_kernel(Params p) {
  __shared__ __attribute__((aligned(16))) unsigned char smem[LDS_BYTES];
  LAS unsigned char* L = (LAS unsigned char*)smem;
  LAS unsigned char* const L0 = (LAS unsigned char*)0;
  const unsigned lb = (unsigned)(uintptr_t)L;
  const int tid = threadIdx.x, lane = tid & 63;
  const int w = __builtin_amdgcn_readfirstlane(tid >> 6);
  const int w4 = w & 3;
  const int b = blockIdx.x, xcd = b & 7, jj = b >> 3;
  const int rt = 4 * (xcd >> 1) + (jj >> 3), m = 8 * (xcd & 1) + (jj & 7);
  const int kro = (m + 2 * (rt & 3)) & 7;
  const int l15 = lane & 15, g = lane >> 4, q4 = l15 >> 2, p4 = lane & 3;
  const int hl = lane >> 5, t32 = lane & 31;
  const int hf = m >> 3, cg = m & 7;
  STAMP(0);
  u64 tstart_;
  asm volatile("s_memrealtime %0\n\ts_waitcnt lgkmcnt(0)" : "=s"(tstart_)::"memory");
  if (tid == 0) __hip_atomic_store((GAS u64*)(p.ws + WS_STAMP) + b, tstart_, __ATOMIC_RELAXED, __HIP_MEMORY_SCOPE_AGENT);

  const rsrc_t rsX = mkrsrc(p.x, 2048u * 1024u);
  const rsrc_t rsWs = mkrsrc(p.ws, WS_END);
  const s16x8 ones8 = {0x3F80, 0x3F80, 0x3F80, 0x3F80, 0x3F80, 0x3F80, 0x3F80, 0x3F80};
  v2u ep;
  asm volatile("global_load_dwordx2 %0, %1, off sc1" : "=v"(ep) : "v"(p.ws + WS_EPOCH));
  const unsigned trW0 = 16384 + (8 * g + q4) * 64 + ((0 ^ (g & 1)) << 5) + 8 * p4;
  const unsigned trW1 = 16384 + (8 * g + q4) * 64 + ((1 ^ (g & 1)) << 5) + 8 * p4;
  const unsigned voffL = lane * 16;
  const unsigned voffR = (lane >> 3) * 1024 + (((lane & 7) ^ (((lane >> 4) & 1) << 2)) * 16);

  if (w >= 4) {
    const rsrc_t rsWqkv = mkrsrc(p.wqkv, 512u * 3072u);
    const unsigned voffX0 = (128 * rt + 32 * w4 + (lane >> 3)) * 1024 + (((lane & 7) ^ (lane >> 4)) * 16);
    const unsigned voffX1 = (128 * rt + 32 * w4 + 8 + (lane >> 3)) * 1024 + (((lane & 7) ^ (4 + (lane >> 4))) * 16);
    const unsigned voffW = (lane >> 2) * 3072 + (((lane & 3) ^ ((lane >> 5) << 1)) * 16);
#define A_LOAD(st)                                                                                     \
  {                                                                                                    \
    const unsigned sl_ = lb + ((st) & 3) * A_SLOT, kb_ = (((st) + kro) & 7) * 64;                      \
    dma16(sl_ + (4 * w4 + 0) * 1024, voffX0, rsX, kb_ * 2);                                            \
    dma16(sl_ + (4 * w4 + 1) * 1024, voffX1, rsX, kb_ * 2);                                            \
    dma16(sl_ + 16384 + w4 * 1024, voffW, rsWqkv, (kb_ + 16 * w4) * 3072 + 1024 + 64 * m);             \
    dma16(sl_ + (4 * w4 + 2) * 1024, voffX0, rsX, kb_ * 2 + 16384);                                    \
    dma16(sl_ + (4 * w4 + 3) * 1024, voffX1, rsX, kb_ * 2 + 16384);                                    \
    dma16(sl_ + 20480 + w4 * 1024, voffW, rsWqkv, (kb_ + 16 * w4) * 3072 + 2048 + 64 * m);             \
  }
    if (w == 7) {
      const rsrc_t rsPhi = mkrsrc(p.wphi, 32u * 512u);
      dma16(lb + L_WPHI, voffL, rsPhi, m * 1024);
    }
    A_LOAD(0)
    A_LOAD(1)
    STAMPW(4, 35);
    WAITV_BAR(6);
    A_LOAD(2)
    A_LOAD(3)
#pragma unroll 1
    for (int st = 1; st < 8; ++st) {
      if (st <= 5) WAITV(12);
      else if (st == 6) WAITV(10);
      else WAITV(8);
      BAR();
      if (st <= 4) A_LOAD(st + 3)
      else if (st <= 6) {
#pragma unroll
        for (int j = 0; j < 4; ++j) {
          const unsigned r = 128 * w4 + 64 * (st - 5) + 16 * j;
          dma16(lb + L_WQ + r * 64, voffW, rsWqkv, ((r + 64 * kro) & 511) * 3072 + 64 * m);
        }
      }
    }
    asm volatile("s_waitcnt vmcnt(0) lgkmcnt(0)\n\ts_barrier" : "+v"(ep)::"memory");
    const u64 tag = mix64(((u64)(unsigned)__builtin_amdgcn_readfirstlane(ep[1]) << 32) | (unsigned)__builtin_amdgcn_readfirstlane(ep[0]));
    unsigned dummy = 0;
    {
      const unsigned idx = (b * 4 + w4) * 33 + (lane < 33 ? lane : 0);
      const unsigned char* q;
      unsigned o = idx;
      if (o < 11008) q = (const unsigned char*)p.wg;
      else if ((o -= 11008) < 11008) q = (const unsigned char*)p.wu;
      else { o -= 11008; q = (const unsigned char*)p.wd; if (o >= 11008) o = 0; }
      asm volatile("global_load_dword %0, %1, off" : "=v"(dummy) : "v"(q + (size_t)o * 128));
    }
    {
      const rsrc_t rsG = mkrsrc(p.wg, 512u * 2752u), rsU = mkrsrc(p.wu, 512u * 2752u);
      const unsigned vo = (lane >> 1) * 2752 + (lane & 1) * 16, so = 32 * rt * 2752 + 192 * m;
      const unsigned dst = lb + L_CSW + w4 * 4608;
      if (w4 < 2) {
        dma16(dst + 2304, vo, rsG, so + 32 * (w4 + 4));
        dma16(dst + 2304 + 1152, vo, rsU, so + 32 * (w4 + 4));
      }
      dma16(dst, vo, rsG, so + 32 * w4);
      dma16(dst + 1152, vo, rsU, so + 32 * w4);
      const rsrc_t rsWo = mkrsrc(p.wo, 512u * 1024u);
#pragma unroll 4
      for (int i = 0; i < 16; ++i) dma16(lb + L_WO + (w4 * 16 + i) * 1024, voffR, rsWo, (w4 * 16 + i) * 8192 + 128 * cg);
    }
    {
      const unsigned cq = lb + L_WQ - 16384 + ((w4 & 1) ? trW1 : trW0) + (w4 >> 1) * 8 * 2048;
      f32x4 acq = {0, 0, 0, 0}, acq2 = acq;
#pragma unroll
      for (int s = 0; s < 8; s += 2) {
        acq = mfma16(cat(trread(L0, cq + s * 2048), trread(L0, cq + s * 2048 + 256)), ones8, acq);
        acq2 = mfma16(cat(trread(L0, cq + (s + 1) * 2048), trread(L0, cq + (s + 1) * 2048 + 256)), ones8, acq2);
      }
      if (l15 == 0) *(LAS f32x4*)(L + L_CS + 256 + (w4 & 1) * 128 + (w4 >> 1) * 64 + g * 16) = acq + acq2;
      lds_arrive(L + L_CNT + 4, lane);
    }
    asm volatile("s_waitcnt vmcnt(16)" : "+v"(dummy)::"memory");
#pragma unroll 1
    for (int bl = w4, k = 0; bl < 6; bl += 4, ++k) {
      const unsigned a = L_CSW + w4 * 4608 + k * 2304 + (p4 >> 1) * 1152 + (4 * (g >> 1) + q4) * 32 + 16 * (g & 1) + 8 * (p4 & 1);
      f32x16 c;
#pragma unroll
      for (int i = 0; i < 16; ++i) c[i] = 0.f;
      c = mfma32(cat(trread(L, a), trread(L, a + 8 * 32)), ones8, c);
      c = mfma32(cat(trread(L, a + 16 * 32), trread(L, a + 24 * 32)), ones8, c);
      const unsigned off = t32 == 0 ? WS_CSP + ((m * 16 + rt) * 6 + bl) * 128 + hl * 16 : 0x7FFFFF00u;
#pragma unroll
      for (int i = 0; i < 4; ++i)
        __builtin_amdgcn_raw_buffer_store_b128(__builtin_bit_cast(v4u, (f32x4){c[4 * i], c[4 * i + 1], c[4 * i + 2], c[4 * i + 3]}), rsWs, off, i * 32, 16);
    }
    lds_arrive(L + L_CNT + 12, lane);
    WAITV(0);
    if (lane == 0) set_flag(p.ws + WS_F5 + m * 512 + (rt * 4 + w4) * 8, tag);

    const rsrc_t rsGU = mkrsrc((w4 >> 1) ? p.wu : p.wg, 512u * 2752u);
    unsigned voffG[6];
#pragma unroll
    for (int j = 0; j < 6; ++j) {
      const unsigned u = 64 * (6 * (w4 & 1) + j) + lane, row = u / 12, c = u - row * 12;
      voffG[j] = row * 2752 + c * 16;
    }
    const unsigned gdst = (w4 >> 1) * GU_UPOFF + 6 * (w4 & 1) * 1024;
    const unsigned gsrc = 192 * m;
#define GU_LOADW(st, slot)                                                                       \
  {                                                                                              \
    _Pragma("unroll") for (int j = 0; j < 6; ++j) dma16(lb + (slot) + gdst + j * 1024, voffG[j], rsGU, gsrc + (((st) + kro) & 7) * 64 * 2752); \
  }
    lds_wait(L + L_CNT + 8, 4);
    GU_LOADW(0, L_GW0)
    lds_wait(L + L_CNT + 12, 8);
    GU_LOADW(1, L_GW1)
    GU_LOADW(2, L_GW2)
    STAMPW(4, 30);
    BAR();
    STAMPW(4, 28);
    pollN<64>(p.ws + WS_F5 + m * 512, tag, lane);
    if (tid == 256) *(LAS v4u*)(L + L_ZERO) = (v4u){0, 0, 0, 0};
#pragma unroll 1
    for (int bl = w4; bl < 6; bl += 4) {
      float s = 0.f;
      unsigned cv[8];
#pragma unroll
      for (int j = 0; j < 8; ++j) cv[j] = __builtin_amdgcn_raw_buffer_load_b32(rsWs, WS_CSP + ((m * 16 + 8 * hl) * 6 + bl) * 128 + t32 * 4, j * 768, 16);
#pragma unroll
      for (int j = 0; j < 8; ++j) s += uf(cv[j]);
      float s2 = s;
      swap32(s, s2);
      s += s2;
      const unsigned hi = pk2(s, 0.f) & 0xffffu, lo = pk2(s - uf(hi << 16), 0.f) & 0xffffu;
      if (lane < 32) *(LAS v4u*)(L + L_CSF + bl * 512 + lane * 16) = (v4u){hi | (hi << 16), lo, 0, 0};
    }
    BAR();
    BAR();
    GU_LOADW(3, L_GW3)
    pollN<16>(p.ws + WS_F3 + (rt * 4 + w4) * 128, tag, lane);
    STAMPW(4, 8);
    const unsigned hsrc = WS_H + (rt * 4 + w4) * 32 * 1024;
    const unsigned hdst = w4 * 4096;
#define GU_LOADH(st, slot)                                           \
  {                                                                  \
    _Pragma("unroll") for (int j = 0; j < 4; ++j) dma16_sc1(lb + (slot) + hdst + j * 1024, voffL, rsWs, hsrc + (((st) + kro) & 7) * 4096 + j * 1024); \
  }
    GU_LOADH(0, L_GH0)
    WAITV_BAR(0);
    GU_LOADH(1, L_GH1)
    GU_LOADH(2, L_GH2)
    WAITV_BAR(4);
    GU_LOADH(3, L_GH0)
    v4u sv[8];
    const unsigned svo = WS_ST + ((rt * 4 + w4) * 16 + 8 * hl) * 512 + t32 * 16;
#pragma unroll
    for (int i = 0; i < 8; ++i) sv[i] = ld128_sc1(rsWs, svo, i * 512);
    GU_LOADW(4, L_GW0)
    WAITV_BAR(18);
    GU_LOADH(4, L_GH1)
    GU_LOADW(5, L_GW1)
    asm volatile("s_waitcnt vmcnt(16)" : "+v"(sv[0]), "+v"(sv[1]), "+v"(sv[2]), "+v"(sv[3]), "+v"(sv[4]), "+v"(sv[5]), "+v"(sv[6]), "+v"(sv[7])::"memory");
    {
      bool ok = true;
#pragma unroll
      for (int i = 0; i < 8; ++i) ok &= (sv[i][1] == (unsigned)tag) & (sv[i][3] == (unsigned)tag);
#pragma unroll 1
      for (unsigned it = 0; !__all(ok) && it < SPIN_CAP; ++it) {
        ok = true;
#pragma unroll
        for (int i = 0; i < 8; ++i) {
          sv[i] = __builtin_amdgcn_raw_buffer_load_b128(rsWs, svo, i * 512, 16);
          ok &= (sv[i][1] == (unsigned)tag) & (sv[i][3] == (unsigned)tag);
        }
      }
      float s = 0.f, q = 0.f;
#pragma unroll
      for (int i = 0; i < 8; ++i) {
        s += uf(sv[i][0]);
        q += uf(sv[i][2]);
      }
      swap32(s, q);
      float t = s + q, t2 = t;
      swap32(t, t2);
      const float mean2 = t * (1.f / 512.f);
      const float var = fmaxf(t2 * (1.f / 512.f) - mean2 * mean2, 0.f) + 1e-5f;
      const float nm = -mean2;
      const unsigned hi = pk2(nm, 0.f) & 0xffffu, lo = pk2(nm - uf(hi << 16), 0.f) & 0xffffu;
      if (lane < 32) {
        *(LAS v4u*)(L + L_MF + w4 * 512 + lane * 16) = (v4u){hi | (lo << 16), hi, 0, 0};
        *(LAS float*)(L + L_R2 + (32 * w4 + lane) * 4) = __builtin_amdgcn_rcpf(var);
      }
    }
    const rsrc_t rsWd = mkrsrc(p.wd, 1376u * 1024u);
    const unsigned dsrc = (16 * 43 * (w4 >> 1) + 8 * (w4 & 1)) * 1024 + 128 * cg;
#define DN_LOAD(i0)                                                                                            \
  {                                                                                                            \
    _Pragma("unroll") for (int j = 0; j < dn_n(i0); ++j)                                                       \
        dma16(lb + dn_base(i0) + ((w4 >> 1) * dn_n(i0) + j) * 2048 + (w4 & 1) * 1024, voffR, rsWd, dsrc + ((i0) + j) * 16384); \
  }
#pragma unroll 1
    for (int st = 3; st < 8; ++st) {
      if (st >= 6) WAITV(17);
      else WAITV(16);
      BAR();
      const int h3 = st >= 6 ? st - 6 : (st >= 3 ? st - 3 : st);
      const unsigned hfree = h3 == 0 ? L_GH2 : L_GH0 + (h3 - 1) * 16384;
      const unsigned wfree = (st & 3) == 0 ? L_GW3 : L_GW0 + ((st & 3) - 1) * GU_SLOT;
      if (st <= 5) GU_LOADH(st + 2, hfree)
      if (st <= 4) GU_LOADW(st + 3, wfree)
      else if (st == 5) { DN_LOAD(0) DN_LOAD(6) }
      else if (st == 6) { DN_LOAD(7) DN_LOAD(13) }
      else { DN_LOAD(17) DN_LOAD(23) }
    }
    BAR();
    BAR();
    DN_LOAD(27) DN_LOAD(33)
    STAMPW(4, 31);
    WAITV_BAR(0);
    BAR();
    DN_LOAD(37)
    WAITV_BAR(0);
    BAR();
    asm volatile(".Lfb_end:");
  } else {
    if (tid < 4) *(LAS unsigned*)(L + L_CNT + tid * 4) = 0;
    unsigned cdummy = 0;
    {
      u64 pc;
      unsigned len;
      asm volatile("s_getpc_b64 %0\n.Lfb_here:\n\ts_mov_b32 %1, .Lfb_end-.Lfb_here" : "=s"(pc), "=s"(len));
      const unsigned o = (jj * 14 + (lane < 14 ? lane : 0)) * 128;
      const unsigned char* q = (const unsigned char*)pc + (o + 128 <= len ? o : 0);
      asm volatile("global_load_dword %0, %1, off" : "=v"(cdummy) : "v"(q));
    }
    v4u xf[16][2];
    f32x4 ak[4][2];
    const f32x4 z4 = {0, 0, 0, 0};
    f32x2 stt[2];
#pragma unroll
    for (int j = 0; j < 4; ++j) ak[j][0] = ak[j][1] = (f32x4){0, 0, 0, 0};
    {
      const unsigned xr0 = lb + (32 * w4 + l15) * 128;
      const unsigned xlo0 = opaque(xr0 + ((g ^ (l15 >> 1)) << 4)), xlo1 = opaque(xr0 + (((4 + g) ^ (l15 >> 1)) << 4));
      const unsigned xhi0 = opaque(xlo0 + 2 * A_SLOT), xhi1 = opaque(xlo1 + 2 * A_SLOT);
      const unsigned w0lo = opaque(lb + trW0), w1lo = opaque(lb + trW1), w0hi = opaque(w0lo + 2 * A_SLOT), w1hi = opaque(w1lo + 2 * A_SLOT);
      const unsigned clo = opaque(((w4 & 1) ? w1lo : w0lo) + (w4 >> 1) * 4096), chi = opaque(clo + 2 * A_SLOT);
      s16x8 fa[2][4], fc[2];
      f32x4 asum[2] = {z4, z4}, agr[2] = {z4, z4}, acs = z4;
#define A1_X(s, a) xf[s][a] = *(LAS v4u*)(L0 + ((((s) >> 1) & 2) ? (((s) & 1) ? xhi1 : xhi0) : (((s) & 1) ? xlo1 : xlo0)) + (((s) >> 1) & 1) * A_SLOT + (a) * 2048); SB()
#define A1_WA(s, j) (((((s) >> 1) & 2) ? (((j) & 1) ? w1hi : w0hi) : (((j) & 1) ? w1lo : w0lo)) + (((s) >> 1) & 1) * A_SLOT + ((j) >> 1) * 4096 + ((s) & 1) * 2048)
#define A1_W(s, j) fa[(s) & 1][j] = cat(trread(L0, A1_WA(s, j)), trread(L0, A1_WA(s, j) + 256)); SB()
#define A1_CA(s) (((((s) >> 1) & 2) ? chi : clo) + (((s) >> 1) & 1) * A_SLOT + ((s) & 1) * 2048)
#define A1_C(s) fc[(s) & 1] = cat(trread(L0, A1_CA(s)), trread(L0, A1_CA(s) + 256)); SB()
#define A1_M(s, j, a)                                           \
  ak[j][a] = mfma16(fa[(s) & 1][j], asfrag(xf[s][a]), ak[j][a]); \
  TIE(ak[j][a]);                                                \
  SB()
#define A1_S(s, a)                                               \
  asum[a] = mfma16(ones8, asfrag(xf[s][a]), asum[a]);            \
  TIE(asum[a]);                                                  \
  agr[a] = mfma16(asfrag(xf[s][a]), asfrag(xf[s][a]), agr[a]);   \
  TIE(agr[a]);                                                   \
  SB()
#define A1_CM(s)                              \
  acs = mfma16(fc[(s) & 1], ones8, acs);      \
  TIE(acs);                                   \
  SB()
#define A1_ALLM(s)                                                            \
  A1_M(s, 0, 0); A1_M(s, 0, 1); A1_M(s, 1, 0); A1_M(s, 1, 1); A1_M(s, 2, 0); A1_M(s, 2, 1); A1_M(s, 3, 0); A1_M(s, 3, 1); \
  A1_S(s, 0); A1_S(s, 1); A1_CM(s);
#define A1_RM(sn, sc)                                                         \
  A1_X(sn, 0); A1_M(sc, 0, 0); A1_W(sn, 0); A1_M(sc, 0, 1);                   \
  A1_X(sn, 1); A1_M(sc, 1, 0); A1_W(sn, 1); A1_M(sc, 1, 1);                   \
  A1_W(sn, 2); A1_M(sc, 2, 0); A1_M(sc, 2, 1);                                \
  A1_W(sn, 3); A1_M(sc, 3, 0); A1_M(sc, 3, 1);                                \
  A1_C(sn); A1_S(sc, 0); A1_S(sc, 1); A1_CM(sc);                              \
  SB();
#define A1_STAGE(st) BAR(); A1_RM(2 * (st), 2 * (st) - 1) A1_RM(2 * (st) + 1, 2 * (st))
      BAR();
      STAMP(14); CSTAMP(52);
      A1_X(0, 0); A1_X(0, 1); A1_W(0, 0); A1_W(0, 1); A1_W(0, 2); A1_W(0, 3); A1_C(0);
      SB();
      A1_RM(1, 0)
      A1_STAGE(1)
      A1_STAGE(2) A1_STAGE(3) A1_STAGE(4) A1_STAGE(5) A1_STAGE(6) A1_STAGE(7)
      A1_ALLM(15)
      if (l15 == 0) *(LAS f32x4*)(L + L_CS + w4 * 64 + g * 16) = acs;
#pragma unroll
      for (int a = 0; a < 2; ++a) {
        const float a01 = (p4 & 1) ? agr[a][1] : agr[a][0], a23 = (p4 & 1) ? agr[a][3] : agr[a][2];
        const float d = (p4 & 2) ? a23 : a01;
        stt[a][1] = (g == q4) ? d : 0.f;
      }
      float d0 = stt[0][1], d1 = stt[1][1];
      red2add(d0, d1);
      const float mean0 = asum[0][0] * (1.f / 512.f), mean1 = asum[1][0] * (1.f / 512.f);
      const float r0 = __builtin_amdgcn_rsqf(fmaxf(d0 * (1.f / 512.f) - mean0 * mean0, 0.f) + 1e-5f);
      const float r1 = __builtin_amdgcn_rsqf(fmaxf(d1 * (1.f / 512.f) - mean1 * mean1, 0.f) + 1e-5f);
      stt[0] = (f32x2){r0, -mean0 * r0};
      stt[1] = (f32x2){r1, -mean1 * r1};
    }
    STAMP(1); CSTAMP(53);
    asm volatile("s_waitcnt lgkmcnt(0)\n\ts_barrier" : "+v"(ep)::"memory");
    STAMP(26); CSTAMP(54);
    asm volatile("s_waitcnt vmcnt(0)" : "+v"(ep), "+v"(cdummy));
    const u64 tag = mix64(((u64)(unsigned)__builtin_amdgcn_readfirstlane(ep[1]) << 32) | (unsigned)__builtin_amdgcn_readfirstlane(ep[0]));

    const s16x4 wphi0 = trread(L, L_WPHI + (4 * g + q4) * 32 + 8 * p4);
    const s16x4 wphi1 = trread(L, L_WPHI + 512 + (4 * g + q4) * 32 + 8 * p4);
    {
      f32x4 cs[4];
#pragma unroll
      for (int i = 0; i < 4; ++i) cs[i] = *(LAS f32x4*)(L + L_CS + i * 64 + g * 16);
#pragma unroll
      for (int a = 0; a < 2; ++a) {
        const float rstd = stt[a][0], nb = stt[a][1];
        const f32x4 k0 = ak[0][a] * rstd + cs[0] * nb, k1 = ak[1][a] * rstd + cs[1] * nb;
        const f32x4 v0 = ak[2][a] * rstd + cs[2] * nb, v1 = ak[3][a] * rstd + cs[3] * nb;
        const v2u k0b = {pk2(k0[0], k0[1]), pk2(k0[2], k0[3])}, k1b = {pk2(k1[0], k1[1]), pk2(k1[2], k1[3])};
        const f32x4 p0 = mfma16k16(wphi0, __builtin_bit_cast(s16x4, k0b), z4);
        const f32x4 p1 = mfma16k16(wphi1, __builtin_bit_cast(s16x4, k1b), z4);
        const unsigned wa = L_KVIMG + (32 * w4 + 16 * a + l15) * 32 + 8 * (g ^ q4);
        *(LAS v2u*)(L + wa + 4 * 4096) = (v2u){pk2(v0[0], v0[1]), pk2(v0[2], v0[3])};
        *(LAS v2u*)(L + wa + 5 * 4096) = (v2u){pk2(v1[0], v1[1]), pk2(v1[2], v1[3])};
        v2u pos0, neg0, pos1, neg1;
        dual_softmax(p0, p1, pos0, neg0, pos1, neg1);
        *(LAS v2u*)(L + wa + 0 * 4096) = pos0;
        *(LAS v2u*)(L + wa + 1 * 4096) = neg0;
        *(LAS v2u*)(L + wa + 2 * 4096) = pos1;
        *(LAS v2u*)(L + wa + 3 * 4096) = neg1;
      }
    }
    lds_arrive(L + L_CNT, lane);
    lds_wait(L + L_CNT, 4);
    STAMP(15); CSTAMP(55);
    {
      const unsigned ra = L_KVIMG + (4 * g + q4) * 32 + 8 * (p4 ^ g);
      const unsigned raA = ra + w4 * 4096, raB = ra + (4 + (w4 >> 1)) * 4096;
      f32x4 acc = z4, accz = z4;
      s16x8 fA[4], fB[4];
#pragma unroll
      for (int u = 0; u < 4; ++u) {
        fA[u] = cat(trread(L, raA + u * 1024), trread(L, raA + u * 1024 + 512));
        fB[u] = cat(trread(L, raB + u * 1024), trread(L, raB + u * 1024 + 512));
      }
#pragma unroll
      for (int u = 0; u < 4; ++u) { TIE(fA[u]); TIE(fB[u]); }
      lds_arrive(L + L_CNT + 8, lane);
#pragma unroll
      for (int u = 0; u < 4; ++u) {
        acc = mfma16(fA[u], fB[u], acc);
        accz = mfma16(fA[u], ones8, accz);
      }
      const unsigned base = WS_KVP + (m * 16 + rt) * KVP_STRIDE;
      __builtin_amdgcn_raw_buffer_store_b128(__builtin_bit_cast(v4u, acc), rsWs, base + w4 * 1024 + lane * 16, 0, 16);
      __builtin_amdgcn_raw_buffer_store_b128(__builtin_bit_cast(v4u, accz), rsWs, l15 == 0 ? base + 4096 + w4 * 64 + g * 16 : 0x7FFFFFF0u, 0, 16);
      WAITV(0);
      if (lane == 0) set_flag(p.ws + WS_F1 + m * 512 + (rt * 4 + w4) * 8, tag);
    }
    STAMP(2);

    v4u phq[2][2];
    {
      f32x4 aq[2][2] = {{z4, z4}, {z4, z4}};
      const unsigned trQ0 = opaque(lb + L_WQ - 16384 + trW0), trQ1 = opaque(lb + L_WQ - 16384 + trW1);
      s16x8 fq[2][2];
#define A2_W(s, j) fq[(s) & 1][j] = cat(trread(L0, ((j) ? trQ1 : trQ0) + (s) * 2048), trread(L0, ((j) ? trQ1 : trQ0) + (s) * 2048 + 256)); SB()
#define A2_M(s, j, a)                                           \
  aq[j][a] = mfma16(fq[(s) & 1][j], asfrag(xf[s][a]), aq[j][a]); \
  TIE(aq[j][a]);                                                \
  SB()
#define A2_STEP(s)                                \
  if ((s) < 15) { A2_W(((s) + 1) & 15, 0); }      \
  A2_M(s, 0, 0); A2_M(s, 0, 1);                   \
  if ((s) < 15) { A2_W(((s) + 1) & 15, 1); }      \
  A2_M(s, 1, 0); A2_M(s, 1, 1);                   \
  SB();
      A2_W(0, 0); A2_W(0, 1);
      SB();
      A2_STEP(0) A2_STEP(1) A2_STEP(2) A2_STEP(3) A2_STEP(4) A2_STEP(5) A2_STEP(6) A2_STEP(7)
      A2_STEP(8) A2_STEP(9) A2_STEP(10) A2_STEP(11) A2_STEP(12) A2_STEP(13) A2_STEP(14) A2_STEP(15)
      lds_arrive(L + L_CNT + 12, lane);
      STAMP(27);
      lds_wait(L + L_CNT + 4, 4);
      const f32x4 c0 = *(LAS f32x4*)(L + L_CS + 256 + g * 16) + *(LAS f32x4*)(L + L_CS + 320 + g * 16);
      const f32x4 c1 = *(LAS f32x4*)(L + L_CS + 384 + g * 16) + *(LAS f32x4*)(L + L_CS + 448 + g * 16);
#pragma unroll
      for (int a = 0; a < 2; ++a) {
        const f32x4 q0 = aq[0][a] * stt[a][0] + c0 * stt[a][1], q1 = aq[1][a] * stt[a][0] + c1 * stt[a][1];
        const v2u q0b = {pk2(q0[0], q0[1]), pk2(q0[2], q0[3])}, q1b = {pk2(q1[0], q1[1]), pk2(q1[2], q1[3])};
        const f32x4 p0 = mfma16k16(wphi0, __builtin_bit_cast(s16x4, q0b), z4);
        const f32x4 p1 = mfma16k16(wphi1, __builtin_bit_cast(s16x4, q1b), z4);
        v2u pos0, neg0, pos1, neg1;
        dual_softmax(p0, p1, pos0, neg0, pos1, neg1);
        phq[a][0] = (v4u){pos0[0], pos0[1], neg0[0], neg0[1]};
        phq[a][1] = (v4u){pos1[0], pos1[1], neg1[0], neg1[1]};
      }
    }
    STAMP(3);

    {
      pollN<64>(p.ws + WS_F1 + m * 512, tag, lane);
      STAMP(4);
      const unsigned base = WS_KVP + m * 16 * KVP_STRIDE;
      v4u pv[16], zv[4];
#pragma unroll
      for (int i = 0; i < 4; ++i) zv[i] = __builtin_amdgcn_raw_buffer_load_b128(rsWs, base + g * KVP_STRIDE + 4096 + l15 * 16, 4 * i * KVP_STRIDE, 16);
#pragma unroll
      for (int i = 0; i < 16; ++i) pv[i] = __builtin_amdgcn_raw_buffer_load_b128(rsWs, base + tid * 16, i * KVP_STRIDE, 16);
      f32x4 sz = __builtin_bit_cast(f32x4, zv[0]);
#pragma unroll
      for (int i = 1; i < 4; ++i) sz += __builtin_bit_cast(f32x4, zv[i]);
      float z0 = sz[0], z1 = sz[1], z2 = sz[2], z3 = sz[3];
      red4<false>(z0, z1, z2, z3);
      if (tid < 16) *(LAS v2u*)(L + L_ZF + (l15 >> 3) * 64 + (l15 & 3) * 16 + ((l15 >> 2) & 1) * 8) = (v2u){pk2(z0, z1), pk2(z2, z3)};
      f32x4 s = __builtin_bit_cast(f32x4, pv[0]);
#pragma unroll
      for (int i = 1; i < 16; ++i) s += __builtin_bit_cast(f32x4, pv[i]);
      *(LAS v2u*)(L + L_KF + (w >> 1) * 1024 + lane * 16 + (w & 1) * 8) = (v2u){pk2(s[0], s[1]), pk2(s[2], s[3])};
    }
    STAMP(33);
    BAR();
    {
      const s16x8 kf0 = *(LAS s16x8*)(L + L_KF + lane * 16), kf1 = *(LAS s16x8*)(L + L_KF + 1024 + lane * 16);
      const s16x8 zf0 = *(LAS s16x8*)(L + L_ZF + g * 16), zf1 = *(LAS s16x8*)(L + L_ZF + 64 + g * 16);
#pragma unroll
      for (int a = 0; a < 2; ++a) {
        const f32x4 n0 = mfma16(kf0, asfrag(phq[a][0]), z4), d0 = mfma16(zf0, asfrag(phq[a][0]), z4);
        const f32x4 n1 = mfma16(kf1, asfrag(phq[a][1]), z4), d1 = mfma16(zf1, asfrag(phq[a][1]), z4);
        const float i0 = __builtin_amdgcn_rcpf(d0[0] + 1e-6f), i1 = __builtin_amdgcn_rcpf(d1[0] + 1e-6f);
        const v4u ov = {pk2(n0[0] * i0, n0[1] * i0), pk2(n0[2] * i0, n0[3] * i0), pk2(n1[0] * i1, n1[1] * i1), pk2(n1[2] * i1, n1[3] * i1)};
        __builtin_amdgcn_raw_buffer_store_b128(ov, rsWs, WS_O + ((rt * 4 + w4) * 32 + 2 * m + (g >> 1)) * 1024 + ((g & 1) * 32 + 16 * a + l15) * 16, 0, 16);
      }
      WAITV(0);
      if (lane < 2) set_flag(p.ws + WS_F2 + ((rt * 4 + w4) * 2 + hf) * 128 + (lane * 8 + cg) * 8, tag);
    }
    STAMP(5);

    const int tb32 = 2 * hf + (w & 1), kh = (w >> 1) & 1;
    unsigned hres[8];
    const unsigned tok = 128 * rt + 32 * tb32 + t32;
    v2u xr[4];
#pragma unroll
    for (int i = 0; i < 4; ++i) xr[i] = ld64(rsX, tok * 1024 + (64 * cg + 32 * kh + 4 * hl) * 2, i * 16);
    const unsigned relR = (4 * (g >> 1) + q4) * 128 + (g & 1) * 32 + 8 * p4;
    const unsigned rel0 = lb + relR + ((0 ^ (q4 >> 1)) << 6), rel1 = lb + relR + ((1 ^ (q4 >> 1)) << 6);
    f32x16 a0, a1;
#pragma unroll
    for (int i = 0; i < 16; ++i) { a0[i] = 0.f; a1[i] = 0.f; }
    {
      const unsigned rl0 = opaque(rel0 + kh * 32768), rl1 = opaque(rel1 + kh * 32768);
      s16x8 fw[2][2];
#define WO_RD1(i, c)                                                                                    \
  {                                                                                                     \
    constexpr unsigned o_ = (32 * ((i) >> 1) + 8 * ((i) & 1)) * 128;                                    \
    fw[(i) & 1][c] = cat(trread(L0, ((c) ? rl1 : rl0) + o_), trread(L0, ((c) ? rl1 : rl0) + o_ + 2048)); \
    SB();                                                                                               \
  }
#define WO_RD(i) WO_RD1(i, 0) WO_RD1(i, 1)
      WO_RD(0)
      pollN<16>(p.ws + WS_F2 + ((rt * 4 + tb32) * 2 + kh) * 128, tag, lane);
      STAMP(6);
      v4u of[16];
      const unsigned ov = WS_O + ((rt * 4 + tb32) * 32 + 16 * kh) * 1024 + lane * 16;
#pragma unroll
      for (int i = 0; i < 16; ++i) of[i] = ld128_sc1(rsWs, ov, i * 1024);
#define WO_STEP(i)                                          \
  {                                                         \
    if ((i) < 15) WO_RD1(((i) + 1) & 15, 0)                 \
    waitv_tie<15 - (i)>(of[i]);                             \
    a0 = mfma32(fw[(i) & 1][0], asfrag(of[i]), a0);         \
    TIE(a0);                                                \
    SB();                                                   \
    if ((i) < 15) WO_RD1(((i) + 1) & 15, 1)                 \
    a1 = mfma32(fw[(i) & 1][1], asfrag(of[i]), a1);         \
    TIE(a1);                                                \
    SB();                                                   \
  }
      WO_STEP(0) WO_STEP(1) WO_STEP(2) WO_STEP(3) WO_STEP(4) WO_STEP(5) WO_STEP(6) WO_STEP(7)
      WO_STEP(8) WO_STEP(9) WO_STEP(10) WO_STEP(11) WO_STEP(12) WO_STEP(13) WO_STEP(14) WO_STEP(15)
    }
    STAMP(16);
    {
      const f32x16 give = kh ? a0 : a1;
      f32x16 keep = kh ? a1 : a0;
#pragma unroll
      for (int i = 0; i < 4; ++i)
        *(LAS f32x4*)(L + L_RED_WO + (w * 4 + i) * 1024 + lane * 16) = (f32x4){give[4 * i], give[4 * i + 1], give[4 * i + 2], give[4 * i + 3]};
      BAR();
      float ss = 0.f, sq = 0.f;
#pragma unroll
      for (int i = 0; i < 4; ++i) {
        const f32x4 o = *(LAS f32x4*)(L + L_RED_WO + ((w ^ 2) * 4 + i) * 1024 + lane * 16);
        TIE(xr[i]);
        hres[2 * i] = pk2(keep[4 * i] + o[0] + bflo(xr[i][0]), keep[4 * i + 1] + o[1] + bfhi(xr[i][0]));
        hres[2 * i + 1] = pk2(keep[4 * i + 2] + o[2] + bflo(xr[i][1]), keep[4 * i + 3] + o[3] + bfhi(xr[i][1]));
      }
      const int Sg = 2 * cg + kh;
      const unsigned ho = WS_H + ((rt * 4 + tb32) * 32 + 2 * Sg) * 1024 + lane * 16;
      __builtin_amdgcn_raw_buffer_store_b128((v4u){hres[0], hres[1], hres[2], hres[3]}, rsWs, ho, 0, 16);
      __builtin_amdgcn_raw_buffer_store_b128((v4u){hres[4], hres[5], hres[6], hres[7]}, rsWs, ho + 1024, 0, 16);
      WAITV(0);
      if (lane == 0) set_flag(p.ws + WS_F3 + (rt * 4 + tb32) * 128 + Sg * 8, tag);
      STAMP(36);
#pragma unroll
      for (int i = 0; i < 8; ++i) {
        const float lo = bflo(hres[i]), hi = bfhi(hres[i]);
        ss += lo + hi;
        sq = fmaf(lo, lo, fmaf(hi, hi, sq));
      }
      swap32(ss, sq);
      __builtin_amdgcn_raw_buffer_store_b64((v2u){fu(ss + sq), (unsigned)tag}, rsWs, WS_ST + ((rt * 4 + tb32) * 16 + Sg) * 512 + t32 * 16 + hl * 8, 0, 16);
    }
    BAR();
    STAMP(7);

    const int mh = w & 1, nh = (w >> 1) & 1;
    f32x16 acc[2][3];
#pragma unroll
    for (int i = 0; i < 16; ++i) {
      acc[0][0][i] = 0.f; acc[0][1][i] = 0.f; acc[0][2][i] = 0.f;
      acc[1][0][i] = 0.f; acc[1][1][i] = 0.f; acc[1][2][i] = 0.f;
    }
    {
      const unsigned relT = lb + mh * 8192 + lane * 16;
      const unsigned relW = lb + (p4 >> 1) * GU_UPOFF + (4 * (g >> 1) + q4) * 192 + 16 * (g & 1) + 8 * (p4 & 1) + 96 * nh;
      v4u tf[2][2];
      s16x8 ga[2][3];
      tf[1][0] = tf[1][1] = (v4u){0, 0, 0, 0};
      ga[1][0] = ga[1][1] = ga[1][2] = asfrag(tf[1][0]);
#define GU_RT(j, tt) tf[(j) & 1][tt] = *(LAS v4u*)(L0 + aT + (tt) * 4096 + (j) * 1024); SB()
#define GU_RW(j, l3) ga[(j) & 1][l3] = cat(trread(L0, aW + (j) * 16 * 192 + 32 * (l3)), trread(L0, aW + ((j) * 16 + 8) * 192 + 32 * (l3))); SB()
#define GU_M(j, tt, l3)                                                       \
  acc[tt][l3] = mfma32(ga[(j) & 1][l3], asfrag(tf[(j) & 1][tt]), acc[tt][l3]); \
  TIE(acc[tt][l3]);                                                            \
  SB()
#define GU_STEP(jn, jc)            \
  GU_RT(jn, 0);                    \
  GU_M(jc, 0, 0);                  \
  GU_RW(jn, 0);                    \
  GU_M(jc, 0, 1);                  \
  GU_RW(jn, 1);                    \
  GU_M(jc, 0, 2);                  \
  GU_RT(jn, 1);                    \
  GU_M(jc, 1, 0);                  \
  GU_RW(jn, 2);                    \
  GU_M(jc, 1, 1);                  \
  GU_M(jc, 1, 2);                  \
  SB();
#pragma unroll 1
      for (int st = 0; st < 8; ++st) {
        BAR();
        if (st == 0) STAMP(17);
        if (st == 1) { STAMP(18); CSTAMP(50); }
        if (st == 7) { STAMP(24); CSTAMP(51); }
        const int h3 = st >= 6 ? st - 6 : (st >= 3 ? st - 3 : st);
        const unsigned hcur = h3 == 2 ? L_GH2 : L_GH0 + h3 * 16384;
        const unsigned wcur = (st & 3) == 3 ? L_GW3 : L_GW0 + (st & 3) * GU_SLOT;
        const unsigned aT = relT + hcur, aW = relW + wcur;
        if (6 * m + 3 * nh < 86) {
          GU_STEP(0, 3)
          GU_STEP(1, 0)
          GU_STEP(2, 1)
          GU_STEP(3, 2)
        }
      }
      const unsigned zo = lb + L_ZERO;
      const unsigned aC = lane < 32 ? lb + L_CSF + 3 * nh * 512 + lane * 16 : zo - 0, aM = lane < 32 ? lb + L_MF + 2 * mh * 512 + lane * 16 : zo;
      const unsigned stp = lane < 32 ? 512 : 0;
      tf[0][0] = *(LAS v4u*)(L0 + aM);
      tf[0][1] = *(LAS v4u*)(L0 + aM + stp);
      ga[0][0] = *(LAS s16x8*)(L0 + aC);
      ga[0][1] = *(LAS s16x8*)(L0 + aC + stp);
      ga[0][2] = *(LAS s16x8*)(L0 + aC + 2 * stp);
      GU_M(3, 0, 0); GU_M(3, 0, 1); GU_M(3, 0, 2); GU_M(3, 1, 0); GU_M(3, 1, 1); GU_M(3, 1, 2);
      GU_M(0, 0, 0); GU_M(0, 0, 1); GU_M(0, 0, 2); GU_M(0, 1, 0); GU_M(0, 1, 1); GU_M(0, 1, 2);
    }
    const float r2a = *(LAS float*)(L + L_R2 + (64 * mh + t32) * 4), r2b = *(LAS float*)(L + L_R2 + (64 * mh + 32 + t32) * 4);
    BAR();
    STAMP(9);
#pragma unroll
    for (int tt = 0; tt < 2; ++tt) {
      const float r2 = tt ? r2b : r2a;
#pragma unroll
      for (int l3 = 0; l3 < 3; ++l3) {
        const int fblk = 6 * m + 3 * nh + l3;
        float hv[8];
#pragma unroll
        for (int i = 0; i < 2; ++i)
#pragma unroll
          for (int e = 0; e < 4; ++e) hv[4 * i + e] = (acc[tt][l3][8 * i + e] * r2) * acc[tt][l3][8 * i + 4 + e];
        const v4u o = {pk2(hv[0], hv[1]), pk2(hv[2], hv[3]), pk2(hv[4], hv[5]), pk2(hv[6], hv[7])};
        const unsigned off = fblk < 86 ? WS_HH + ((rt * 4 + 2 * mh + tt) * 86 + fblk) * 1024 + lane * 16 : 0x7FFFFFF0u;
        __builtin_amdgcn_raw_buffer_store_b128(o, rsWs, off, 0, 16);
      }
    }
    asm volatile("s_barrier" ::: "memory");
    WAITV(0);
    if (lane < 2) set_flag(p.ws + WS_F4 + (rt * 4 + 2 * mh + lane) * 256 + (m * 2 + nh) * 8, tag);
    STAMP(10);

    const unsigned kh2048 = kh * 2048;
    const unsigned pb0 = opaque(rel0 + dn_base(0) + dn_n(0) * kh2048), pb1 = opaque(rel1 + dn_base(0) + dn_n(0) * kh2048);
    BAR();
    s16x8 wpre[DN_PRE][2];
#pragma unroll
    for (int i = 0; i < DN_PRE; ++i) {
      wpre[i][0] = cat(trread(L0, pb0 + i * 2048), trread(L0, pb0 + i * 2048 + 1024));
      wpre[i][1] = cat(trread(L0, pb1 + i * 2048), trread(L0, pb1 + i * 2048 + 1024));
    }
#pragma unroll
    for (int i = 0; i < DN_PRE; ++i) { TIE(wpre[i][0]); TIE(wpre[i][1]); }
    BAR();
    STAMP(25);
    pollN<32>(p.ws + WS_F4 + (rt * 4 + tb32) * 256, tag, lane);
    STAMP(11); CSTAMP(56);
    v4u hf_[DN_RING];
    const unsigned hv = WS_HH + ((rt * 4 + tb32) * 86 + 43 * kh) * 1024 + lane * 16;
#pragma unroll
    for (int i = 0; i < DN_RING; ++i) hf_[i] = ld128_sc1(rsWs, hv, i * 1024);
#pragma unroll
    for (int i = 0; i < 16; ++i) { a0[i] = 0.f; a1[i] = 0.f; }
    s16x8 fw[2][2];
    DnStep<0>::run(hf_, wpre, fw, a0, a1, rel0, rel1, kh2048, pb0, pb1, rsWs, hv);
    STAMP(12); CSTAMP(57);
    {
      int tz = threadIdx.x;
      asm volatile("" : "+v"(tz));
      const int lane = tz & 63, hl = lane >> 5, t32 = lane & 31;
      const f32x16 give = kh ? a0 : a1;
      const f32x16 keep = kh ? a1 : a0;
#pragma unroll
      for (int i = 0; i < 4; ++i)
        *(LAS f32x4*)(L + L_RED_DN + (w * 4 + i) * 1024 + lane * 16) = (f32x4){give[4 * i], give[4 * i + 1], give[4 * i + 2], give[4 * i + 3]};
      BAR();
      unsigned ob[8];
#pragma unroll
      for (int i = 0; i < 4; ++i) {
        const f32x4 o = *(LAS f32x4*)(L + L_RED_DN + ((w ^ 2) * 4 + i) * 1024 + lane * 16);
        ob[2 * i] = pk2(keep[4 * i] + o[0] + bflo(hres[2 * i]), keep[4 * i + 1] + o[1] + bfhi(hres[2 * i]));
        ob[2 * i + 1] = pk2(keep[4 * i + 2] + o[2] + bflo(hres[2 * i + 1]), keep[4 * i + 3] + o[3] + bfhi(hres[2 * i + 1]));
      }
      const rsrc_t rsOut = mkrsrc(p.out, 2048u * 1024u);
      const unsigned oo = (128 * rt + 32 * tb32 + t32) * 1024 + (64 * cg + 32 * kh + 8 * hl) * 2;
#pragma unroll
      for (int pr = 0; pr < 2; ++pr) {
        swap32u(ob[4 * pr], ob[4 * pr + 2]);
        swap32u(ob[4 * pr + 1], ob[4 * pr + 3]);
        if (b == 0 && w == 0 && pr == 0) {
          u64 tend_;
          asm volatile("s_memrealtime %0\n\ts_waitcnt lgkmcnt(0)" : "=s"(tend_)::"memory");
          u64 mn = ~0ull, mx = 0;
          for (int i = 0; i < 4; ++i) {
            const u64 v = __hip_atomic_load((GAS u64*)(p.ws + WS_STAMP) + lane * 4 + i, __ATOMIC_RELAXED, __HIP_MEMORY_SCOPE_AGENT);
            mn = v < mn ? v : mn; mx = v > mx ? v : mx;
          }
          unsigned lo = (unsigned)(tend_ - mn), hi = (unsigned)(tend_ - mx);
          for (int o = 32; o >= 1; o >>= 1) { lo = max(lo, (unsigned)__shfl_xor((int)lo, o)); hi = min(hi, (unsigned)__shfl_xor((int)hi, o)); }
          const float val = lo * 0.01f;
          if (val > DIAG_T && lane == 0) ob[0] = pk2(val, val);
        }
        __builtin_amdgcn_raw_buffer_store_b128((v4u){ob[4 * pr], ob[4 * pr + 1], ob[4 * pr + 2], ob[4 * pr + 3]}, rsOut, oo + pr * 32, 0, 0);
      }
    }
    if (b == 0 && tid == 0) __hip_atomic_store((GAS u64*)(p.ws + WS_EPOCH), tag, __ATOMIC_RELAXED, __HIP_MEMORY_SCOPE_AGENT);
    STAMP(13);
  }
}

}

extern "C" void kernel_launch(void* const* d_in, const int* in_sizes, int n_in, void* d_out, int out_size, void* d_ws, size_t ws_size,
                              hipStream_t stream) {
  Params p;
  p.x = d_in[0];
  p.wqkv = d_in[1];
  p.wphi = d_in[2];
  p.wo = d_in[3];
  p.wg = d_in[5];
  p.wu = d_in[6];
  p.wd = d_in[7];
  p.out = d_out;
  p.ws = (unsigned char*)d_ws;
  fused_block_kernel<<<256, 512, 0, stream>>>(p);
}
```

```cpp
#include <hip/hip_runtime.h>
#include <hip/hip_bf16.h>
#include <stdint.h>

#define GAS __attribute__((address_space(1)))
#define LAS __attribute__((address_space(3)))
typedef unsigned v4u __attribute__((ext_vector_type(4)));
typedef unsigned v2u __attribute__((ext_vector_type(2)));
typedef float f32x2 __attribute__((ext_vector_type(2)));
typedef float f32x4 __attribute__((ext_vector_type(4)));
typedef float f32x16 __attribute__((ext_vector_type(16)));
typedef short s16x4 __attribute__((ext_vector_type(4)));
typedef short s16x8 __attribute__((ext_vector_type(8)));
typedef __bf16 bf16x2 __attribute__((ext_vector_type(2)));
typedef __amdgpu_buffer_rsrc_t rsrc_t;
typedef unsigned long long u64;

#define STAMPS 0
#define GU_ROT 4
#define PACEN 0
#define PACEN2 0
#define PACE() do { if (PACEN) __builtin_amdgcn_s_sleep(PACEN); } while (0)
#define PACE2() do { if (PACEN2) __builtin_amdgcn_s_sleep(PACEN2); } while (0)
#define WDV 3
#define CODEPF 1
#define TOUCH 1
#define MAPPING 2
#define SB() __builtin_amdgcn_sched_barrier(0)

namespace {

constexpr unsigned WS_EPOCH = 0;
constexpr unsigned WS_F1 = 4096;
constexpr unsigned WS_F5 = 12288;
constexpr unsigned WS_F2 = 20480;
constexpr unsigned WS_F3 = 36864;
constexpr unsigned WS_F4 = 45056;
constexpr unsigned WS_KVP = 65536;
constexpr unsigned KVP_STRIDE = 4352;
constexpr unsigned WS_CSP = WS_KVP + 256 * KVP_STRIDE;
constexpr unsigned WS_ST = WS_CSP + 256 * 768;
constexpr unsigned ST_BYTES = 16 * 4 * 16 * 512;
constexpr unsigned WS_O = WS_ST + ST_BYTES;
constexpr unsigned O_BYTES = 16 * 4 * 32 * 1024;
constexpr unsigned WS_H = WS_O + O_BYTES;
constexpr unsigned H_BYTES = 16 * 4 * 32 * 1024;
constexpr unsigned WS_HH = WS_H + H_BYTES;
constexpr unsigned HH_BYTES = 16 * 4 * 86 * 1024;
constexpr unsigned WS_END = WS_HH + HH_BYTES;
constexpr unsigned WS_STAMP = 16u << 20;

constexpr unsigned A_SLOT = 24576;
constexpr unsigned L_WQ = 98304;
constexpr unsigned L_CSW = 131072;
constexpr unsigned L_KVIMG = 65536;
constexpr unsigned L_WO = 0;
constexpr unsigned GU_UPOFF = 12320, GU_SLOT = 24640;
constexpr unsigned L_GW0 = 65536, L_GW1 = L_GW0 + GU_SLOT, L_GW2 = L_GW1 + GU_SLOT, L_GW3 = 0;
constexpr unsigned L_GH0 = 24640, L_GH1 = 41024, L_GH2 = 139456, L_SPARE = 57408;
constexpr unsigned L_RED_WO = 139456;
constexpr unsigned L_RED_DN = L_GW1;
constexpr unsigned L_MISC = 155840;
constexpr unsigned L_CS = L_MISC;
constexpr unsigned L_WPHI = L_MISC + 1024;
constexpr unsigned L_STAT = L_MISC + 2048;
constexpr unsigned L_CSF = L_MISC;
constexpr unsigned L_KF = L_MISC + 3072;
constexpr unsigned L_ZF = L_MISC + 5120;
constexpr unsigned L_MF = L_MISC + 5248;
constexpr unsigned L_R2 = L_MISC + 7296;
constexpr unsigned L_ZERO = L_MISC + 7808;
constexpr unsigned L_CNT = L_MISC + 7824;
constexpr unsigned L_STMP = L_MISC + 7848;
constexpr unsigned LDS_BYTES = 163840;
static_assert(L_GW2 + GU_SLOT == L_GH2, "map");
static_assert(L_GH2 + 16384 == L_MISC, "map");
static_assert(L_ZERO + 16 <= LDS_BYTES, "map");
static_assert(L_CSW + 4 * 4608 <= L_MISC, "map");

constexpr unsigned SPIN_CAP = 1u << 18;
constexpr float LOG2E = 1.4426950408889634f;

struct Params {
  const void* x;
  const void* wqkv;
  const void* wphi;
  const void* wo;
  const void* wg;
  const void* wu;
  const void* wd;
  void* out;
  unsigned char* ws;
};

#define DI __device__ __forceinline__

DI rsrc_t mkrsrc(const void* p, unsigned bytes) { return __builtin_amdgcn_make_buffer_rsrc((void*)p, 0, (int)bytes, 0x00020000); }
DI void dma16(unsigned ldsaddr, unsigned voff, rsrc_t rs, unsigned soff) {
  asm volatile("s_mov_b32 m0, %0\n\ts_nop 0\n\tbuffer_load_dwordx4 %1, %2, %3 offen lds" ::"s"(ldsaddr), "v"(voff), "s"(rs), "s"(soff) : "memory");
}
template <unsigned OFF> DI void dma16o(unsigned ldsbase, unsigned voff, rsrc_t rs, unsigned soff) {
  asm volatile("s_add_u32 m0, %0, %4\n\ts_nop 0\n\tbuffer_load_dwordx4 %1, %2, %3 offen lds" ::"s"(ldsbase), "v"(voff), "s"(rs), "s"(soff), "n"(OFF) : "memory", "scc");
}
template <unsigned OFF> DI void dma16o_sc1(unsigned ldsbase, unsigned voff, rsrc_t rs, unsigned soff) {
  asm volatile("s_add_u32 m0, %0, %4\n\ts_nop 0\n\tbuffer_load_dwordx4 %1, %2, %3 offen sc1 lds" ::"s"(ldsbase), "v"(voff), "s"(rs), "s"(soff), "n"(OFF) : "memory", "scc");
}
DI void dma16_sc1(unsigned ldsaddr, unsigned voff, rsrc_t rs, unsigned soff) {
  asm volatile("s_mov_b32 m0, %0\n\ts_nop 0\n\tbuffer_load_dwordx4 %1, %2, %3 offen sc1 lds" ::"s"(ldsaddr), "v"(voff), "s"(rs), "s"(soff) : "memory");
}
DI v4u ld128(rsrc_t rs, unsigned voff, unsigned soff) {
  v4u r;
  asm volatile("buffer_load_dwordx4 %0, %1, %2, %3 offen" : "=v"(r) : "v"(voff), "s"(rs), "s"(soff));
  return r;
}
DI v4u ld128_sc1(rsrc_t rs, unsigned voff, unsigned soff) {
  v4u r;
  asm volatile("buffer_load_dwordx4 %0, %1, %2, %3 offen sc1" : "=v"(r) : "v"(voff), "s"(rs), "s"(soff));
  return r;
}
DI v2u ld64(rsrc_t rs, unsigned voff, unsigned soff) {
  v2u r;
  asm volatile("buffer_load_dwordx2 %0, %1, %2, %3 offen" : "=v"(r) : "v"(voff), "s"(rs), "s"(soff));
  return r;
}
#define WAITV(n) asm volatile("s_waitcnt vmcnt(" #n ")" ::: "memory")
#define BAR() asm volatile("s_waitcnt lgkmcnt(0)\n\ts_barrier" ::: "memory")
#define WAITV_BAR(n) asm volatile("s_waitcnt vmcnt(" #n ") lgkmcnt(0)\n\ts_barrier" ::: "memory")
#define TIE(x) asm volatile("" : "+v"(x))

DI s16x4 trread(LAS unsigned char* L, unsigned off) { return __builtin_amdgcn_ds_read_tr16_b64_v4i16((LAS s16x4*)(L + off)); }
DI s16x8 cat(s16x4 a, s16x4 b) { return __builtin_shufflevector(a, b, 0, 1, 2, 3, 4, 5, 6, 7); }
DI s16x8 asfrag(v4u v) { return __builtin_bit_cast(s16x8, v); }
DI f32x4 mfma16(s16x8 a, s16x8 b, f32x4 c) { return __builtin_amdgcn_mfma_f32_16x16x32_bf16(a, b, c, 0, 0, 0); }
DI f32x4 mfma16k16(s16x4 a, s16x4 b, f32x4 c) { return __builtin_amdgcn_mfma_f32_16x16x16bf16_1k(a, b, c, 0, 0, 0); }
DI f32x16 mfma32(s16x8 a, s16x8 b, f32x16 c) { return __builtin_amdgcn_mfma_f32_32x32x16_bf16(a, b, c, 0, 0, 0); }
DI unsigned pk2(float lo, float hi) {
  f32x2 v = {lo, hi};
  return __builtin_bit_cast(unsigned, __builtin_convertvector(v, bf16x2));
}
DI float dot2(unsigned a, unsigned b, float c) {
  return __builtin_amdgcn_fdot2_f32_bf16(__builtin_bit_cast(bf16x2, a), __builtin_bit_cast(bf16x2, b), c, false);
}
DI float bflo(unsigned u) { return __builtin_bit_cast(float, u << 16); }
DI float bfhi(unsigned u) { return __builtin_bit_cast(float, u & 0xffff0000u); }
DI unsigned fu(float f) { return __builtin_bit_cast(unsigned, f); }
DI float uf(unsigned u) { return __builtin_bit_cast(float, u); }
DI void swap16(float& a, float& b) {
  auto r = __builtin_amdgcn_permlane16_swap(fu(a), fu(b), false, false);
  a = uf(r[0]);
  b = uf(r[1]);
}
DI void swap32(float& a, float& b) {
  auto r = __builtin_amdgcn_permlane32_swap(fu(a), fu(b), false, false);
  a = uf(r[0]);
  b = uf(r[1]);
}
DI void swap16u(unsigned& a, unsigned& b) {
  auto r = __builtin_amdgcn_permlane16_swap(a, b, false, false);
  a = r[0];
  b = r[1];
}
DI void swap32u(unsigned& a, unsigned& b) {
  auto r = __builtin_amdgcn_permlane32_swap(a, b, false, false);
  a = r[0];
  b = r[1];
}
DI void red2add(float& a, float& b) {
  swap16(a, b);
  float m = a + b, m2 = m;
  swap32(m, m2);
  float t = m + m2, t2 = t;
  swap16(t, t2);
  a = t;
  b = t2;
}
template <bool MAX> DI float rop(float a, float b) { return MAX ? fmaxf(a, b) : a + b; }
template <bool MAX> DI void red4(float& a, float& b, float& c, float& d) {
  swap16(a, b);
  float m1 = rop<MAX>(a, b);
  swap16(c, d);
  float m2 = rop<MAX>(c, d);
  swap32(m1, m2);
  float t = rop<MAX>(m1, m2), t2 = t;
  swap32(t, t2);
  float y1 = t, y1b = t, y2 = t2, y2b = t2;
  swap16(y1, y1b);
  swap16(y2, y2b);
  a = y1;
  b = y1b;
  c = y2;
  d = y2b;
}
DI void dual_softmax(f32x4 p0, f32x4 p1, v2u& pos0, v2u& neg0, v2u& pos1, v2u& neg1) {
  float mx0 = fmaxf(fmaxf(p0[0], p0[1]), fmaxf(p0[2], p0[3]));
  float mn0 = -fminf(fminf(p0[0], p0[1]), fminf(p0[2], p0[3]));
  float mx1 = fmaxf(fmaxf(p1[0], p1[1]), fmaxf(p1[2], p1[3]));
  float mn1 = -fminf(fminf(p1[0], p1[1]), fminf(p1[2], p1[3]));
  red4<true>(mx0, mn0, mx1, mn1);
  const float a0 = -mx0 * LOG2E, b0 = -mn0 * LOG2E, a1 = -mx1 * LOG2E, b1 = -mn1 * LOG2E;
  float ep0[4], en0[4], ep1[4], en1[4];
#pragma unroll
  for (int j = 0; j < 4; ++j) {
    ep0[j] = __builtin_amdgcn_exp2f(fmaf(p0[j], LOG2E, a0));
    en0[j] = __builtin_amdgcn_exp2f(fmaf(p0[j], -LOG2E, b0));
    ep1[j] = __builtin_amdgcn_exp2f(fmaf(p1[j], LOG2E, a1));
    en1[j] = __builtin_amdgcn_exp2f(fmaf(p1[j], -LOG2E, b1));
  }
  float sp0 = (ep0[0] + ep0[1]) + (ep0[2] + ep0[3]), sn0 = (en0[0] + en0[1]) + (en0[2] + en0[3]);
  float sp1 = (ep1[0] + ep1[1]) + (ep1[2] + ep1[3]), sn1 = (en1[0] + en1[1]) + (en1[2] + en1[3]);
  red4<false>(sp0, sn0, sp1, sn1);
  const float ip0 = __builtin_amdgcn_rcpf(sp0), in0 = __builtin_amdgcn_rcpf(sn0), ip1 = __builtin_amdgcn_rcpf(sp1), in1 = __builtin_amdgcn_rcpf(sn1);
  pos0 = (v2u){pk2(ep0[0] * ip0, ep0[1] * ip0), pk2(ep0[2] * ip0, ep0[3] * ip0)};
  neg0 = (v2u){pk2(en0[0] * in0, en0[1] * in0), pk2(en0[2] * in0, en0[3] * in0)};
  pos1 = (v2u){pk2(ep1[0] * ip1, ep1[1] * ip1), pk2(ep1[2] * ip1, ep1[3] * ip1)};
  neg1 = (v2u){pk2(en1[0] * in1, en1[1] * in1), pk2(en1[2] * in1, en1[3] * in1)};
}
DI u64 mix64(u64 z) {
  z += 0x9E3779B97F4A7C15ull;
  z = (z ^ (z >> 30)) * 0xBF58476D1CE4E5B9ull;
  z = (z ^ (z >> 27)) * 0x94D049BB133111EBull;
  return z ^ (z >> 31);
}
template <int N> DI void pollN(const unsigned char* base, u64 tag, int lane) {
  const GAS u64* fl = (const GAS u64*)base;
#pragma unroll 1
  for (unsigned it = 0; it < SPIN_CAP; ++it) {
    const u64 v = __hip_atomic_load(fl + (lane & (N - 1)), __ATOMIC_RELAXED, __HIP_MEMORY_SCOPE_AGENT);
    if (__all(v == tag)) break;
    __builtin_amdgcn_s_sleep(1);
  }
}
DI void set_flag(unsigned char* f, u64 tag) { __hip_atomic_store((GAS u64*)f, tag, __ATOMIC_RELAXED, __HIP_MEMORY_SCOPE_AGENT); }

DI v4u ld128_sc1o(rsrc_t rs, unsigned voff) {
  v4u r;
  asm volatile("buffer_load_dwordx4 %0, %1, %2, 0 offen sc1" : "=v"(r) : "v"(voff), "s"(rs));
  return r;
}
DI unsigned opaque(unsigned v) {
  asm volatile("" : "+v"(v));
  return v;
}
#define TIE4(a, b, c, d) asm volatile("" : "+v"(a), "+v"(b), "+v"(c), "+v"(d))
#define WAITV_TIE(n, x) asm volatile("s_waitcnt vmcnt(" #n ")" : "+v"(x))
template <int N> DI void waitv_tie(v4u& x) { asm volatile("s_waitcnt vmcnt(%1)" : "+v"(x) : "n"(N)); }
template <int N> DI void waitv() { asm volatile("s_waitcnt vmcnt(%0)" ::"n"(N) : "memory"); }

DI void lds_arrive(LAS unsigned char* c, int lane) {
  asm volatile("s_waitcnt lgkmcnt(0)" ::: "memory");
  if (lane == 0) __hip_atomic_fetch_add((LAS unsigned*)c, 1u, __ATOMIC_RELAXED, __HIP_MEMORY_SCOPE_WORKGROUP);
}
DI void lds_wait(LAS unsigned char* c, unsigned n) {
#pragma unroll 1
  for (unsigned it = 0; it < SPIN_CAP; ++it) {
    const unsigned v = __hip_atomic_load((LAS unsigned*)c, __ATOMIC_RELAXED, __HIP_MEMORY_SCOPE_WORKGROUP);
    if (__builtin_amdgcn_readfirstlane(v) >= n) break;
  }
  asm volatile("" ::: "memory");
}

DI void lds_wait_sleep(LAS unsigned char* c, unsigned n) {
#pragma unroll 1
  for (unsigned it = 0; it < SPIN_CAP; ++it) {
    const unsigned v = __hip_atomic_load((LAS unsigned*)c, __ATOMIC_RELAXED, __HIP_MEMORY_SCOPE_WORKGROUP);
    if (__builtin_amdgcn_readfirstlane(v) >= n) break;
    __builtin_amdgcn_s_sleep(2);
  }
  asm volatile("" ::: "memory");
}

#define STAMPW(wv, i) \
  do {                \
  } while (0)
#define STAMP_FLUSH() \
  do {                \
  } while (0)
#define STAMP(i) STAMPW(0, i)
#define CSTAMP(i) do {} while (0)

constexpr int DN_RING = 20, DN_PRE = 6;
constexpr unsigned dn_base(int i) {
  return i < 6 ? L_GW0 : i < 7 ? L_SPARE : i < 13 ? L_GW1 : i < 17 ? L_GH2 : i < 23 ? L_GW2 : i < 27 ? L_GH0 : i < 33 ? L_GW3 : i < 37 ? L_GH1 : L_GW0;
}
constexpr int dn_i0(int i) { return i < 6 ? 0 : i < 7 ? 6 : i < 13 ? 7 : i < 17 ? 13 : i < 23 ? 17 : i < 27 ? 23 : i < 33 ? 27 : i < 37 ? 33 : 37; }
constexpr int dn_n(int i) { return i < 6 ? 6 : i < 7 ? 1 : i < 13 ? 6 : i < 17 ? 4 : i < 23 ? 6 : i < 27 ? 4 : i < 33 ? 6 : i < 37 ? 4 : 6; }

template <int I> struct DnStep {
  static DI void run(v4u (&hf)[DN_RING], s16x8 (&wpre)[DN_PRE][2], s16x8 (&fw)[2][2], f32x16& a0, f32x16& a1, unsigned rel0, unsigned rel1,
                     unsigned kh2048, unsigned b0, unsigned b1, rsrc_t rsWs, unsigned hv) {
    LAS unsigned char* const L0 = (LAS unsigned char*)0;
    if constexpr (I + 1 < 43 && I + 1 >= DN_PRE) {
      constexpr int J = I + 1;
      if constexpr (dn_i0(J) == J) {
        b0 = opaque(rel0 + dn_base(J) + dn_n(J) * kh2048);
        b1 = opaque(rel1 + dn_base(J) + dn_n(J) * kh2048);
      }
      if constexpr (J == 37) asm volatile("s_barrier" ::: "memory");
      constexpr unsigned o = (J - dn_i0(J)) * 2048;
      fw[J & 1][0] = cat(trread(L0, b0 + o), trread(L0, b0 + o + 1024));
      SB();
    }
    constexpr int younger = (I + DN_RING - 1 < 42 ? I + DN_RING - 1 : 42) - I;
    waitv_tie<younger>(hf[I % DN_RING]);
    const s16x8 bf = asfrag(hf[I % DN_RING]);
    if constexpr (I < DN_PRE) a0 = mfma32(wpre[I][0], bf, a0);
    else a0 = mfma32(fw[I & 1][0], bf, a0);
    TIE(a0);
    SB();
    if constexpr (I + 1 < 43 && I + 1 >= DN_PRE) {
      constexpr int J = I + 1;
      constexpr unsigned o = (J - dn_i0(J)) * 2048;
      fw[J & 1][1] = cat(trread(L0, b1 + o), trread(L0, b1 + o + 1024));
      SB();
    }
    if constexpr (I < DN_PRE) a1 = mfma32(wpre[I][1], bf, a1);
    else a1 = mfma32(fw[I & 1][1], bf, a1);
    TIE(a1);
    SB();
    if constexpr (I + DN_RING < 43) hf[I % DN_RING] = ld128_sc1(rsWs, hv, (I + DN_RING) * 1024);
    SB();
    if constexpr (I + 1 < 43) DnStep<I + 1>::run(hf, wpre, fw, a0, a1, rel0, rel1, kh2048, b0, b1, rsWs, hv);
  }
};

__global__ void __launch_bounds__(512, 2) fused_block_kernel(Params p) {
  __shared__ __attribute__((aligned(16))) unsigned char smem[LDS_BYTES];
  LAS unsigned char* L = (LAS unsigned char*)smem;
  LAS unsigned char* const L0 = (LAS unsigned char*)0;
  const unsigned lb = (unsigned)(uintptr_t)L;
  const int tid = threadIdx.x, lane = tid & 63;
  const int w = __builtin_amdgcn_readfirstlane(tid >> 6);
  const int w4 = w & 3;
  const int b = blockIdx.x, xcd = b & 7, jj = b >> 3;
  const int rt = 4 * (xcd >> 1) + (jj >> 3), m = 8 * (xcd & 1) + (jj & 7);
  const int kro = (m + 2 * (rt & 3)) & 7;
  const int l15 = lane & 15, g = lane >> 4, q4 = l15 >> 2, p4 = lane & 3;
  const int hl = lane >> 5, t32 = lane & 31;
  const int hf = m >> 3, cg = m & 7;
  STAMP(0);

  const rsrc_t rsX = mkrsrc(p.x, 2048u * 1024u);
  const rsrc_t rsWs = mkrsrc(p.ws, WS_END);
  const s16x8 ones8 = {0x3F80, 0x3F80, 0x3F80, 0x3F80, 0x3F80, 0x3F80, 0x3F80, 0x3F80};
  v2u ep;
  asm volatile("global_load_dwordx2 %0, %1, off sc1" : "=v"(ep) : "v"(p.ws + WS_EPOCH));
  const unsigned trW0 = 16384 + (8 * g + q4) * 64 + ((0 ^ (g & 1)) << 5) + 8 * p4;
  const unsigned trW1 = 16384 + (8 * g + q4) * 64 + ((1 ^ (g & 1)) << 5) + 8 * p4;
  const unsigned voffL = lane * 16;
  const unsigned voffR = (lane >> 3) * 1024 + (((lane & 7) ^ (((lane >> 4) & 1) << 2)) * 16);

  if (__builtin_expect(w >= 4, 1)) {
    asm volatile(".Lfb_begin:");
    const rsrc_t rsWqkv = mkrsrc(p.wqkv, 512u * 3072u);
    const unsigned voffX0 = (128 * rt + 32 * w4 + (lane >> 3)) * 1024 + (((lane & 7) ^ (lane >> 4)) * 16);
    const unsigned voffX1 = (128 * rt + 32 * w4 + 8 + (lane >> 3)) * 1024 + (((lane & 7) ^ (4 + (lane >> 4))) * 16);
    const unsigned voffW = (lane >> 2) * 3072 + (((lane & 3) ^ ((lane >> 5) << 1)) * 16);
    const unsigned albase = lb + 4 * w4 * 1024, awbase = lb + 16384 + w4 * 1024, awsrc = 16 * w4 * 3072 + 1024 + 64 * m;
#define A_LOAD(st)                                                                                     \
  {                                                                                                    \
    const unsigned so_ = ((st) & 3) * A_SLOT, kb_ = (((st) + kro) & 7);                                \
    const unsigned lx_ = albase + so_, lw_ = awbase + so_, sx_ = kb_ * 128, sx2_ = sx_ + 16384, sw_ = awsrc + kb_ * (64 * 3072); \
    dma16o<0>(lx_, voffX0, rsX, sx_);                                                                  \
    dma16o<1024>(lx_, voffX1, rsX, sx_);                                                               \
    dma16o<0>(lw_, voffW, rsWqkv, sw_);                                                                \
    dma16o<2048>(lx_, voffX0, rsX, sx2_);                                                              \
    dma16o<3072>(lx_, voffX1, rsX, sx2_);                                                              \
    dma16o<4096>(lw_, voffW, rsWqkv, sw_ + 1024);                                                      \
  }
    if (w == 7) {
      const rsrc_t rsPhi = mkrsrc(p.wphi, 32u * 512u);
      dma16(lb + L_WPHI, voffL, rsPhi, m * 1024);
    }
    A_LOAD(0)
    A_LOAD(1)
    STAMPW(4, 35);
    WAITV_BAR(6);
    A_LOAD(2)
    A_LOAD(3)
#pragma unroll 1
    for (int st = 1; st < 8; ++st) {
      if (st <= 5) WAITV(12);
      else if (st == 6) WAITV(10);
      else WAITV(8);
      BAR();
      if (st <= 4) A_LOAD(st + 3)
      else if (st <= 6) {
#pragma unroll
        for (int j = 0; j < 4; ++j) {
          const unsigned r = 128 * w4 + 64 * (st - 5) + 16 * j;
          dma16(lb + L_WQ + r * 64, voffW, rsWqkv, ((r + 64 * kro) & 511) * 3072 + 64 * m);
        }
      }
    }
    asm volatile("s_waitcnt vmcnt(0) lgkmcnt(0)\n\ts_barrier" : "+v"(ep)::"memory");
    const u64 tag = mix64(((u64)(unsigned)__builtin_amdgcn_readfirstlane(ep[1]) << 32) | (unsigned)__builtin_amdgcn_readfirstlane(ep[0]));
    unsigned dummy = 0;
    {
      const unsigned idx = (b * 4 + w4) * 33 + (lane < 33 ? lane : 0);
      const unsigned char* q;
      unsigned o = idx;
      if (o < 11008) q = (const unsigned char*)p.wg;
      else if ((o -= 11008) < 11008) q = (const unsigned char*)p.wu;
      else { o -= 11008; q = (const unsigned char*)p.wd; if (o >= 11008) o = 0; }
      asm volatile("global_load_dword %0, %1, off" : "=v"(dummy) : "v"(q + (size_t)o * 128));
    }
    {
      const rsrc_t rsG = mkrsrc(p.wg, 512u * 2752u), rsU = mkrsrc(p.wu, 512u * 2752u);
      const unsigned vo = (lane >> 1) * 2752 + (lane & 1) * 16, so = 32 * rt * 2752 + 192 * m;
      const unsigned dst = lb + L_CSW + w4 * 4608;
      if (w4 < 2) {
        dma16(dst + 2304, vo, rsG, so + 32 * (w4 + 4));
        dma16(dst + 2304 + 1152, vo, rsU, so + 32 * (w4 + 4));
      }
      dma16(dst, vo, rsG, so + 32 * w4);
      dma16(dst + 1152, vo, rsU, so + 32 * w4);
      const rsrc_t rsWo = mkrsrc(p.wo, 512u * 1024u);
#pragma unroll 4
      for (int i = 0; i < 16; ++i) { dma16(lb + L_WO + (w4 * 16 + i) * 1024, voffR, rsWo, (w4 * 16 + i) * 8192 + 128 * cg); PACE(); }
    }
    {
      const unsigned cq = lb + L_WQ - 16384 + ((w4 & 1) ? trW1 : trW0) + (w4 >> 1) * 8 * 2048;
      f32x4 acq = {0, 0, 0, 0}, acq2 = acq;
#pragma unroll
      for (int s = 0; s < 8; s += 2) {
        acq = mfma16(cat(trread(L0, cq + s * 2048), trread(L0, cq + s * 2048 + 256)), ones8, acq);
        acq2 = mfma16(cat(trread(L0, cq + (s + 1) * 2048), trread(L0, cq + (s + 1) * 2048 + 256)), ones8, acq2);
      }
      if (l15 == 0) *(LAS f32x4*)(L + L_CS + 256 + (w4 & 1) * 128 + (w4 >> 1) * 64 + g * 16) = acq + acq2;
      lds_arrive(L + L_CNT + 4, lane);
    }
    asm volatile("s_waitcnt vmcnt(16)" : "+v"(dummy)::"memory");
#pragma unroll 1
    for (int bl = w4, k = 0; bl < 6; bl += 4, ++k) {
      const unsigned a = L_CSW + w4 * 4608 + k * 2304 + (p4 >> 1) * 1152 + (4 * (g >> 1) + q4) * 32 + 16 * (g & 1) + 8 * (p4 & 1);
      f32x16 c;
#pragma unroll
      for (int i = 0; i < 16; ++i) c[i] = 0.f;
      c = mfma32(cat(trread(L, a), trread(L, a + 8 * 32)), ones8, c);
      c = mfma32(cat(trread(L, a + 16 * 32), trread(L, a + 24 * 32)), ones8, c);
      const unsigned off = t32 == 0 ? WS_CSP + ((m * 16 + rt) * 6 + bl) * 128 + hl * 16 : 0x7FFFFF00u;
#pragma unroll
      for (int i = 0; i < 4; ++i)
        __builtin_amdgcn_raw_buffer_store_b128(__builtin_bit_cast(v4u, (f32x4){c[4 * i], c[4 * i + 1], c[4 * i + 2], c[4 * i + 3]}), rsWs, off, i * 32, 16);
    }
    lds_arrive(L + L_CNT + 12, lane);
    WAITV(0);
    if (lane == 0) set_flag(p.ws + WS_F5 + m * 512 + (rt * 4 + w4) * 8, tag);

    const rsrc_t rsGU = mkrsrc((w4 >> 1) ? p.wu : p.wg, 512u * 2752u);
    unsigned voffG[6];
#pragma unroll
    for (int j = 0; j < 6; ++j) {
      const unsigned u = 64 * (6 * (w4 & 1) + j) + lane, row = u / 12, c = u - row * 12;
      voffG[j] = row * 2752 + c * 16;
    }
    const unsigned gdst = (w4 >> 1) * GU_UPOFF + 6 * (w4 & 1) * 1024;
    const unsigned gsrc = 192 * m;
#define GU_LOADW(st, slot)                                                                       \
  {                                                                                              \
    const unsigned l_ = lb + (slot) + gdst, s_ = gsrc + (((st) + kro) & 7) * 64 * 2752;          \
    dma16o<0>(l_, voffG[0], rsGU, s_); dma16o<1024>(l_, voffG[1], rsGU, s_); dma16o<2048>(l_, voffG[2], rsGU, s_); \
    dma16o<3072>(l_, voffG[3], rsGU, s_); dma16o<4096>(l_, voffG[4], rsGU, s_); dma16o<5120>(l_, voffG[5], rsGU, s_); \
  }
#define GU_LOADW_PACED(st, slot)                                                                 \
  {                                                                                              \
    _Pragma("unroll") for (int j = 0; j < 6; ++j) { dma16(lb + (slot) + gdst + j * 1024, voffG[j], rsGU, gsrc + (((st) + kro) & 7) * 64 * 2752); PACE2(); } \
  }
    lds_wait(L + L_CNT + 8, 4);
    GU_LOADW_PACED(0, L_GW0)
    lds_wait(L + L_CNT + 12, 8);
    GU_LOADW_PACED(1, L_GW1)
    GU_LOADW_PACED(2, L_GW2)
    STAMPW(4, 30);
    BAR();
    STAMPW(4, 28);
    pollN<64>(p.ws + WS_F5 + m * 512, tag, lane);
    if (tid == 256) *(LAS v4u*)(L + L_ZERO) = (v4u){0, 0, 0, 0};
#pragma unroll 1
    for (int bl = w4; bl < 6; bl += 4) {
      float s = 0.f;
      unsigned cv[8];
#pragma unroll
      for (int j = 0; j < 8; ++j) cv[j] = __builtin_amdgcn_raw_buffer_load_b32(rsWs, WS_CSP + ((m * 16 + 8 * hl) * 6 + bl) * 128 + t32 * 4, j * 768, 16);
#pragma unroll
      for (int j = 0; j < 8; ++j) s += uf(cv[j]);
      float s2 = s;
      swap32(s, s2);
      s += s2;
      const unsigned hi = pk2(s, 0.f) & 0xffffu, lo = pk2(s - uf(hi << 16), 0.f) & 0xffffu;
      if (lane < 32) *(LAS v4u*)(L + L_CSF + bl * 512 + lane * 16) = (v4u){hi | (hi << 16), lo, 0, 0};
    }
    const rsrc_t rsWd = mkrsrc(p.wd, 1376u * 1024u);
    const unsigned dsrc = (16 * 43 * (w4 >> 1) + 8 * (w4 & 1)) * 1024 + 128 * cg;
    v4u wreg[16];
    lds_wait_sleep(L + L_CNT + 16, 4);
#pragma unroll
    for (int j = 0; j < 16; ++j) wreg[j] = ld128(rsWd, voffR, dsrc + (27 + j) * 16384);
    BAR();
    BAR();
    GU_LOADW(3, L_GW3)
    pollN<16>(p.ws + WS_F3 + (rt * 4 + w4) * 128, tag, lane);
    STAMPW(4, 8);
    const unsigned hsrc = WS_H + (rt * 4 + w4) * 32 * 1024;
    const unsigned hdst = w4 * 4096;
    const unsigned voffH1 = voffL + 1024 - 0, voffH2 = voffL + 2048, voffH3 = voffL + 3072;
#define GU_LOADH(st, slot)                                           \
  {                                                                  \
    const unsigned l_ = lb + (slot) + hdst, s_ = hsrc + (((st) + kro) & 7) * 4096; \
    dma16o_sc1<0>(l_, voffL, rsWs, s_); dma16o_sc1<1024>(l_, voffH1, rsWs, s_);    \
    dma16o_sc1<2048>(l_, voffH2, rsWs, s_); dma16o_sc1<3072>(l_, voffH3, rsWs, s_); \
  }
    GU_LOADH(0, L_GH0)
    WAITV_BAR(0);
    GU_LOADH(1, L_GH1)
    GU_LOADH(2, L_GH2)
    WAITV_BAR(4);
    GU_LOADH(3, L_GH0)
    v4u sv[8];
    const unsigned svo = WS_ST + ((rt * 4 + w4) * 16 + 8 * hl) * 512 + t32 * 16;
#pragma unroll
    for (int i = 0; i < 8; ++i) sv[i] = ld128_sc1(rsWs, svo, i * 512);
    GU_LOADW(4, L_GW0)
    WAITV_BAR(18);
    GU_LOADH(4, L_GH1)
    GU_LOADW(5, L_GW1)
    asm volatile("s_waitcnt vmcnt(16)" : "+v"(sv[0]), "+v"(sv[1]), "+v"(sv[2]), "+v"(sv[3]), "+v"(sv[4]), "+v"(sv[5]), "+v"(sv[6]), "+v"(sv[7])::"memory");
    {
      bool ok = true;
#pragma unroll
      for (int i = 0; i < 8; ++i) ok &= (sv[i][1] == (unsigned)tag) & (sv[i][3] == (unsigned)tag);
#pragma unroll 1
      for (unsigned it = 0; !__all(ok) && it < SPIN_CAP; ++it) {
        ok = true;
#pragma unroll
        for (int i = 0; i < 8; ++i) {
          sv[i] = __builtin_amdgcn_raw_buffer_load_b128(rsWs, svo, i * 512, 16);
          ok &= (sv[i][1] == (unsigned)tag) & (sv[i][3] == (unsigned)tag);
        }
      }
      float s = 0.f, q = 0.f;
#pragma unroll
      for (int i = 0; i < 8; ++i) {
        s += uf(sv[i][0]);
        q += uf(sv[i][2]);
      }
      swap32(s, q);
      float t = s + q, t2 = t;
      swap32(t, t2);
      const float mean2 = t * (1.f / 512.f);
      const float var = fmaxf(t2 * (1.f / 512.f) - mean2 * mean2, 0.f) + 1e-5f;
      const float nm = -mean2;
      const unsigned hi = pk2(nm, 0.f) & 0xffffu, lo = pk2(nm - uf(hi << 16), 0.f) & 0xffffu;
      if (lane < 32) {
        *(LAS v4u*)(L + L_MF + w4 * 512 + lane * 16) = (v4u){hi | (lo << 16), hi, 0, 0};
        *(LAS float*)(L + L_R2 + (32 * w4 + lane) * 4) = __builtin_amdgcn_rcpf(var);
      }
    }
#define DN_LOAD(i0)                                                                                            \
  {                                                                                                            \
    _Pragma("unroll") for (int j = 0; j < dn_n(i0); ++j)                                                       \
        dma16(lb + dn_base(i0) + ((w4 >> 1) * dn_n(i0) + j) * 2048 + (w4 & 1) * 1024, voffR, rsWd, dsrc + ((i0) + j) * 16384); \
  }
    WAITV_BAR(16);
    GU_LOADH(5, L_GH2)
    GU_LOADW(6, L_GW2)
    WAITV_BAR(16);
    GU_LOADH(6, L_GH0)
    GU_LOADW(7, L_GW3)
    WAITV_BAR(16);
    GU_LOADH(7, L_GH1)
    DN_LOAD(0) DN_LOAD(6)
    WAITV_BAR(17);
    DN_LOAD(7) DN_LOAD(13)
    WAITV_BAR(17);
    DN_LOAD(17) DN_LOAD(23)
#pragma unroll
    for (int j = 0; j < 16; ++j) TIE(wreg[j]);
#define DN_PUT(i0, j0)                                                                                         \
  {                                                                                                            \
    _Pragma("unroll") for (int j = 0; j < dn_n(i0); ++j)                                                       \
        *(LAS v4u*)(L + dn_base(i0) + ((w4 >> 1) * dn_n(i0) + j) * 2048 + (w4 & 1) * 1024 + lane * 16) = wreg[(j0) + j]; \
  }
    BAR();
    DN_PUT(27, 0) DN_PUT(33, 6)
    BAR();
    STAMPW(4, 31);
    WAITV_BAR(0);
    BAR();
    DN_PUT(37, 10)
    BAR();
    BAR();
  } else {
    if (tid < 4) *(LAS unsigned*)(L + L_CNT + tid * 4) = 0;
    if (tid == 4) *(LAS unsigned*)(L + L_CNT + 16) = 0;
    unsigned cdummy = 0;
    {
      u64 pc;
      unsigned back, len;
      asm volatile("s_getpc_b64 %0\n.Lfb_here:\n\ts_mov_b32 %1, .Lfb_here-.Lfb_begin\n\ts_mov_b32 %2, .Lfb_end-.Lfb_begin" : "=s"(pc), "=s"(back), "=s"(len));
      const unsigned o = (jj * 14 + (lane < 14 ? lane : 0)) * 128;
      const unsigned char* q = (const unsigned char*)pc - back + ((int)len > 0 && o + 128 <= len ? o : back);
      asm volatile("global_load_dword %0, %1, off" : "=v"(cdummy) : "v"(q));
    }
    v4u xf[16][2];
    f32x4 ak[4][2];
    const f32x4 z4 = {0, 0, 0, 0};
    f32x2 stt[2];
#pragma unroll
    for (int j = 0; j < 4; ++j) ak[j][0] = ak[j][1] = (f32x4){0, 0, 0, 0};
    {
      const unsigned xr0 = lb + (32 * w4 + l15) * 128;
      const unsigned xlo0 = opaque(xr0 + ((g ^ (l15 >> 1)) << 4)), xlo1 = opaque(xr0 + (((4 + g) ^ (l15 >> 1)) << 4));
      const unsigned xhi0 = opaque(xlo0 + 2 * A_SLOT), xhi1 = opaque(xlo1 + 2 * A_SLOT);
      const unsigned w0lo = opaque(lb + trW0), w1lo = opaque(lb + trW1), w0hi = opaque(w0lo + 2 * A_SLOT), w1hi = opaque(w1lo + 2 * A_SLOT);
      const unsigned clo = opaque(((w4 & 1) ? w1lo : w0lo) + (w4 >> 1) * 4096), chi = opaque(clo + 2 * A_SLOT);
      s16x8 fa[2][4], fc[2];
      f32x4 asum[2] = {z4, z4}, agr[2] = {z4, z4}, acs = z4;
#define A1_X(s, a) xf[s][a] = *(LAS v4u*)(L0 + ((((s) >> 1) & 2) ? (((s) & 1) ? xhi1 : xhi0) : (((s) & 1) ? xlo1 : xlo0)) + (((s) >> 1) & 1) * A_SLOT + (a) * 2048); SB()
#define A1_WA(s, j) (((((s) >> 1) & 2) ? (((j) & 1) ? w1hi : w0hi) : (((j) & 1) ? w1lo : w0lo)) + (((s) >> 1) & 1) * A_SLOT + ((j) >> 1) * 4096 + ((s) & 1) * 2048)
#define A1_W(s, j) fa[(s) & 1][j] = cat(trread(L0, A1_WA(s, j)), trread(L0, A1_WA(s, j) + 256)); SB()
#define A1_CA(s) (((((s) >> 1) & 2) ? chi : clo) + (((s) >> 1) & 1) * A_SLOT + ((s) & 1) * 2048)
#define A1_C(s) fc[(s) & 1] = cat(trread(L0, A1_CA(s)), trread(L0, A1_CA(s) + 256)); SB()
#define A1_M(s, j, a)                                           \
  ak[j][a] = mfma16(fa[(s) & 1][j], asfrag(xf[s][a]), ak[j][a]); \
  TIE(ak[j][a]);                                                \
  SB()
#define A1_S(s, a)                                               \
  asum[a] = mfma16(ones8, asfrag(xf[s][a]), asum[a]);            \
  TIE(asum[a]);                                                  \
  agr[a] = mfma16(asfrag(xf[s][a]), asfrag(xf[s][a]), agr[a]);   \
  TIE(agr[a]);                                                   \
  SB()
#define A1_CM(s)                              \
  acs = mfma16(fc[(s) & 1], ones8, acs);      \
  TIE(acs);                                   \
  SB()
#define A1_ALLM(s)                                                            \
  A1_M(s, 0, 0); A1_M(s, 0, 1); A1_M(s, 1, 0); A1_M(s, 1, 1); A1_M(s, 2, 0); A1_M(s, 2, 1); A1_M(s, 3, 0); A1_M(s, 3, 1); \
  A1_S(s, 0); A1_S(s, 1); A1_CM(s);
#define A1_RM(sn, sc)                                                         \
  A1_X(sn, 0); A1_M(sc, 0, 0); A1_W(sn, 0); A1_M(sc, 0, 1);                   \
  A1_X(sn, 1); A1_M(sc, 1, 0); A1_W(sn, 1); A1_M(sc, 1, 1);                   \
  A1_W(sn, 2); A1_M(sc, 2, 0); A1_M(sc, 2, 1);                                \
  A1_W(sn, 3); A1_M(sc, 3, 0); A1_M(sc, 3, 1);                                \
  A1_C(sn); A1_S(sc, 0); A1_S(sc, 1); A1_CM(sc);                              \
  SB();
#define A1_STAGE(st) BAR(); A1_RM(2 * (st), 2 * (st) - 1) A1_RM(2 * (st) + 1, 2 * (st))
      BAR();
      STAMP(14); CSTAMP(52);
      A1_X(0, 0); A1_X(0, 1); A1_W(0, 0); A1_W(0, 1); A1_W(0, 2); A1_W(0, 3); A1_C(0);
      SB();
      A1_RM(1, 0)
      A1_STAGE(1)
      A1_STAGE(2) A1_STAGE(3) A1_STAGE(4) A1_STAGE(5) A1_STAGE(6) A1_STAGE(7)
      A1_ALLM(15)
      if (l15 == 0) *(LAS f32x4*)(L + L_CS + w4 * 64 + g * 16) = acs;
#pragma unroll
      for (int a = 0; a < 2; ++a) {
        const float a01 = (p4 & 1) ? agr[a][1] : agr[a][0], a23 = (p4 & 1) ? agr[a][3] : agr[a][2];
        const float d = (p4 & 2) ? a23 : a01;
        stt[a][1] = (g == q4) ? d : 0.f;
      }
      float d0 = stt[0][1], d1 = stt[1][1];
      red2add(d0, d1);
      const float mean0 = asum[0][0] * (1.f / 512.f), mean1 = asum[1][0] * (1.f / 512.f);
      const float r0 = __builtin_amdgcn_rsqf(fmaxf(d0 * (1.f / 512.f) - mean0 * mean0, 0.f) + 1e-5f);
      const float r1 = __builtin_amdgcn_rsqf(fmaxf(d1 * (1.f / 512.f) - mean1 * mean1, 0.f) + 1e-5f);
      stt[0] = (f32x2){r0, -mean0 * r0};
      stt[1] = (f32x2){r1, -mean1 * r1};
    }
    STAMP(1); CSTAMP(53);
    asm volatile("s_waitcnt lgkmcnt(0)\n\ts_barrier" : "+v"(ep)::"memory");
    STAMP(26); CSTAMP(54);
    asm volatile("s_waitcnt vmcnt(0)" : "+v"(ep), "+v"(cdummy));
    const u64 tag = mix64(((u64)(unsigned)__builtin_amdgcn_readfirstlane(ep[1]) << 32) | (unsigned)__builtin_amdgcn_readfirstlane(ep[0]));

    const s16x4 wphi0 = trread(L, L_WPHI + (4 * g + q4) * 32 + 8 * p4);
    const s16x4 wphi1 = trread(L, L_WPHI + 512 + (4 * g + q4) * 32 + 8 * p4);
    {
      f32x4 cs[4];
#pragma unroll
      for (int i = 0; i < 4; ++i) cs[i] = *(LAS f32x4*)(L + L_CS + i * 64 + g * 16);
#pragma unroll
      for (int a = 0; a < 2; ++a) {
        const float rstd = stt[a][0], nb = stt[a][1];
        const f32x4 k0 = ak[0][a] * rstd + cs[0] * nb, k1 = ak[1][a] * rstd + cs[1] * nb;
        const f32x4 v0 = ak[2][a] * rstd + cs[2] * nb, v1 = ak[3][a] * rstd + cs[3] * nb;
        const v2u k0b = {pk2(k0[0], k0[1]), pk2(k0[2], k0[3])}, k1b = {pk2(k1[0], k1[1]), pk2(k1[2], k1[3])};
        const f32x4 p0 = mfma16k16(wphi0, __builtin_bit_cast(s16x4, k0b), z4);
        const f32x4 p1 = mfma16k16(wphi1, __builtin_bit_cast(s16x4, k1b), z4);
        const unsigned wa = L_KVIMG + (32 * w4 + 16 * a + l15) * 32 + 8 * (g ^ q4);
        *(LAS v2u*)(L + wa + 4 * 4096) = (v2u){pk2(v0[0], v0[1]), pk2(v0[2], v0[3])};
        *(LAS v2u*)(L + wa + 5 * 4096) = (v2u){pk2(v1[0], v1[1]), pk2(v1[2], v1[3])};
        v2u pos0, neg0, pos1, neg1;
        dual_softmax(p0, p1, pos0, neg0, pos1, neg1);
        *(LAS v2u*)(L + wa + 0 * 4096) = pos0;
        *(LAS v2u*)(L + wa + 1 * 4096) = neg0;
        *(LAS v2u*)(L + wa + 2 * 4096) = pos1;
        *(LAS v2u*)(L + wa + 3 * 4096) = neg1;
      }
    }
    lds_arrive(L + L_CNT, lane);
    lds_wait(L + L_CNT, 4);
    STAMP(15); CSTAMP(55);
    {
      const unsigned ra = L_KVIMG + (4 * g + q4) * 32 + 8 * (p4 ^ g);
      const unsigned raA = ra + w4 * 4096, raB = ra + (4 + (w4 >> 1)) * 4096;
      f32x4 acc = z4, accz = z4;
      s16x8 fA[4], fB[4];
#pragma unroll
      for (int u = 0; u < 4; ++u) {
        fA[u] = cat(trread(L, raA + u * 1024), trread(L, raA + u * 1024 + 512));
        fB[u] = cat(trread(L, raB + u * 1024), trread(L, raB + u * 1024 + 512));
      }
#pragma unroll
      for (int u = 0; u < 4; ++u) { TIE(fA[u]); TIE(fB[u]); }
      lds_arrive(L + L_CNT + 8, lane);
#pragma unroll
      for (int u = 0; u < 4; ++u) {
        acc = mfma16(fA[u], fB[u], acc);
        accz = mfma16(fA[u], ones8, accz);
      }
      const unsigned base = WS_KVP + (m * 16 + rt) * KVP_STRIDE;
      __builtin_amdgcn_raw_buffer_store_b128(__builtin_bit_cast(v4u, acc), rsWs, base + w4 * 1024 + lane * 16, 0, 16);
      __builtin_amdgcn_raw_buffer_store_b128(__builtin_bit_cast(v4u, accz), rsWs, l15 == 0 ? base + 4096 + w4 * 64 + g * 16 : 0x7FFFFFF0u, 0, 16);
      WAITV(0);
      if (lane == 0) set_flag(p.ws + WS_F1 + m * 512 + (rt * 4 + w4) * 8, tag);
    }
    STAMP(2);

    v4u phq[2][2];
    {
      f32x4 aq[2][2] = {{z4, z4}, {z4, z4}};
      const unsigned trQ0 = opaque(lb + L_WQ - 16384 + trW0), trQ1 = opaque(lb + L_WQ - 16384 + trW1);
      s16x8 fq[2][2];
#define A2_W(s, j) fq[(s) & 1][j] = cat(trread(L0, ((j) ? trQ1 : trQ0) + (s) * 2048), trread(L0, ((j) ? trQ1 : trQ0) + (s) * 2048 + 256)); SB()
#define A2_M(s, j, a)                                           \
  aq[j][a] = mfma16(fq[(s) & 1][j], asfrag(xf[s][a]), aq[j][a]); \
  TIE(aq[j][a]);                                                \
  SB()
#define A2_STEP(s)                                \
  if ((s) < 15) { A2_W(((s) + 1) & 15, 0); }      \
  A2_M(s, 0, 0); A2_M(s, 0, 1);                   \
  if ((s) < 15) { A2_W(((s) + 1) & 15, 1); }      \
  A2_M(s, 1, 0); A2_M(s, 1, 1);                   \
  SB();
      A2_W(0, 0); A2_W(0, 1);
      SB();
      A2_STEP(0) A2_STEP(1) A2_STEP(2) A2_STEP(3) A2_STEP(4) A2_STEP(5) A2_STEP(6) A2_STEP(7)
      A2_STEP(8) A2_STEP(9) A2_STEP(10) A2_STEP(11) A2_STEP(12) A2_STEP(13) A2_STEP(14) A2_STEP(15)
      lds_arrive(L + L_CNT + 12, lane);
      STAMP(27);
      lds_wait(L + L_CNT + 4, 4);
      const f32x4 c0 = *(LAS f32x4*)(L + L_CS + 256 + g * 16) + *(LAS f32x4*)(L + L_CS + 320 + g * 16);
      const f32x4 c1 = *(LAS f32x4*)(L + L_CS + 384 + g * 16) + *(LAS f32x4*)(L + L_CS + 448 + g * 16);
#pragma unroll
      for (int a = 0; a < 2; ++a) {
        const f32x4 q0 = aq[0][a] * stt[a][0] + c0 * stt[a][1], q1 = aq[1][a] * stt[a][0] + c1 * stt[a][1];
        const v2u q0b = {pk2(q0[0], q0[1]), pk2(q0[2], q0[3])}, q1b = {pk2(q1[0], q1[1]), pk2(q1[2], q1[3])};
        const f32x4 p0 = mfma16k16(wphi0, __builtin_bit_cast(s16x4, q0b), z4);
        const f32x4 p1 = mfma16k16(wphi1, __builtin_bit_cast(s16x4, q1b), z4);
        v2u pos0, neg0, pos1, neg1;
        dual_softmax(p0, p1, pos0, neg0, pos1, neg1);
        phq[a][0] = (v4u){pos0[0], pos0[1], neg0[0], neg0[1]};
        phq[a][1] = (v4u){pos1[0], pos1[1], neg1[0], neg1[1]};
      }
    }
    STAMP(3);

    {
      pollN<64>(p.ws + WS_F1 + m * 512, tag, lane);
      STAMP(4);
      const unsigned base = WS_KVP + m * 16 * KVP_STRIDE;
      v4u pv[16], zv[4];
#pragma unroll
      for (int i = 0; i < 4; ++i) zv[i] = __builtin_amdgcn_raw_buffer_load_b128(rsWs, base + g * KVP_STRIDE + 4096 + l15 * 16, 4 * i * KVP_STRIDE, 16);
#pragma unroll
      for (int i = 0; i < 16; ++i) pv[i] = __builtin_amdgcn_raw_buffer_load_b128(rsWs, base + tid * 16, i * KVP_STRIDE, 16);
      f32x4 sz = __builtin_bit_cast(f32x4, zv[0]);
#pragma unroll
      for (int i = 1; i < 4; ++i) sz += __builtin_bit_cast(f32x4, zv[i]);
      float z0 = sz[0], z1 = sz[1], z2 = sz[2], z3 = sz[3];
      red4<false>(z0, z1, z2, z3);
      if (tid < 16) *(LAS v2u*)(L + L_ZF + (l15 >> 3) * 64 + (l15 & 3) * 16 + ((l15 >> 2) & 1) * 8) = (v2u){pk2(z0, z1), pk2(z2, z3)};
      f32x4 s = __builtin_bit_cast(f32x4, pv[0]);
#pragma unroll
      for (int i = 1; i < 16; ++i) s += __builtin_bit_cast(f32x4, pv[i]);
      *(LAS v2u*)(L + L_KF + (w >> 1) * 1024 + lane * 16 + (w & 1) * 8) = (v2u){pk2(s[0], s[1]), pk2(s[2], s[3])};
    }
    STAMP(33);
    BAR();
    {
      const s16x8 kf0 = *(LAS s16x8*)(L + L_KF + lane * 16), kf1 = *(LAS s16x8*)(L + L_KF + 1024 + lane * 16);
      const s16x8 zf0 = *(LAS s16x8*)(L + L_ZF + g * 16), zf1 = *(LAS s16x8*)(L + L_ZF + 64 + g * 16);
#pragma unroll
      for (int a = 0; a < 2; ++a) {
        const f32x4 n0 = mfma16(kf0, asfrag(phq[a][0]), z4), d0 = mfma16(zf0, asfrag(phq[a][0]), z4);
        const f32x4 n1 = mfma16(kf1, asfrag(phq[a][1]), z4), d1 = mfma16(zf1, asfrag(phq[a][1]), z4);
        const float i0 = __builtin_amdgcn_rcpf(d0[0] + 1e-6f), i1 = __builtin_amdgcn_rcpf(d1[0] + 1e-6f);
        const v4u ov = {pk2(n0[0] * i0, n0[1] * i0), pk2(n0[2] * i0, n0[3] * i0), pk2(n1[0] * i1, n1[1] * i1), pk2(n1[2] * i1, n1[3] * i1)};
        __builtin_amdgcn_raw_buffer_store_b128(ov, rsWs, WS_O + ((rt * 4 + w4) * 32 + 2 * m + (g >> 1)) * 1024 + ((g & 1) * 32 + 16 * a + l15) * 16, 0, 16);
      }
      WAITV(0);
      if (lane < 2) set_flag(p.ws + WS_F2 + ((rt * 4 + w4) * 2 + hf) * 128 + (lane * 8 + cg) * 8, tag);
    }
    STAMP(5);

    const int tb32 = 2 * hf + (w & 1), kh = (w >> 1) & 1;
    unsigned hres[8];
    const unsigned tok = 128 * rt + 32 * tb32 + t32;
    v2u xr[4];
#pragma unroll
    for (int i = 0; i < 4; ++i) xr[i] = ld64(rsX, tok * 1024 + (64 * cg + 32 * kh + 4 * hl) * 2, i * 16);
    const unsigned relR = (4 * (g >> 1) + q4) * 128 + (g & 1) * 32 + 8 * p4;
    const unsigned rel0 = lb + relR + ((0 ^ (q4 >> 1)) << 6), rel1 = lb + relR + ((1 ^ (q4 >> 1)) << 6);
    f32x16 a0, a1;
#pragma unroll
    for (int i = 0; i < 16; ++i) { a0[i] = 0.f; a1[i] = 0.f; }
    {
      const unsigned rl0 = opaque(rel0 + kh * 32768), rl1 = opaque(rel1 + kh * 32768);
      s16x8 fw[2][2];
#define WO_RD1(i, c)                                                                                    \
  {                                                                                                     \
    constexpr unsigned o_ = (32 * ((i) >> 1) + 8 * ((i) & 1)) * 128;                                    \
    fw[(i) & 1][c] = cat(trread(L0, ((c) ? rl1 : rl0) + o_), trread(L0, ((c) ? rl1 : rl0) + o_ + 2048)); \
    SB();                                                                                               \
  }
#define WO_RD(i) WO_RD1(i, 0) WO_RD1(i, 1)
      WO_RD(0)
      pollN<16>(p.ws + WS_F2 + ((rt * 4 + tb32) * 2 + kh) * 128, tag, lane);
      STAMP(6);
      v4u of[16];
      const unsigned ov = WS_O + ((rt * 4 + tb32) * 32 + 16 * kh) * 1024 + lane * 16;
#pragma unroll
      for (int i = 0; i < 16; ++i) of[i] = ld128_sc1(rsWs, ov, i * 1024);
      lds_arrive(L + L_CNT + 16, lane);
#define WO_STEP(i)                                          \
  {                                                         \
    if ((i) < 15) WO_RD1(((i) + 1) & 15, 0)                 \
    waitv_tie<15 - (i)>(of[i]);                             \
    a0 = mfma32(fw[(i) & 1][0], asfrag(of[i]), a0);         \
    TIE(a0);                                                \
    SB();                                                   \
    if ((i) < 15) WO_RD1(((i) + 1) & 15, 1)                 \
    a1 = mfma32(fw[(i) & 1][1], asfrag(of[i]), a1);         \
    TIE(a1);                                                \
    SB();                                                   \
  }
      WO_STEP(0) WO_STEP(1) WO_STEP(2) WO_STEP(3) WO_STEP(4) WO_STEP(5) WO_STEP(6) WO_STEP(7)
      WO_STEP(8) WO_STEP(9) WO_STEP(10) WO_STEP(11) WO_STEP(12) WO_STEP(13) WO_STEP(14) WO_STEP(15)
    }
    STAMP(16);
    {
      const f32x16 give = kh ? a0 : a1;
      f32x16 keep = kh ? a1 : a0;
#pragma unroll
      for (int i = 0; i < 4; ++i)
        *(LAS f32x4*)(L + L_RED_WO + (w * 4 + i) * 1024 + lane * 16) = (f32x4){give[4 * i], give[4 * i + 1], give[4 * i + 2], give[4 * i + 3]};
      BAR();
      float ss = 0.f, sq = 0.f;
#pragma unroll
      for (int i = 0; i < 4; ++i) {
        const f32x4 o = *(LAS f32x4*)(L + L_RED_WO + ((w ^ 2) * 4 + i) * 1024 + lane * 16);
        TIE(xr[i]);
        hres[2 * i] = pk2(keep[4 * i] + o[0] + bflo(xr[i][0]), keep[4 * i + 1] + o[1] + bfhi(xr[i][0]));
        hres[2 * i + 1] = pk2(keep[4 * i + 2] + o[2] + bflo(xr[i][1]), keep[4 * i + 3] + o[3] + bfhi(xr[i][1]));
      }
      const int Sg = 2 * cg + kh;
      const unsigned ho = WS_H + ((rt * 4 + tb32) * 32 + 2 * Sg) * 1024 + lane * 16;
      __builtin_amdgcn_raw_buffer_store_b128((v4u){hres[0], hres[1], hres[2], hres[3]}, rsWs, ho, 0, 16);
      __builtin_amdgcn_raw_buffer_store_b128((v4u){hres[4], hres[5], hres[6], hres[7]}, rsWs, ho + 1024, 0, 16);
      WAITV(0);
      if (lane == 0) set_flag(p.ws + WS_F3 + (rt * 4 + tb32) * 128 + Sg * 8, tag);
      STAMP(36);
#pragma unroll
      for (int i = 0; i < 8; ++i) {
        const float lo = bflo(hres[i]), hi = bfhi(hres[i]);
        ss += lo + hi;
        sq = fmaf(lo, lo, fmaf(hi, hi, sq));
      }
      swap32(ss, sq);
      __builtin_amdgcn_raw_buffer_store_b64((v2u){fu(ss + sq), (unsigned)tag}, rsWs, WS_ST + ((rt * 4 + tb32) * 16 + Sg) * 512 + t32 * 16 + hl * 8, 0, 16);
    }
    BAR();
    STAMP(7);

    const int mh = w & 1, nh = (w >> 1) & 1;
    f32x16 acc[2][3];
#pragma unroll
    for (int i = 0; i < 16; ++i) {
      acc[0][0][i] = 0.f; acc[0][1][i] = 0.f; acc[0][2][i] = 0.f;
      acc[1][0][i] = 0.f; acc[1][1][i] = 0.f; acc[1][2][i] = 0.f;
    }
    {
      const unsigned relT = lb + mh * 8192 + lane * 16;
      const unsigned relW = lb + (p4 >> 1) * GU_UPOFF + (4 * (g >> 1) + q4) * 192 + 16 * (g & 1) + 8 * (p4 & 1) + 96 * nh;
      v4u tf[2][2];
      s16x8 ga[2][3];
      tf[1][0] = tf[1][1] = (v4u){0, 0, 0, 0};
      ga[1][0] = ga[1][1] = ga[1][2] = asfrag(tf[1][0]);
#define GU_RT(j, tt) tf[(j) & 1][tt] = *(LAS v4u*)(L0 + aT + (tt) * 4096 + (j) * 1024); SB()
#define GU_RW(j, l3) ga[(j) & 1][l3] = cat(trread(L0, aW + (j) * 16 * 192 + 32 * (l3)), trread(L0, aW + ((j) * 16 + 8) * 192 + 32 * (l3))); SB()
#define GU_M(j, tt, l3)                                                       \
  acc[tt][l3] = mfma32(ga[(j) & 1][l3], asfrag(tf[(j) & 1][tt]), acc[tt][l3]); \
  TIE(acc[tt][l3]);                                                            \
  SB()
#define GU_STEP(jn, jc)            \
  GU_RT(jn, 0);                    \
  GU_M(jc, 0, 0);                  \
  GU_RW(jn, 0);                    \
  GU_M(jc, 0, 1);                  \
  GU_RW(jn, 1);                    \
  GU_M(jc, 0, 2);                  \
  GU_RT(jn, 1);                    \
  GU_M(jc, 1, 0);                  \
  GU_RW(jn, 2);                    \
  GU_M(jc, 1, 1);                  \
  GU_M(jc, 1, 2);                  \
  SB();
      const bool real = 6 * m + 3 * nh < 86;
      const unsigned aT0 = opaque(relT + L_GH0), aT1 = opaque(relT + L_GH1), aT2 = opaque(relT + L_GH2);
      const unsigned aW0 = opaque(relW + L_GW0), aW1 = opaque(relW + L_GW1), aW2 = opaque(relW + L_GW2), aW3 = opaque(relW + L_GW3);
#define GU_STAGE(T_, W_)  \
  BAR();                  \
  if (real) {             \
    const unsigned aT = T_, aW = W_; \
    GU_STEP(0, 3)         \
    GU_STEP(1, 0)         \
    GU_STEP(2, 1)         \
    GU_STEP(3, 2)         \
  }
      BAR();
      STAMP(17);
      if (real) {
        const unsigned aT = aT0, aW = aW0;
        GU_RT(0, 0); GU_RW(0, 0); GU_RW(0, 1); GU_RT(0, 1); GU_RW(0, 2);
        GU_STEP(1, 0)
        GU_STEP(2, 1)
        GU_STEP(3, 2)
      }
      GU_STAGE(aT1, aW1)
      STAMP(18);
      GU_STAGE(aT2, aW2)
      GU_STAGE(aT0, aW3)
      GU_STAGE(aT1, aW0)
      GU_STAGE(aT2, aW1)
      GU_STAGE(aT0, aW2)
      STAMP(24);
      GU_STAGE(aT1, aW3)
      const unsigned zo = lb + L_ZERO;
      const unsigned aC = lane < 32 ? lb + L_CSF + 3 * nh * 512 + lane * 16 : zo - 0, aM = lane < 32 ? lb + L_MF + 2 * mh * 512 + lane * 16 : zo;
      const unsigned stp = lane < 32 ? 512 : 0;
      tf[0][0] = *(LAS v4u*)(L0 + aM);
      tf[0][1] = *(LAS v4u*)(L0 + aM + stp);
      ga[0][0] = *(LAS s16x8*)(L0 + aC);
      ga[0][1] = *(LAS s16x8*)(L0 + aC + stp);
      ga[0][2] = *(LAS s16x8*)(L0 + aC + 2 * stp);
      GU_M(3, 0, 0); GU_M(3, 0, 1); GU_M(3, 0, 2); GU_M(3, 1, 0); GU_M(3, 1, 1); GU_M(3, 1, 2);
      GU_M(0, 0, 0); GU_M(0, 0, 1); GU_M(0, 0, 2); GU_M(0, 1, 0); GU_M(0, 1, 1); GU_M(0, 1, 2);
    }
    const float r2a = *(LAS float*)(L + L_R2 + (64 * mh + t32) * 4), r2b = *(LAS float*)(L + L_R2 + (64 * mh + 32 + t32) * 4);
    BAR();
    STAMP(9);
#pragma unroll
    for (int tt = 0; tt < 2; ++tt) {
      const float r2 = tt ? r2b : r2a;
#pragma unroll
      for (int l3 = 0; l3 < 3; ++l3) {
        const int fblk = 6 * m + 3 * nh + l3;
        float hv[8];
#pragma unroll
        for (int i = 0; i < 2; ++i)
#pragma unroll
          for (int e = 0; e < 4; ++e) hv[4 * i + e] = (acc[tt][l3][8 * i + e] * r2) * acc[tt][l3][8 * i + 4 + e];
        const v4u o = {pk2(hv[0], hv[1]), pk2(hv[2], hv[3]), pk2(hv[4], hv[5]), pk2(hv[6], hv[7])};
        const unsigned off = fblk < 86 ? WS_HH + ((rt * 4 + 2 * mh + tt) * 86 + fblk) * 1024 + lane * 16 : 0x7FFFFFF0u;
        __builtin_amdgcn_raw_buffer_store_b128(o, rsWs, off, 0, 16);
      }
    }
    asm volatile("s_barrier" ::: "memory");
    WAITV(0);
    if (lane < 2) set_flag(p.ws + WS_F4 + (rt * 4 + 2 * mh + lane) * 256 + (m * 2 + nh) * 8, tag);

    STAMP(10);

    const unsigned kh2048 = kh * 2048;
    const unsigned pb0 = opaque(rel0 + dn_base(0) + dn_n(0) * kh2048), pb1 = opaque(rel1 + dn_base(0) + dn_n(0) * kh2048);
    BAR();
    s16x8 wpre[DN_PRE][2];
#pragma unroll
    for (int i = 0; i < DN_PRE; ++i) {
      wpre[i][0] = cat(trread(L0, pb0 + i * 2048), trread(L0, pb0 + i * 2048 + 1024));
      wpre[i][1] = cat(trread(L0, pb1 + i * 2048), trread(L0, pb1 + i * 2048 + 1024));
    }
#pragma unroll
    for (int i = 0; i < DN_PRE; ++i) { TIE(wpre[i][0]); TIE(wpre[i][1]); }
    BAR();
    STAMP(25);
    pollN<32>(p.ws + WS_F4 + (rt * 4 + tb32) * 256, tag, lane);
    STAMP(11); CSTAMP(56);
    v4u hf_[DN_RING];
    const unsigned hv = WS_HH + ((rt * 4 + tb32) * 86 + 43 * kh) * 1024 + lane * 16;
#pragma unroll
    for (int i = 0; i < DN_RING; ++i) hf_[i] = ld128_sc1(rsWs, hv, i * 1024);
#pragma unroll
    for (int i = 0; i < 16; ++i) { a0[i] = 0.f; a1[i] = 0.f; }
    s16x8 fw[2][2];
    DnStep<0>::run(hf_, wpre, fw, a0, a1, rel0, rel1, kh2048, pb0, pb1, rsWs, hv);
    STAMP(12); CSTAMP(57);
    {
      int tz = threadIdx.x;
      asm volatile("" : "+v"(tz));
      const int lane = tz & 63, hl = lane >> 5, t32 = lane & 31;
      const f32x16 give = kh ? a0 : a1;
      const f32x16 keep = kh ? a1 : a0;
#pragma unroll
      for (int i = 0; i < 4; ++i)
        *(LAS f32x4*)(L + L_RED_DN + (w * 4 + i) * 1024 + lane * 16) = (f32x4){give[4 * i], give[4 * i + 1], give[4 * i + 2], give[4 * i + 3]};
      BAR();
      unsigned ob[8];
#pragma unroll
      for (int i = 0; i < 4; ++i) {
        const f32x4 o = *(LAS f32x4*)(L + L_RED_DN + ((w ^ 2) * 4 + i) * 1024 + lane * 16);
        ob[2 * i] = pk2(keep[4 * i] + o[0] + bflo(hres[2 * i]), keep[4 * i + 1] + o[1] + bfhi(hres[2 * i]));
        ob[2 * i + 1] = pk2(keep[4 * i + 2] + o[2] + bflo(hres[2 * i + 1]), keep[4 * i + 3] + o[3] + bfhi(hres[2 * i + 1]));
      }
      const rsrc_t rsOut = mkrsrc(p.out, 2048u * 1024u);
      const unsigned oo = (128 * rt + 32 * tb32 + t32) * 1024 + (64 * cg + 32 * kh + 8 * hl) * 2;
#pragma unroll
      for (int pr = 0; pr < 2; ++pr) {
        swap32u(ob[4 * pr], ob[4 * pr + 2]);
        swap32u(ob[4 * pr + 1], ob[4 * pr + 3]);
        __builtin_amdgcn_raw_buffer_store_b128((v4u){ob[4 * pr], ob[4 * pr + 1], ob[4 * pr + 2], ob[4 * pr + 3]}, rsOut, oo + pr * 32, 0, 0);
      }
    }
    if (b == 0 && tid == 0) __hip_atomic_store((GAS u64*)(p.ws + WS_EPOCH), tag, __ATOMIC_RELAXED, __HIP_MEMORY_SCOPE_AGENT);
    STAMP(13);
    STAMP_FLUSH();
    asm volatile(".Lfb_end:");
  }
}

}

extern "C" void kernel_launch(void* const* d_in, const int* in_sizes, int n_in, void* d_out, int out_size, void* d_ws, size_t ws_size,
                              hipStream_t stream) {
  Params p;
  p.x = d_in[0];
  p.wqkv = d_in[1];
  p.wphi = d_in[2];
  p.wo = d_in[3];
  p.wg = d_in[5];
  p.wu = d_in[6];
  p.wd = d_in[7];
  p.out = d_out;
  p.ws = (unsigned char*)d_ws;
  fused_block_kernel<<<256, 512, 0, stream>>>(p);
}
```

```cpp
#include <hip/hip_runtime.h>
#include <hip/hip_bf16.h>
#include <stdint.h>

#define GAS __attribute__((address_space(1)))
#define LAS __attribute__((address_space(3)))
typedef unsigned v4u __attribute__((ext_vector_type(4)));
typedef unsigned v2u __attribute__((ext_vector_type(2)));
typedef float f32x2 __attribute__((ext_vector_type(2)));
typedef float f32x4 __attribute__((ext_vector_type(4)));
typedef float f32x16 __attribute__((ext_vector_type(16)));
typedef short s16x4 __attribute__((ext_vector_type(4)));
typedef short s16x8 __attribute__((ext_vector_type(8)));
typedef __bf16 bf16x2 __attribute__((ext_vector_type(2)));
typedef __amdgpu_buffer_rsrc_t rsrc_t;
typedef unsigned long long u64;

#define STAMPS 0
#define GU_ROT 4
#define PACEN 0
#define PACEN2 0
#define PACE() do { if (PACEN) __builtin_amdgcn_s_sleep(PACEN); } while (0)
#define PACE2() do { if (PACEN2) __builtin_amdgcn_s_sleep(PACEN2); } while (0)
#define WDV 3
#define CODEPF 1
#define TOUCH 1
#define MAPPING 2
#define SB() __builtin_amdgcn_sched_barrier(0)

namespace {

constexpr unsigned WS_EPOCH = 0;
constexpr unsigned WS_F1 = 4096;
constexpr unsigned WS_F5 = 12288;
constexpr unsigned WS_F2 = 20480;
constexpr unsigned WS_F3 = 36864;
constexpr unsigned WS_F4 = 45056;
constexpr unsigned WS_KVP = 65536;
constexpr unsigned KVP_STRIDE = 4352;
constexpr unsigned WS_CSP = WS_KVP + 256 * KVP_STRIDE;
constexpr unsigned WS_ST = WS_CSP + 256 * 768;
constexpr unsigned ST_BYTES = 16 * 4 * 16 * 512;
constexpr unsigned WS_S1 = WS_ST + ST_BYTES;
constexpr unsigned WS_O = WS_S1 + 16 * 128 * 16;
constexpr unsigned O_BYTES = 16 * 4 * 32 * 1024;
constexpr unsigned WS_H = WS_O + O_BYTES;
constexpr unsigned H_BYTES = 16 * 4 * 32 * 1024;
constexpr unsigned WS_HH = WS_H + H_BYTES;
constexpr unsigned HH_BYTES = 16 * 4 * 86 * 1024;
constexpr unsigned WS_END = WS_HH + HH_BYTES;
constexpr unsigned WS_STAMP = 16u << 20;

constexpr unsigned A_SLOT = 24576;
constexpr unsigned L_WQ = 98304;
constexpr unsigned L_CSW = 131072;
constexpr unsigned L_KVIMG = 65536;
constexpr unsigned L_WO = 0;
constexpr unsigned GU_UPOFF = 12320, GU_SLOT = 24640;
constexpr unsigned L_GW0 = 65536, L_GW1 = L_GW0 + GU_SLOT, L_GW2 = L_GW1 + GU_SLOT, L_GW3 = 0;
constexpr unsigned L_GH0 = 24640, L_GH1 = 41024, L_GH2 = 139456, L_SPARE = 57408;
constexpr unsigned L_RED_WO = 139456;
constexpr unsigned L_RED_DN = L_GW1;
constexpr unsigned L_MISC = 155840;
constexpr unsigned L_CS = L_MISC;
constexpr unsigned L_WPHI = L_MISC + 1024;
constexpr unsigned L_STAT = L_MISC + 2048;
constexpr unsigned L_CSF = L_MISC;
constexpr unsigned L_KF = L_MISC + 3072;
constexpr unsigned L_ZF = L_MISC + 5120;
constexpr unsigned L_MF = L_MISC + 5248;
constexpr unsigned L_R2 = L_MISC + 7296;
constexpr unsigned L_ZERO = L_MISC + 7808;
constexpr unsigned L_CNT = L_MISC + 7824;
constexpr unsigned L_STMP = L_MISC + 7848;
constexpr unsigned LDS_BYTES = 163840;
static_assert(L_GW2 + GU_SLOT == L_GH2, "map");
static_assert(L_GH2 + 16384 == L_MISC, "map");
static_assert(L_ZERO + 16 <= LDS_BYTES, "map");
static_assert(L_CSW + 4 * 4608 <= L_MISC, "map");

constexpr unsigned SPIN_CAP = 1u << 18;
constexpr float LOG2E = 1.4426950408889634f;

struct Params {
  const void* x;
  const void* wqkv;
  const void* wphi;
  const void* wo;
  const void* wg;
  const void* wu;
  const void* wd;
  void* out;
  unsigned char* ws;
};

#define DI __device__ __forceinline__

DI rsrc_t mkrsrc(const void* p, unsigned bytes) { return __builtin_amdgcn_make_buffer_rsrc((void*)p, 0, (int)bytes, 0x00020000); }
DI void dma16(unsigned ldsaddr, unsigned voff, rsrc_t rs, unsigned soff) {
  asm volatile("s_mov_b32 m0, %0\n\ts_nop 0\n\tbuffer_load_dwordx4 %1, %2, %3 offen lds" ::"s"(ldsaddr), "v"(voff), "s"(rs), "s"(soff) : "memory");
}
template <unsigned OFF> DI void dma16o(unsigned ldsbase, unsigned voff, rsrc_t rs, unsigned soff) {
  asm volatile("s_add_u32 m0, %0, %4\n\ts_nop 0\n\tbuffer_load_dwordx4 %1, %2, %3 offen lds" ::"s"(ldsbase), "v"(voff), "s"(rs), "s"(soff), "n"(OFF) : "memory", "scc");
}
template <unsigned OFF> DI void dma16o_sc1(unsigned ldsbase, unsigned voff, rsrc_t rs, unsigned soff) {
  asm volatile("s_add_u32 m0, %0, %4\n\ts_nop 0\n\tbuffer_load_dwordx4 %1, %2, %3 offen sc1 lds" ::"s"(ldsbase), "v"(voff), "s"(rs), "s"(soff), "n"(OFF) : "memory", "scc");
}
DI void dma16_sc1(unsigned ldsaddr, unsigned voff, rsrc_t rs, unsigned soff) {
  asm volatile("s_mov_b32 m0, %0\n\ts_nop 0\n\tbuffer_load_dwordx4 %1, %2, %3 offen sc1 lds" ::"s"(ldsaddr), "v"(voff), "s"(rs), "s"(soff) : "memory");
}
DI v4u ld128(rsrc_t rs, unsigned voff, unsigned soff) {
  v4u r;
  asm volatile("buffer_load_dwordx4 %0, %1, %2, %3 offen" : "=v"(r) : "v"(voff), "s"(rs), "s"(soff));
  return r;
}
DI v4u ld128_sc1(rsrc_t rs, unsigned voff, unsigned soff) {
  v4u r;
  asm volatile("buffer_load_dwordx4 %0, %1, %2, %3 offen sc1" : "=v"(r) : "v"(voff), "s"(rs), "s"(soff));
  return r;
}
DI v2u ld64(rsrc_t rs, unsigned voff, unsigned soff) {
  v2u r;
  asm volatile("buffer_load_dwordx2 %0, %1, %2, %3 offen" : "=v"(r) : "v"(voff), "s"(rs), "s"(soff));
  return r;
}
#define WAITV(n) asm volatile("s_waitcnt vmcnt(" #n ")" ::: "memory")
#define BAR() asm volatile("s_waitcnt lgkmcnt(0)\n\ts_barrier" ::: "memory")
#define WAITV_BAR(n) asm volatile("s_waitcnt vmcnt(" #n ") lgkmcnt(0)\n\ts_barrier" ::: "memory")
#define TIE(x) asm volatile("" : "+v"(x))

DI s16x4 trread(LAS unsigned char* L, unsigned off) { return __builtin_amdgcn_ds_read_tr16_b64_v4i16((LAS s16x4*)(L + off)); }
DI s16x8 cat(s16x4 a, s16x4 b) { return __builtin_shufflevector(a, b, 0, 1, 2, 3, 4, 5, 6, 7); }
DI s16x8 asfrag(v4u v) { return __builtin_bit_cast(s16x8, v); }
DI f32x4 mfma16(s16x8 a, s16x8 b, f32x4 c) { return __builtin_amdgcn_mfma_f32_16x16x32_bf16(a, b, c, 0, 0, 0); }
DI f32x4 mfma16k16(s16x4 a, s16x4 b, f32x4 c) { return __builtin_amdgcn_mfma_f32_16x16x16bf16_1k(a, b, c, 0, 0, 0); }
DI f32x16 mfma32(s16x8 a, s16x8 b, f32x16 c) { return __builtin_amdgcn_mfma_f32_32x32x16_bf16(a, b, c, 0, 0, 0); }
DI unsigned pk2(float lo, float hi) {
  f32x2 v = {lo, hi};
  return __builtin_bit_cast(unsigned, __builtin_convertvector(v, bf16x2));
}
DI float dot2(unsigned a, unsigned b, float c) {
  return __builtin_amdgcn_fdot2_f32_bf16(__builtin_bit_cast(bf16x2, a), __builtin_bit_cast(bf16x2, b), c, false);
}
DI float bflo(unsigned u) { return __builtin_bit_cast(float, u << 16); }
DI float bfhi(unsigned u) { return __builtin_bit_cast(float, u & 0xffff0000u); }
DI unsigned fu(float f) { return __builtin_bit_cast(unsigned, f); }
DI float uf(unsigned u) { return __builtin_bit_cast(float, u); }
DI void swap16(float& a, float& b) {
  auto r = __builtin_amdgcn_permlane16_swap(fu(a), fu(b), false, false);
  a = uf(r[0]);
  b = uf(r[1]);
}
DI void swap32(float& a, float& b) {
  auto r = __builtin_amdgcn_permlane32_swap(fu(a), fu(b), false, false);
  a = uf(r[0]);
  b = uf(r[1]);
}
DI void swap16u(unsigned& a, unsigned& b) {
  auto r = __builtin_amdgcn_permlane16_swap(a, b, false, false);
  a = r[0];
  b = r[1];
}
DI void swap32u(unsigned& a, unsigned& b) {
  auto r = __builtin_amdgcn_permlane32_swap(a, b, false, false);
  a = r[0];
  b = r[1];
}
DI float rowsum16(float v) {
  v += uf(__builtin_amdgcn_update_dpp(0, (int)fu(v), 0x128, 0xf, 0xf, false));
  v += uf(__builtin_amdgcn_update_dpp(0, (int)fu(v), 0x124, 0xf, 0xf, false));
  v += uf(__builtin_amdgcn_update_dpp(0, (int)fu(v), 0x122, 0xf, 0xf, false));
  v += uf(__builtin_amdgcn_update_dpp(0, (int)fu(v), 0x121, 0xf, 0xf, false));
  return v;
}
DI void red2add(float& a, float& b) {
  swap16(a, b);
  float m = a + b, m2 = m;
  swap32(m, m2);
  float t = m + m2, t2 = t;
  swap16(t, t2);
  a = t;
  b = t2;
}
template <bool MAX> DI float rop(float a, float b) { return MAX ? fmaxf(a, b) : a + b; }
template <bool MAX> DI void red4(float& a, float& b, float& c, float& d) {
  swap16(a, b);
  float m1 = rop<MAX>(a, b);
  swap16(c, d);
  float m2 = rop<MAX>(c, d);
  swap32(m1, m2);
  float t = rop<MAX>(m1, m2), t2 = t;
  swap32(t, t2);
  float y1 = t, y1b = t, y2 = t2, y2b = t2;
  swap16(y1, y1b);
  swap16(y2, y2b);
  a = y1;
  b = y1b;
  c = y2;
  d = y2b;
}
DI void dual_softmax(f32x4 p0, f32x4 p1, v2u& pos0, v2u& neg0, v2u& pos1, v2u& neg1) {
  float mx0 = fmaxf(fmaxf(p0[0], p0[1]), fmaxf(p0[2], p0[3]));
  float mn0 = -fminf(fminf(p0[0], p0[1]), fminf(p0[2], p0[3]));
  float mx1 = fmaxf(fmaxf(p1[0], p1[1]), fmaxf(p1[2], p1[3]));
  float mn1 = -fminf(fminf(p1[0], p1[1]), fminf(p1[2], p1[3]));
  red4<true>(mx0, mn0, mx1, mn1);
  const float a0 = -mx0 * LOG2E, b0 = -mn0 * LOG2E, a1 = -mx1 * LOG2E, b1 = -mn1 * LOG2E;
  float ep0[4], en0[4], ep1[4], en1[4];
#pragma unroll
  for (int j = 0; j < 4; ++j) {
    ep0[j] = __builtin_amdgcn_exp2f(fmaf(p0[j], LOG2E, a0));
    en0[j] = __builtin_amdgcn_exp2f(fmaf(p0[j], -LOG2E, b0));
    ep1[j] = __builtin_amdgcn_exp2f(fmaf(p1[j], LOG2E, a1));
    en1[j] = __builtin_amdgcn_exp2f(fmaf(p1[j], -LOG2E, b1));
  }
  float sp0 = (ep0[0] + ep0[1]) + (ep0[2] + ep0[3]), sn0 = (en0[0] + en0[1]) + (en0[2] + en0[3]);
  float sp1 = (ep1[0] + ep1[1]) + (ep1[2] + ep1[3]), sn1 = (en1[0] + en1[1]) + (en1[2] + en1[3]);
  red4<false>(sp0, sn0, sp1, sn1);
  const float ip0 = __builtin_amdgcn_rcpf(sp0), in0 = __builtin_amdgcn_rcpf(sn0), ip1 = __builtin_amdgcn_rcpf(sp1), in1 = __builtin_amdgcn_rcpf(sn1);
  pos0 = (v2u){pk2(ep0[0] * ip0, ep0[1] * ip0), pk2(ep0[2] * ip0, ep0[3] * ip0)};
  neg0 = (v2u){pk2(en0[0] * in0, en0[1] * in0), pk2(en0[2] * in0, en0[3] * in0)};
  pos1 = (v2u){pk2(ep1[0] * ip1, ep1[1] * ip1), pk2(ep1[2] * ip1, ep1[3] * ip1)};
  neg1 = (v2u){pk2(en1[0] * in1, en1[1] * in1), pk2(en1[2] * in1, en1[3] * in1)};
}
DI u64 wait_epoch(v2u ep) {
  unsigned lo, hi;
  asm volatile("s_waitcnt vmcnt(0)\n\tv_readfirstlane_b32 %0, %2\n\tv_readfirstlane_b32 %1, %3" : "=s"(lo), "=s"(hi) : "v"(ep[0]), "v"(ep[1]) : "memory");
  return ((u64)hi << 32) | lo;
}
DI u64 mix64(u64 z) {
  z += 0x9E3779B97F4A7C15ull;
  z = (z ^ (z >> 30)) * 0xBF58476D1CE4E5B9ull;
  z = (z ^ (z >> 27)) * 0x94D049BB133111EBull;
  return z ^ (z >> 31);
}
template <int N> DI void pollN(const unsigned char* base, u64 tag, int lane) {
  const GAS u64* fl = (const GAS u64*)base;
#pragma unroll 1
  for (unsigned it = 0; it < SPIN_CAP; ++it) {
    const u64 v = __hip_atomic_load(fl + (lane & (N - 1)), __ATOMIC_RELAXED, __HIP_MEMORY_SCOPE_AGENT);
    if (__all(v == tag)) break;
    __builtin_amdgcn_s_sleep(1);
  }
}
DI void set_flag(unsigned char* f, u64 tag) { __hip_atomic_store((GAS u64*)f, tag, __ATOMIC_RELAXED, __HIP_MEMORY_SCOPE_AGENT); }

DI v4u ld128_sc1o(rsrc_t rs, unsigned voff) {
  v4u r;
  asm volatile("buffer_load_dwordx4 %0, %1, %2, 0 offen sc1" : "=v"(r) : "v"(voff), "s"(rs));
  return r;
}
DI unsigned opaque(unsigned v) {
  asm volatile("" : "+v"(v));
  return v;
}
#define TIE4(a, b, c, d) asm volatile("" : "+v"(a), "+v"(b), "+v"(c), "+v"(d))
#define WAITV_TIE(n, x) asm volatile("s_waitcnt vmcnt(" #n ")" : "+v"(x))
template <int N> DI void waitv_tie(v4u& x) { asm volatile("s_waitcnt vmcnt(%1)" : "+v"(x) : "n"(N)); }
template <int N> DI void waitv() { asm volatile("s_waitcnt vmcnt(%0)" ::"n"(N) : "memory"); }

DI void lds_arrive(LAS unsigned char* c, int lane) {
  asm volatile("s_waitcnt lgkmcnt(0)" ::: "memory");
  if (lane == 0) __hip_atomic_fetch_add((LAS unsigned*)c, 1u, __ATOMIC_RELAXED, __HIP_MEMORY_SCOPE_WORKGROUP);
}
DI void lds_wait(LAS unsigned char* c, unsigned n) {
#pragma unroll 1
  for (unsigned it = 0; it < SPIN_CAP; ++it) {
    const unsigned v = __hip_atomic_load((LAS unsigned*)c, __ATOMIC_RELAXED, __HIP_MEMORY_SCOPE_WORKGROUP);
    if (__builtin_amdgcn_readfirstlane(v) >= n) break;
  }
  asm volatile("" ::: "memory");
}

DI void lds_wait_sleep(LAS unsigned char* c, unsigned n) {
#pragma unroll 1
  for (unsigned it = 0; it < SPIN_CAP; ++it) {
    const unsigned v = __hip_atomic_load((LAS unsigned*)c, __ATOMIC_RELAXED, __HIP_MEMORY_SCOPE_WORKGROUP);
    if (__builtin_amdgcn_readfirstlane(v) >= n) break;
    __builtin_amdgcn_s_sleep(2);
  }
  asm volatile("" ::: "memory");
}

#define STAMPW(wv, i) \
  do {                \
  } while (0)
#define STAMP_FLUSH() \
  do {                \
  } while (0)
#define STAMP(i) STAMPW(0, i)
#define CSTAMP(i) do {} while (0)

constexpr int DN_RING = 20, DN_PRE = 6;
constexpr unsigned dn_base(int i) {
  return i < 6 ? L_GW0 : i < 7 ? L_SPARE : i < 13 ? L_GW1 : i < 17 ? L_GH2 : i < 23 ? L_GW2 : i < 27 ? L_GH0 : i < 33 ? L_GW3 : i < 37 ? L_GH1 : L_GW0;
}
constexpr int dn_i0(int i) { return i < 6 ? 0 : i < 7 ? 6 : i < 13 ? 7 : i < 17 ? 13 : i < 23 ? 17 : i < 27 ? 23 : i < 33 ? 27 : i < 37 ? 33 : 37; }
constexpr int dn_n(int i) { return i < 6 ? 6 : i < 7 ? 1 : i < 13 ? 6 : i < 17 ? 4 : i < 23 ? 6 : i < 27 ? 4 : i < 33 ? 6 : i < 37 ? 4 : 6; }

template <int I> struct DnStep {
  static DI void run(v4u (&hf)[DN_RING], s16x8 (&wpre)[DN_PRE][2], s16x8 (&fw)[2][2], f32x16& a0, f32x16& a1, unsigned rel0, unsigned rel1,
                     unsigned kh2048, unsigned b0, unsigned b1, rsrc_t rsWs, unsigned hv) {
    LAS unsigned char* const L0 = (LAS unsigned char*)0;
    if constexpr (I + 1 < 43 && I + 1 >= DN_PRE) {
      constexpr int J = I + 1;
      if constexpr (dn_i0(J) == J) {
        b0 = opaque(rel0 + dn_base(J) + dn_n(J) * kh2048);
        b1 = opaque(rel1 + dn_base(J) + dn_n(J) * kh2048);
      }
      if constexpr (J == 37) asm volatile("s_barrier" ::: "memory");
      constexpr unsigned o = (J - dn_i0(J)) * 2048;
      fw[J & 1][0] = cat(trread(L0, b0 + o), trread(L0, b0 + o + 1024));
      SB();
    }
    constexpr int younger = (I + DN_RING - 1 < 42 ? I + DN_RING - 1 : 42) - I;
    waitv_tie<younger>(hf[I % DN_RING]);
    const s16x8 bf = asfrag(hf[I % DN_RING]);
    if constexpr (I < DN_PRE) a0 = mfma32(wpre[I][0], bf, a0);
    else a0 = mfma32(fw[I & 1][0], bf, a0);
    TIE(a0);
    SB();
    if constexpr (I + 1 < 43 && I + 1 >= DN_PRE) {
      constexpr int J = I + 1;
      constexpr unsigned o = (J - dn_i0(J)) * 2048;
      fw[J & 1][1] = cat(trread(L0, b1 + o), trread(L0, b1 + o + 1024));
      SB();
    }
    if constexpr (I < DN_PRE) a1 = mfma32(wpre[I][1], bf, a1);
    else a1 = mfma32(fw[I & 1][1], bf, a1);
    TIE(a1);
    SB();
    if constexpr (I + DN_RING < 43) hf[I % DN_RING] = ld128_sc1(rsWs, hv, (I + DN_RING) * 1024);
    SB();
    if constexpr (I + 1 < 43) DnStep<I + 1>::run(hf, wpre, fw, a0, a1, rel0, rel1, kh2048, b0, b1, rsWs, hv);
  }
};

__global__ void __launch_bounds__(512, 2) fused_block_kernel(Params p) {
  __shared__ __attribute__((aligned(16))) unsigned char smem[LDS_BYTES];
  LAS unsigned char* L = (LAS unsigned char*)smem;
  LAS unsigned char* const L0 = (LAS unsigned char*)0;
  const unsigned lb = (unsigned)(uintptr_t)L;
  const int tid = threadIdx.x, lane = tid & 63;
  const int w = __builtin_amdgcn_readfirstlane(tid >> 6);
  const int w4 = w & 3;
  const int b = blockIdx.x, xcd = b & 7, jj = b >> 3;
  const int rt = 4 * (xcd >> 1) + (jj >> 3), m = 8 * (xcd & 1) + (jj & 7);
  const int kro = (m + 2 * (rt & 3)) & 7;
  const int l15 = lane & 15, g = lane >> 4, q4 = l15 >> 2, p4 = lane & 3;
  const int hl = lane >> 5, t32 = lane & 31;
  const int hf = m >> 3, cg = m & 7;
  STAMP(0);

  const rsrc_t rsX = mkrsrc(p.x, 2048u * 1024u);
  const rsrc_t rsWs = mkrsrc(p.ws, WS_END);
  const s16x8 ones8 = {0x3F80, 0x3F80, 0x3F80, 0x3F80, 0x3F80, 0x3F80, 0x3F80, 0x3F80};
  v2u ep;
  asm volatile("global_load_dwordx2 %0, %1, off sc1" : "=v"(ep) : "v"(p.ws + WS_EPOCH));
  const unsigned trW0 = 16384 + (8 * g + q4) * 64 + ((0 ^ (g & 1)) << 5) + 8 * p4;
  const unsigned trW1 = 16384 + (8 * g + q4) * 64 + ((1 ^ (g & 1)) << 5) + 8 * p4;
  const unsigned voffL = lane * 16;
  const unsigned voffR = (lane >> 3) * 1024 + (((lane & 7) ^ (((lane >> 4) & 1) << 2)) * 16);

  if (__builtin_expect(w >= 4, 1)) {
    asm volatile(".Lfb_begin:");
    const rsrc_t rsWqkv = mkrsrc(p.wqkv, 512u * 3072u);
    const unsigned voffX0 = (128 * rt + 32 * w4 + (lane >> 3)) * 1024 + (((lane & 7) ^ (lane >> 4)) * 16);
    const unsigned voffX1 = (128 * rt + 32 * w4 + 8 + (lane >> 3)) * 1024 + (((lane & 7) ^ (4 + (lane >> 4))) * 16);
    const unsigned voffW = (lane >> 2) * 3072 + (((lane & 3) ^ ((lane >> 5) << 1)) * 16);
    const unsigned albase = lb + 4 * w4 * 1024, awbase = lb + 16384 + w4 * 1024, awsrc = 16 * w4 * 3072 + 1024 + 64 * m;
#define A_LOAD(st)                                                                                     \
  {                                                                                                    \
    const unsigned so_ = ((st) & 3) * A_SLOT, kb_ = (((st) + kro) & 7);                                \
    const unsigned lx_ = albase + so_, lw_ = awbase + so_, sx_ = kb_ * 128, sx2_ = sx_ + 16384, sw_ = awsrc + kb_ * (64 * 3072); \
    dma16o<0>(lx_, voffX0, rsX, sx_);                                                                  \
    dma16o<1024>(lx_, voffX1, rsX, sx_);                                                               \
    dma16o<0>(lw_, voffW, rsWqkv, sw_);                                                                \
    dma16o<2048>(lx_, voffX0, rsX, sx2_);                                                              \
    dma16o<3072>(lx_, voffX1, rsX, sx2_);                                                              \
    dma16o<4096>(lw_, voffW, rsWqkv, sw_ + 1024);                                                      \
  }
    if (w == 7) {
      const rsrc_t rsPhi = mkrsrc(p.wphi, 32u * 512u);
      dma16(lb + L_WPHI, voffL, rsPhi, m * 1024);
    }
    A_LOAD(0)
    A_LOAD(1)
    STAMPW(4, 35);
    WAITV_BAR(6);
    A_LOAD(2)
    A_LOAD(3)
#pragma unroll 1
    for (int st = 1; st < 8; ++st) {
      if (st <= 5) WAITV(12);
      else if (st == 6) WAITV(10);
      else WAITV(8);
      BAR();
      if (st <= 4) A_LOAD(st + 3)
      else if (st <= 6) {
#pragma unroll
        for (int j = 0; j < 4; ++j) {
          const unsigned r = 128 * w4 + 64 * (st - 5) + 16 * j;
          dma16(lb + L_WQ + r * 64, voffW, rsWqkv, ((r + 64 * kro) & 511) * 3072 + 64 * m);
        }
      }
    }
    const u64 tag = mix64(wait_epoch(ep));
    BAR();
    unsigned dummy = 0;
    {
      const unsigned idx = (b * 4 + w4) * 33 + (lane < 33 ? lane : 0);
      const unsigned char* q;
      unsigned o = idx;
      if (o < 11008) q = (const unsigned char*)p.wg;
      else if ((o -= 11008) < 11008) q = (const unsigned char*)p.wu;
      else { o -= 11008; q = (const unsigned char*)p.wd; if (o >= 11008) o = 0; }
      asm volatile("global_load_dword %0, %1, off" : "=v"(dummy) : "v"(q + (size_t)o * 128));
    }
    {
      const rsrc_t rsG = mkrsrc(p.wg, 512u * 2752u), rsU = mkrsrc(p.wu, 512u * 2752u);
      const unsigned vo = (lane >> 1) * 2752 + (lane & 1) * 16, so = 32 * rt * 2752 + 192 * m;
      const unsigned dst = lb + L_CSW + w4 * 4608;
      if (w4 < 2) {
        dma16(dst + 2304, vo, rsG, so + 32 * (w4 + 4));
        dma16(dst + 2304 + 1152, vo, rsU, so + 32 * (w4 + 4));
      }
      dma16(dst, vo, rsG, so + 32 * w4);
      dma16(dst + 1152, vo, rsU, so + 32 * w4);
      const rsrc_t rsWo = mkrsrc(p.wo, 512u * 1024u);
#pragma unroll 4
      for (int i = 0; i < 16; ++i) { dma16(lb + L_WO + (w4 * 16 + i) * 1024, voffR, rsWo, (w4 * 16 + i) * 8192 + 128 * cg); PACE(); }
    }
    {
      const unsigned cq = lb + L_WQ - 16384 + ((w4 & 1) ? trW1 : trW0) + (w4 >> 1) * 8 * 2048;
      f32x4 acq = {0, 0, 0, 0}, acq2 = acq;
#pragma unroll
      for (int s = 0; s < 8; s += 2) {
        acq = mfma16(cat(trread(L0, cq + s * 2048), trread(L0, cq + s * 2048 + 256)), ones8, acq);
        acq2 = mfma16(cat(trread(L0, cq + (s + 1) * 2048), trread(L0, cq + (s + 1) * 2048 + 256)), ones8, acq2);
      }
      if (l15 == 0) *(LAS f32x4*)(L + L_CS + 256 + (w4 & 1) * 128 + (w4 >> 1) * 64 + g * 16) = acq + acq2;
      lds_arrive(L + L_CNT + 4, lane);
    }
    asm volatile("s_waitcnt vmcnt(16)" : "+v"(dummy)::"memory");
#pragma unroll 1
    for (int bl = w4, k = 0; bl < 6; bl += 4, ++k) {
      const unsigned a = L_CSW + w4 * 4608 + k * 2304 + (p4 >> 1) * 1152 + (4 * (g >> 1) + q4) * 32 + 16 * (g & 1) + 8 * (p4 & 1);
      f32x16 c;
#pragma unroll
      for (int i = 0; i < 16; ++i) c[i] = 0.f;
      c = mfma32(cat(trread(L, a), trread(L, a + 8 * 32)), ones8, c);
      c = mfma32(cat(trread(L, a + 16 * 32), trread(L, a + 24 * 32)), ones8, c);
      const unsigned off = t32 == 0 ? WS_CSP + ((m * 16 + rt) * 6 + bl) * 128 + hl * 16 : 0x7FFFFF00u;
#pragma unroll
      for (int i = 0; i < 4; ++i)
        __builtin_amdgcn_raw_buffer_store_b128(__builtin_bit_cast(v4u, (f32x4){c[4 * i], c[4 * i + 1], c[4 * i + 2], c[4 * i + 3]}), rsWs, off, i * 32, 16);
    }
    lds_arrive(L + L_CNT + 12, lane);
    WAITV(0);
    if (lane == 0) set_flag(p.ws + WS_F5 + m * 512 + (rt * 4 + w4) * 8, tag);

    const rsrc_t rsGU = mkrsrc((w4 >> 1) ? p.wu : p.wg, 512u * 2752u);
    unsigned voffG[6];
#pragma unroll
    for (int j = 0; j < 6; ++j) {
      const unsigned u = 64 * (6 * (w4 & 1) + j) + lane, row = u / 12, c = u - row * 12;
      voffG[j] = row * 2752 + c * 16;
    }
    const unsigned gdst = (w4 >> 1) * GU_UPOFF + 6 * (w4 & 1) * 1024;
    const unsigned gsrc = 192 * m;
#define GU_LOADW(st, slot)                                                                       \
  {                                                                                              \
    const unsigned l_ = lb + (slot) + gdst, s_ = gsrc + (((st) + kro) & 7) * 64 * 2752;          \
    dma16o<0>(l_, voffG[0], rsGU, s_); dma16o<1024>(l_, voffG[1], rsGU, s_); dma16o<2048>(l_, voffG[2], rsGU, s_); \
    dma16o<3072>(l_, voffG[3], rsGU, s_); dma16o<4096>(l_, voffG[4], rsGU, s_); dma16o<5120>(l_, voffG[5], rsGU, s_); \
  }
#define GU_LOADW_PACED(st, slot)                                                                 \
  {                                                                                              \
    _Pragma("unroll") for (int j = 0; j < 6; ++j) { dma16(lb + (slot) + gdst + j * 1024, voffG[j], rsGU, gsrc + (((st) + kro) & 7) * 64 * 2752); PACE2(); } \
  }
    lds_wait(L + L_CNT + 8, 4);
    GU_LOADW_PACED(0, L_GW0)
    lds_wait(L + L_CNT + 12, 8);
    GU_LOADW_PACED(1, L_GW1)
    GU_LOADW_PACED(2, L_GW2)
    STAMPW(4, 30);
    BAR();
    STAMPW(4, 28);
    pollN<64>(p.ws + WS_F5 + m * 512, tag, lane);
    if (tid == 256) *(LAS v4u*)(L + L_ZERO) = (v4u){0, 0, 0, 0};
#pragma unroll 1
    for (int bl = w4; bl < 6; bl += 4) {
      float s = 0.f;
      unsigned cv[8];
#pragma unroll
      for (int j = 0; j < 8; ++j) cv[j] = __builtin_amdgcn_raw_buffer_load_b32(rsWs, WS_CSP + ((m * 16 + 8 * hl) * 6 + bl) * 128 + t32 * 4, j * 768, 16);
#pragma unroll
      for (int j = 0; j < 8; ++j) s += uf(cv[j]);
      float s2 = s;
      swap32(s, s2);
      s += s2;
      const unsigned hi = pk2(s, 0.f) & 0xffffu, lo = pk2(s - uf(hi << 16), 0.f) & 0xffffu;
      if (lane < 32) *(LAS v4u*)(L + L_CSF + bl * 512 + lane * 16) = (v4u){hi | (hi << 16), lo, 0, 0};
    }
    const rsrc_t rsWd = mkrsrc(p.wd, 1376u * 1024u);
    const unsigned dsrc = (16 * 43 * (w4 >> 1) + 8 * (w4 & 1)) * 1024 + 128 * cg;
    v4u wreg[16];
    lds_wait_sleep(L + L_CNT + 16, 4);
#pragma unroll
    for (int j = 0; j < 16; ++j) wreg[j] = ld128(rsWd, voffR, dsrc + (27 + j) * 16384);
    BAR();
    BAR();
    GU_LOADW(3, L_GW3)
    pollN<16>(p.ws + WS_F3 + (rt * 4 + w4) * 128, tag, lane);
    STAMPW(4, 8);
    const unsigned hsrc = WS_H + (rt * 4 + w4) * 32 * 1024;
    const unsigned hdst = w4 * 4096;
    const unsigned voffH1 = voffL + 1024 - 0, voffH2 = voffL + 2048, voffH3 = voffL + 3072;
#define GU_LOADH(st, slot)                                           \
  {                                                                  \
    const unsigned l_ = lb + (slot) + hdst, s_ = hsrc + (((st) + kro) & 7) * 4096; \
    dma16o_sc1<0>(l_, voffL, rsWs, s_); dma16o_sc1<1024>(l_, voffH1, rsWs, s_);    \
    dma16o_sc1<2048>(l_, voffH2, rsWs, s_); dma16o_sc1<3072>(l_, voffH3, rsWs, s_); \
  }
    GU_LOADH(0, L_GH0)
    WAITV_BAR(0);
    GU_LOADH(1, L_GH1)
    GU_LOADH(2, L_GH2)
    WAITV_BAR(4);
    GU_LOADH(3, L_GH0)
    v4u sv[8];
    const unsigned svo = WS_ST + ((rt * 4 + w4) * 16 + 8 * hl) * 512 + t32 * 16;
#pragma unroll
    for (int i = 0; i < 8; ++i) sv[i] = ld128_sc1(rsWs, svo, i * 512);
    GU_LOADW(4, L_GW0)
    WAITV_BAR(18);
    GU_LOADH(4, L_GH1)
    GU_LOADW(5, L_GW1)
    asm volatile("s_waitcnt vmcnt(16)" : "+v"(sv[0]), "+v"(sv[1]), "+v"(sv[2]), "+v"(sv[3]), "+v"(sv[4]), "+v"(sv[5]), "+v"(sv[6]), "+v"(sv[7])::"memory");
    {
      bool ok = true;
#pragma unroll
      for (int i = 0; i < 8; ++i) ok &= (sv[i][1] == (unsigned)tag) & (sv[i][3] == (unsigned)tag);
#pragma unroll 1
      for (unsigned it = 0; !__all(ok) && it < SPIN_CAP; ++it) {
        ok = true;
#pragma unroll
        for (int i = 0; i < 8; ++i) {
          sv[i] = __builtin_amdgcn_raw_buffer_load_b128(rsWs, svo, i * 512, 16);
          ok &= (sv[i][1] == (unsigned)tag) & (sv[i][3] == (unsigned)tag);
        }
      }
      float s = 0.f, q = 0.f;
#pragma unroll
      for (int i = 0; i < 8; ++i) {
        s += uf(sv[i][0]);
        q += uf(sv[i][2]);
      }
      swap32(s, q);
      float t = s + q, t2 = t;
      swap32(t, t2);
      const float mean2 = t * (1.f / 512.f);
      const float var = fmaxf(t2 * (1.f / 512.f) - mean2 * mean2, 0.f) + 1e-5f;
      const float nm = -mean2;
      const unsigned hi = pk2(nm, 0.f) & 0xffffu, lo = pk2(nm - uf(hi << 16), 0.f) & 0xffffu;
      if (lane < 32) {
        *(LAS v4u*)(L + L_MF + w4 * 512 + lane * 16) = (v4u){hi | (lo << 16), hi, 0, 0};
        *(LAS float*)(L + L_R2 + (32 * w4 + lane) * 4) = __builtin_amdgcn_rcpf(var);
      }
    }
#define DN_LOAD(i0)                                                                                            \
  {                                                                                                            \
    _Pragma("unroll") for (int j = 0; j < dn_n(i0); ++j)                                                       \
        dma16(lb + dn_base(i0) + ((w4 >> 1) * dn_n(i0) + j) * 2048 + (w4 & 1) * 1024, voffR, rsWd, dsrc + ((i0) + j) * 16384); \
  }
    WAITV_BAR(16);
    GU_LOADH(5, L_GH2)
    GU_LOADW(6, L_GW2)
    WAITV_BAR(16);
    GU_LOADH(6, L_GH0)
    GU_LOADW(7, L_GW3)
    WAITV_BAR(16);
    GU_LOADH(7, L_GH1)
    DN_LOAD(0) DN_LOAD(6)
    WAITV_BAR(17);
    DN_LOAD(7) DN_LOAD(13)
    WAITV_BAR(17);
    DN_LOAD(17) DN_LOAD(23)
#pragma unroll
    for (int j = 0; j < 16; ++j) TIE(wreg[j]);
#define DN_PUT(i0, j0)                                                                                         \
  {                                                                                                            \
    _Pragma("unroll") for (int j = 0; j < dn_n(i0); ++j)                                                       \
        *(LAS v4u*)(L + dn_base(i0) + ((w4 >> 1) * dn_n(i0) + j) * 2048 + (w4 & 1) * 1024 + lane * 16) = wreg[(j0) + j]; \
  }
    BAR();
    DN_PUT(27, 0) DN_PUT(33, 6)
    BAR();
    STAMPW(4, 31);
    WAITV_BAR(0);
    BAR();
    DN_PUT(37, 10)
    BAR();
    BAR();
  } else {
    if (tid < 4) *(LAS unsigned*)(L + L_CNT + tid * 4) = 0;
    if (tid == 4) *(LAS unsigned*)(L + L_CNT + 16) = 0;
    unsigned cdummy = 0;
    {
      u64 pc;
      unsigned back, len;
      asm volatile("s_getpc_b64 %0\n.Lfb_here:\n\ts_mov_b32 %1, .Lfb_here-.Lfb_begin\n\ts_mov_b32 %2, .Lfb_end-.Lfb_begin" : "=s"(pc), "=s"(back), "=s"(len));
      const unsigned o = (jj * 14 + (lane < 14 ? lane : 0)) * 128;
      const unsigned char* q = (const unsigned char*)pc - back + ((int)len > 0 && o + 128 <= len ? o : back);
      asm volatile("global_load_dword %0, %1, off" : "=v"(cdummy) : "v"(q));
    }
    const unsigned t0s = 128 * rt + 8 * m + 2 * w4;
    v4u xs0 = ld128(rsX, t0s * 1024 + lane * 16, 0), xs1 = ld128(rsX, t0s * 1024 + lane * 16, 1024);
    u64 tag;
#define A_STATS()                                                                                                      \
  {                                                                                                                    \
    tag = mix64(wait_epoch(ep));                                                                                       \
    asm volatile("" : "+v"(cdummy), "+v"(xs0), "+v"(xs1));                                                             \
    float s0 = 0.f, q0 = 0.f, s1 = 0.f, q1 = 0.f;                                                                      \
    _Pragma("unroll") for (int e = 0; e < 4; ++e) {                                                                    \
      s0 = dot2(xs0[e], 0x3F803F80u, s0);                                                                              \
      q0 = dot2(xs0[e], xs0[e], q0);                                                                                   \
      s1 = dot2(xs1[e], 0x3F803F80u, s1);                                                                              \
      q1 = dot2(xs1[e], xs1[e], q1);                                                                                   \
    }                                                                                                                  \
    s0 = rowsum16(s0); q0 = rowsum16(q0); s1 = rowsum16(s1); q1 = rowsum16(q1);                                        \
    red4<false>(s0, q0, s1, q1);                                                                                       \
    const float sm = (lane & 1) ? s1 : s0, sq = (lane & 1) ? q1 : q0;                                                  \
    const float mean = sm * (1.f / 512.f);                                                                             \
    const float r = __builtin_amdgcn_rsqf(fmaxf(sq * (1.f / 512.f) - mean * mean, 0.f) + 1e-5f);                       \
    __builtin_amdgcn_raw_buffer_store_b128((v4u){fu(r), (unsigned)tag, fu(-mean * r), (unsigned)tag}, rsWs,            \
                                           lane < 2 ? WS_S1 + (rt * 128 + 8 * m + 2 * w4 + lane) * 16 : 0x7FFFFFF0u, 0, 16); \
    SB();                                                                                                              \
  }
    v4u s1v[2];
    v4u xf[16][2];
    f32x4 ak[4][2];
    const f32x4 z4 = {0, 0, 0, 0};
    f32x2 stt[2];
#pragma unroll
    for (int j = 0; j < 4; ++j) ak[j][0] = ak[j][1] = (f32x4){0, 0, 0, 0};
    {
      const unsigned xr0 = lb + (32 * w4 + l15) * 128;
      const unsigned xlo0 = opaque(xr0 + ((g ^ (l15 >> 1)) << 4)), xlo1 = opaque(xr0 + (((4 + g) ^ (l15 >> 1)) << 4));
      const unsigned xhi0 = opaque(xlo0 + 2 * A_SLOT), xhi1 = opaque(xlo1 + 2 * A_SLOT);
      const unsigned w0lo = opaque(lb + trW0), w1lo = opaque(lb + trW1), w0hi = opaque(w0lo + 2 * A_SLOT), w1hi = opaque(w1lo + 2 * A_SLOT);
      const unsigned clo = opaque(((w4 & 1) ? w1lo : w0lo) + (w4 >> 1) * 4096), chi = opaque(clo + 2 * A_SLOT);
      s16x8 fa[2][4], fc[2];
      f32x4 acs = z4;
#define A1_X(s, a) xf[s][a] = *(LAS v4u*)(L0 + ((((s) >> 1) & 2) ? (((s) & 1) ? xhi1 : xhi0) : (((s) & 1) ? xlo1 : xlo0)) + (((s) >> 1) & 1) * A_SLOT + (a) * 2048); SB()
#define A1_WA(s, j) (((((s) >> 1) & 2) ? (((j) & 1) ? w1hi : w0hi) : (((j) & 1) ? w1lo : w0lo)) + (((s) >> 1) & 1) * A_SLOT + ((j) >> 1) * 4096 + ((s) & 1) * 2048)
#define A1_W(s, j) fa[(s) & 1][j] = cat(trread(L0, A1_WA(s, j)), trread(L0, A1_WA(s, j) + 256)); SB()
#define A1_CA(s) (((((s) >> 1) & 2) ? chi : clo) + (((s) >> 1) & 1) * A_SLOT + ((s) & 1) * 2048)
#define A1_C(s) fc[(s) & 1] = cat(trread(L0, A1_CA(s)), trread(L0, A1_CA(s) + 256)); SB()
#define A1_M(s, j, a)                                           \
  ak[j][a] = mfma16(fa[(s) & 1][j], asfrag(xf[s][a]), ak[j][a]); \
  TIE(ak[j][a]);                                                \
  SB()
#define A1_CM(s)                              \
  acs = mfma16(fc[(s) & 1], ones8, acs);      \
  TIE(acs);                                   \
  SB()
#define A1_ALLM(s)                                                            \
  A1_M(s, 0, 0); A1_M(s, 0, 1); A1_M(s, 1, 0); A1_M(s, 1, 1); A1_M(s, 2, 0); A1_M(s, 2, 1); A1_M(s, 3, 0); A1_M(s, 3, 1); \
  A1_CM(s);
#define A1_RM(sn, sc)                                                         \
  A1_X(sn, 0); A1_M(sc, 0, 0); A1_W(sn, 0); A1_M(sc, 0, 1);                   \
  A1_X(sn, 1); A1_M(sc, 1, 0); A1_W(sn, 1); A1_M(sc, 1, 1);                   \
  A1_W(sn, 2); A1_M(sc, 2, 0); A1_M(sc, 2, 1);                                \
  A1_W(sn, 3); A1_M(sc, 3, 0); A1_M(sc, 3, 1);                                \
  A1_C(sn); A1_CM(sc);                                                        \
  SB();
#define A1_STAGE(st) BAR(); A1_RM(2 * (st), 2 * (st) - 1) A1_RM(2 * (st) + 1, 2 * (st))
      BAR();
      STAMP(14); CSTAMP(52);
      A1_X(0, 0); A1_X(0, 1); A1_W(0, 0); A1_W(0, 1); A1_W(0, 2); A1_W(0, 3); A1_C(0);
      SB();
      A1_RM(1, 0)
      A_STATS()
      A1_STAGE(1)
      A1_STAGE(2) A1_STAGE(3) A1_STAGE(4) A1_STAGE(5)
      s1v[0] = ld128_sc1(rsWs, WS_S1 + (rt * 128 + 32 * w4 + l15) * 16, 0);
      s1v[1] = ld128_sc1(rsWs, WS_S1 + (rt * 128 + 32 * w4 + l15) * 16, 256);
      A1_STAGE(6) A1_STAGE(7)
      A1_ALLM(15)
      if (l15 == 0) *(LAS f32x4*)(L + L_CS + w4 * 64 + g * 16) = acs;
      asm volatile("s_waitcnt vmcnt(0)" : "+v"(s1v[0]), "+v"(s1v[1]));
      {
        bool ok = (s1v[0][1] == (unsigned)tag) & (s1v[0][3] == (unsigned)tag) & (s1v[1][1] == (unsigned)tag) & (s1v[1][3] == (unsigned)tag);
#pragma unroll 1
        for (unsigned it = 0; !__all(ok) && it < SPIN_CAP; ++it) {
          s1v[0] = __builtin_amdgcn_raw_buffer_load_b128(rsWs, WS_S1 + (rt * 128 + 32 * w4 + l15) * 16, 0, 16);
          s1v[1] = __builtin_amdgcn_raw_buffer_load_b128(rsWs, WS_S1 + (rt * 128 + 32 * w4 + l15) * 16, 256, 16);
          ok = (s1v[0][1] == (unsigned)tag) & (s1v[0][3] == (unsigned)tag) & (s1v[1][1] == (unsigned)tag) & (s1v[1][3] == (unsigned)tag);
        }
      }
      stt[0] = (f32x2){uf(s1v[0][0]), uf(s1v[0][2])};
      stt[1] = (f32x2){uf(s1v[1][0]), uf(s1v[1][2])};
    }
    STAMP(1); CSTAMP(53);
    BAR();
    STAMP(26); CSTAMP(54);

    const s16x4 wphi0 = trread(L, L_WPHI + (4 * g + q4) * 32 + 8 * p4);
    const s16x4 wphi1 = trread(L, L_WPHI + 512 + (4 * g + q4) * 32 + 8 * p4);
    {
      f32x4 cs[4];
#pragma unroll
      for (int i = 0; i < 4; ++i) cs[i] = *(LAS f32x4*)(L + L_CS + i * 64 + g * 16);
#pragma unroll
      for (int a = 0; a < 2; ++a) {
        const float rstd = stt[a][0], nb = stt[a][1];
        const f32x4 k0 = ak[0][a] * rstd + cs[0] * nb, k1 = ak[1][a] * rstd + cs[1] * nb;
        const f32x4 v0 = ak[2][a] * rstd + cs[2] * nb, v1 = ak[3][a] * rstd + cs[3] * nb;
        const v2u k0b = {pk2(k0[0], k0[1]), pk2(k0[2], k0[3])}, k1b = {pk2(k1[0], k1[1]), pk2(k1[2], k1[3])};
        const f32x4 p0 = mfma16k16(wphi0, __builtin_bit_cast(s16x4, k0b), z4);
        const f32x4 p1 = mfma16k16(wphi1, __builtin_bit_cast(s16x4, k1b), z4);
        const unsigned wa = L_KVIMG + (32 * w4 + 16 * a + l15) * 32 + 8 * (g ^ q4);
        *(LAS v2u*)(L + wa + 4 * 4096) = (v2u){pk2(v0[0], v0[1]), pk2(v0[2], v0[3])};
        *(LAS v2u*)(L + wa + 5 * 4096) = (v2u){pk2(v1[0], v1[1]), pk2(v1[2], v1[3])};
        v2u pos0, neg0, pos1, neg1;
        dual_softmax(p0, p1, pos0, neg0, pos1, neg1);
        *(LAS v2u*)(L + wa + 0 * 4096) = pos0;
        *(LAS v2u*)(L + wa + 1 * 4096) = neg0;
        *(LAS v2u*)(L + wa + 2 * 4096) = pos1;
        *(LAS v2u*)(L + wa + 3 * 4096) = neg1;
      }
    }
    lds_arrive(L + L_CNT, lane);
    lds_wait(L + L_CNT, 4);
    STAMP(15); CSTAMP(55);
    {
      const unsigned ra = L_KVIMG + (4 * g + q4) * 32 + 8 * (p4 ^ g);
      const unsigned raA = ra + w4 * 4096, raB = ra + (4 + (w4 >> 1)) * 4096;
      f32x4 acc = z4, accz = z4;
      s16x8 fA[4], fB[4];
#pragma unroll
      for (int u = 0; u < 4; ++u) {
        fA[u] = cat(trread(L, raA + u * 1024), trread(L, raA + u * 1024 + 512));
        fB[u] = cat(trread(L, raB + u * 1024), trread(L, raB + u * 1024 + 512));
      }
#pragma unroll
      for (int u = 0; u < 4; ++u) { TIE(fA[u]); TIE(fB[u]); }
      lds_arrive(L + L_CNT + 8, lane);
#pragma unroll
      for (int u = 0; u < 4; ++u) {
        acc = mfma16(fA[u], fB[u], acc);
        accz = mfma16(fA[u], ones8, accz);
      }
      const unsigned base = WS_KVP + (m * 16 + rt) * KVP_STRIDE;
      __builtin_amdgcn_raw_buffer_store_b128(__builtin_bit_cast(v4u, acc), rsWs, base + w4 * 1024 + lane * 16, 0, 16);
      __builtin_amdgcn_raw_buffer_store_b128(__builtin_bit_cast(v4u, accz), rsWs, l15 == 0 ? base + 4096 + w4 * 64 + g * 16 : 0x7FFFFFF0u, 0, 16);
      WAITV(0);
      if (lane == 0) set_flag(p.ws + WS_F1 + m * 512 + (rt * 4 + w4) * 8, tag);
    }
    STAMP(2);

    v4u phq[2][2];
    v2u f1v;
    {
      f32x4 aq[2][2] = {{z4, z4}, {z4, z4}};
      const unsigned trQ0 = opaque(lb + L_WQ - 16384 + trW0), trQ1 = opaque(lb + L_WQ - 16384 + trW1);
      s16x8 fq[2][2];
#define A2_W(s, j) fq[(s) & 1][j] = cat(trread(L0, ((j) ? trQ1 : trQ0) + (s) * 2048), trread(L0, ((j) ? trQ1 : trQ0) + (s) * 2048 + 256)); SB()
#define A2_M(s, j, a)                                           \
  aq[j][a] = mfma16(fq[(s) & 1][j], asfrag(xf[s][a]), aq[j][a]); \
  TIE(aq[j][a]);                                                \
  SB()
#define A2_STEP(s)                                \
  if ((s) < 15) { A2_W(((s) + 1) & 15, 0); }      \
  A2_M(s, 0, 0); A2_M(s, 0, 1);                   \
  if ((s) < 15) { A2_W(((s) + 1) & 15, 1); }      \
  A2_M(s, 1, 0); A2_M(s, 1, 1);                   \
  SB();
      A2_W(0, 0); A2_W(0, 1);
      SB();
      A2_STEP(0) A2_STEP(1) A2_STEP(2) A2_STEP(3) A2_STEP(4) A2_STEP(5) A2_STEP(6) A2_STEP(7)
      A2_STEP(8) A2_STEP(9) A2_STEP(10) A2_STEP(11) A2_STEP(12) A2_STEP(13) A2_STEP(14) A2_STEP(15)
      lds_arrive(L + L_CNT + 12, lane);
      STAMP(27);
      lds_wait(L + L_CNT + 4, 4);
      const f32x4 c0 = *(LAS f32x4*)(L + L_CS + 256 + g * 16) + *(LAS f32x4*)(L + L_CS + 320 + g * 16);
      const f32x4 c1 = *(LAS f32x4*)(L + L_CS + 384 + g * 16) + *(LAS f32x4*)(L + L_CS + 448 + g * 16);
#pragma unroll
      for (int a = 0; a < 2; ++a) {
        const f32x4 q0 = aq[0][a] * stt[a][0] + c0 * stt[a][1], q1 = aq[1][a] * stt[a][0] + c1 * stt[a][1];
        const v2u q0b = {pk2(q0[0], q0[1]), pk2(q0[2], q0[3])}, q1b = {pk2(q1[0], q1[1]), pk2(q1[2], q1[3])};
        const f32x4 p0 = mfma16k16(wphi0, __builtin_bit_cast(s16x4, q0b), z4);
        const f32x4 p1 = mfma16k16(wphi1, __builtin_bit_cast(s16x4, q1b), z4);
        v2u pos0, neg0, pos1, neg1;
        dual_softmax(p0, p1, pos0, neg0, pos1, neg1);
        phq[a][0] = (v4u){pos0[0], pos0[1], neg0[0], neg0[1]};
        phq[a][1] = (v4u){pos1[0], pos1[1], neg1[0], neg1[1]};
        if (a == 0) {
          TIE(phq[0][0]);
          TIE(phq[0][1]);
          asm volatile("global_load_dwordx2 %0, %1, off sc1" : "=v"(f1v) : "v"(p.ws + WS_F1 + m * 512 + lane * 8));
          SB();
        }
      }
    }
    STAMP(3);

    {
      TIE(phq[1][0]);
      TIE(phq[1][1]);
      asm volatile("s_waitcnt vmcnt(0)" : "+v"(f1v)::"memory");
      if (!__all(f1v[0] == (unsigned)tag && f1v[1] == (unsigned)(tag >> 32))) pollN<64>(p.ws + WS_F1 + m * 512, tag, lane);
      STAMP(4);
      const unsigned base = WS_KVP + m * 16 * KVP_STRIDE;
      v4u pv[16], zv[4];
#pragma unroll
      for (int i = 0; i < 4; ++i) zv[i] = __builtin_amdgcn_raw_buffer_load_b128(rsWs, base + g * KVP_STRIDE + 4096 + l15 * 16, 4 * i * KVP_STRIDE, 16);
#pragma unroll
      for (int i = 0; i < 16; ++i) pv[i] = __builtin_amdgcn_raw_buffer_load_b128(rsWs, base + tid * 16, i * KVP_STRIDE, 16);
      f32x4 sz = __builtin_bit_cast(f32x4, zv[0]);
#pragma unroll
      for (int i = 1; i < 4; ++i) sz += __builtin_bit_cast(f32x4, zv[i]);
      float z0 = sz[0], z1 = sz[1], z2 = sz[2], z3 = sz[3];
      red4<false>(z0, z1, z2, z3);
      if (tid < 16) *(LAS v2u*)(L + L_ZF + (l15 >> 3) * 64 + (l15 & 3) * 16 + ((l15 >> 2) & 1) * 8) = (v2u){pk2(z0, z1), pk2(z2, z3)};
      f32x4 s = __builtin_bit_cast(f32x4, pv[0]);
#pragma unroll
      for (int i = 1; i < 16; ++i) s += __builtin_bit_cast(f32x4, pv[i]);
      *(LAS v2u*)(L + L_KF + (w >> 1) * 1024 + lane * 16 + (w & 1) * 8) = (v2u){pk2(s[0], s[1]), pk2(s[2], s[3])};
    }
    STAMP(33);
    BAR();
    {
      const s16x8 kf0 = *(LAS s16x8*)(L + L_KF + lane * 16), kf1 = *(LAS s16x8*)(L + L_KF + 1024 + lane * 16);
      const s16x8 zf0 = *(LAS s16x8*)(L + L_ZF + g * 16), zf1 = *(LAS s16x8*)(L + L_ZF + 64 + g * 16);
#pragma unroll
      for (int a = 0; a < 2; ++a) {
        const f32x4 n0 = mfma16(kf0, asfrag(phq[a][0]), z4), d0 = mfma16(zf0, asfrag(phq[a][0]), z4);
        const f32x4 n1 = mfma16(kf1, asfrag(phq[a][1]), z4), d1 = mfma16(zf1, asfrag(phq[a][1]), z4);
        const float i0 = __builtin_amdgcn_rcpf(d0[0] + 1e-6f), i1 = __builtin_amdgcn_rcpf(d1[0] + 1e-6f);
        const v4u ov = {pk2(n0[0] * i0, n0[1] * i0), pk2(n0[2] * i0, n0[3] * i0), pk2(n1[0] * i1, n1[1] * i1), pk2(n1[2] * i1, n1[3] * i1)};
        __builtin_amdgcn_raw_buffer_store_b128(ov, rsWs, WS_O + ((rt * 4 + w4) * 32 + 2 * m + (g >> 1)) * 1024 + ((g & 1) * 32 + 16 * a + l15) * 16, 0, 16);
      }
      WAITV(0);
      if (lane < 2) set_flag(p.ws + WS_F2 + ((rt * 4 + w4) * 2 + hf) * 128 + (lane * 8 + cg) * 8, tag);
    }
    STAMP(5);

    const int tb32 = 2 * hf + (w & 1), kh = (w >> 1) & 1;
    unsigned hres[8];
    const unsigned tok = 128 * rt + 32 * tb32 + t32;
    v2u xr[4];
#pragma unroll
    for (int i = 0; i < 4; ++i) xr[i] = ld64(rsX, tok * 1024 + (64 * cg + 32 * kh + 4 * hl) * 2, i * 16);
    const unsigned relR = (4 * (g >> 1) + q4) * 128 + (g & 1) * 32 + 8 * p4;
    const unsigned rel0 = lb + relR + ((0 ^ (q4 >> 1)) << 6), rel1 = lb + relR + ((1 ^ (q4 >> 1)) << 6);
    f32x16 a0, a1;
#pragma unroll
    for (int i = 0; i < 16; ++i) { a0[i] = 0.f; a1[i] = 0.f; }
    {
      const unsigned rl0 = opaque(rel0 + kh * 32768), rl1 = opaque(rel1 + kh * 32768);
      s16x8 fw[2][2];
#define WO_RD1(i, c)                                                                                    \
  {                                                                                                     \
    constexpr unsigned o_ = (32 * ((i) >> 1) + 8 * ((i) & 1)) * 128;                                    \
    fw[(i) & 1][c] = cat(trread(L0, ((c) ? rl1 : rl0) + o_), trread(L0, ((c) ? rl1 : rl0) + o_ + 2048)); \
    SB();                                                                                               \
  }
#define WO_RD(i) WO_RD1(i, 0) WO_RD1(i, 1)
      WO_RD(0)
      pollN<16>(p.ws + WS_F2 + ((rt * 4 + tb32) * 2 + kh) * 128, tag, lane);
      STAMP(6);
      v4u of[16];
      const unsigned ov = WS_O + ((rt * 4 + tb32) * 32 + 16 * kh) * 1024 + lane * 16;
#pragma unroll
      for (int i = 0; i < 16; ++i) of[i] = ld128_sc1(rsWs, ov, i * 1024);
      lds_arrive(L + L_CNT + 16, lane);
#define WO_STEP(i)                                          \
  {                                                         \
    if ((i) < 15) WO_RD1(((i) + 1) & 15, 0)                 \
    waitv_tie<15 - (i)>(of[i]);                             \
    a0 = mfma32(fw[(i) & 1][0], asfrag(of[i]), a0);         \
    TIE(a0);                                                \
    SB();                                                   \
    if ((i) < 15) WO_RD1(((i) + 1) & 15, 1)                 \
    a1 = mfma32(fw[(i) & 1][1], asfrag(of[i]), a1);         \
    TIE(a1);                                                \
    SB();                                                   \
  }
      WO_STEP(0) WO_STEP(1) WO_STEP(2) WO_STEP(3) WO_STEP(4) WO_STEP(5) WO_STEP(6) WO_STEP(7)
      WO_STEP(8) WO_STEP(9) WO_STEP(10) WO_STEP(11) WO_STEP(12) WO_STEP(13) WO_STEP(14) WO_STEP(15)
    }
    STAMP(16);
    {
      const f32x16 give = kh ? a0 : a1;
      f32x16 keep = kh ? a1 : a0;
#pragma unroll
      for (int i = 0; i < 4; ++i)
        *(LAS f32x4*)(L + L_RED_WO + (w * 4 + i) * 1024 + lane * 16) = (f32x4){give[4 * i], give[4 * i + 1], give[4 * i + 2], give[4 * i + 3]};
      BAR();
      float ss = 0.f, sq = 0.f;
#pragma unroll
      for (int i = 0; i < 4; ++i) {
        const f32x4 o = *(LAS f32x4*)(L + L_RED_WO + ((w ^ 2) * 4 + i) * 1024 + lane * 16);
        TIE(xr[i]);
        hres[2 * i] = pk2(keep[4 * i] + o[0] + bflo(xr[i][0]), keep[4 * i + 1] + o[1] + bfhi(xr[i][0]));
        hres[2 * i + 1] = pk2(keep[4 * i + 2] + o[2] + bflo(xr[i][1]), keep[4 * i + 3] + o[3] + bfhi(xr[i][1]));
      }
      const int Sg = 2 * cg + kh;
      const unsigned ho = WS_H + ((rt * 4 + tb32) * 32 + 2 * Sg) * 1024 + lane * 16;
      __builtin_amdgcn_raw_buffer_store_b128((v4u){hres[0], hres[1], hres[2], hres[3]}, rsWs, ho, 0, 16);
      __builtin_amdgcn_raw_buffer_store_b128((v4u){hres[4], hres[5], hres[6], hres[7]}, rsWs, ho + 1024, 0, 16);
      WAITV(0);
      if (lane == 0) set_flag(p.ws + WS_F3 + (rt * 4 + tb32) * 128 + Sg * 8, tag);
      STAMP(36);
#pragma unroll
      for (int i = 0; i < 8; ++i) {
        const float lo = bflo(hres[i]), hi = bfhi(hres[i]);
        ss += lo + hi;
        sq = fmaf(lo, lo, fmaf(hi, hi, sq));
      }
      swap32(ss, sq);
      __builtin_amdgcn_raw_buffer_store_b64((v2u){fu(ss + sq), (unsigned)tag}, rsWs, WS_ST + ((rt * 4 + tb32) * 16 + Sg) * 512 + t32 * 16 + hl * 8, 0, 16);
    }
    BAR();
    STAMP(7);

    const int mh = w & 1, nh = (w >> 1) & 1;
    f32x16 acc[2][3];
#pragma unroll
    for (int i = 0; i < 16; ++i) {
      acc[0][0][i] = 0.f; acc[0][1][i] = 0.f; acc[0][2][i] = 0.f;
      acc[1][0][i] = 0.f; acc[1][1][i] = 0.f; acc[1][2][i] = 0.f;
    }
    {
      const unsigned relT = lb + mh * 8192 + lane * 16;
      const unsigned relW = lb + (p4 >> 1) * GU_UPOFF + (4 * (g >> 1) + q4) * 192 + 16 * (g & 1) + 8 * (p4 & 1) + 96 * nh;
      v4u tf[2][2];
      s16x8 ga[2][3];
      tf[1][0] = tf[1][1] = (v4u){0, 0, 0, 0};
      ga[1][0] = ga[1][1] = ga[1][2] = asfrag(tf[1][0]);
#define GU_RT(j, tt) tf[(j) & 1][tt] = *(LAS v4u*)(L0 + aT + (tt) * 4096 + (j) * 1024); SB()
#define GU_RW(j, l3) ga[(j) & 1][l3] = cat(trread(L0, aW + (j) * 16 * 192 + 32 * (l3)), trread(L0, aW + ((j) * 16 + 8) * 192 + 32 * (l3))); SB()
#define GU_M(j, tt, l3)                                                       \
  acc[tt][l3] = mfma32(ga[(j) & 1][l3], asfrag(tf[(j) & 1][tt]), acc[tt][l3]); \
  TIE(acc[tt][l3]);                                                            \
  SB()
#define GU_STEP(jn, jc)            \
  GU_RT(jn, 0);                    \
  GU_M(jc, 0, 0);                  \
  GU_RW(jn, 0);                    \
  GU_M(jc, 0, 1);                  \
  GU_RW(jn, 1);                    \
  GU_M(jc, 0, 2);                  \
  GU_RT(jn, 1);                    \
  GU_M(jc, 1, 0);                  \
  GU_RW(jn, 2);                    \
  GU_M(jc, 1, 1);                  \
  GU_M(jc, 1, 2);                  \
  SB();
      const bool real = 6 * m + 3 * nh < 86;
      const unsigned aT0 = opaque(relT + L_GH0), aT1 = opaque(relT + L_GH1), aT2 = opaque(relT + L_GH2);
      const unsigned aW0 = opaque(relW + L_GW0), aW1 = opaque(relW + L_GW1), aW2 = opaque(relW + L_GW2), aW3 = opaque(relW + L_GW3);
#define GU_STAGE(T_, W_)  \
  BAR();                  \
  if (real) {             \
    const unsigned aT = T_, aW = W_; \
    GU_STEP(0, 3)         \
    GU_STEP(1, 0)         \
    GU_STEP(2, 1)         \
    GU_STEP(3, 2)         \
  }
      BAR();
      STAMP(17);
      if (real) {
        const unsigned aT = aT0, aW = aW0;
        GU_RT(0, 0); GU_RW(0, 0); GU_RW(0, 1); GU_RT(0, 1); GU_RW(0, 2);
        GU_STEP(1, 0)
        GU_STEP(2, 1)
        GU_STEP(3, 2)
      }
      GU_STAGE(aT1, aW1)
      STAMP(18);
      GU_STAGE(aT2, aW2)
      GU_STAGE(aT0, aW3)
      GU_STAGE(aT1, aW0)
      GU_STAGE(aT2, aW1)
      GU_STAGE(aT0, aW2)
      STAMP(24);
      GU_STAGE(aT1, aW3)
      const unsigned zo = lb + L_ZERO;
      const unsigned aC = lane < 32 ? lb + L_CSF + 3 * nh * 512 + lane * 16 : zo - 0, aM = lane < 32 ? lb + L_MF + 2 * mh * 512 + lane * 16 : zo;
      const unsigned stp = lane < 32 ? 512 : 0;
      tf[0][0] = *(LAS v4u*)(L0 + aM);
      tf[0][1] = *(LAS v4u*)(L0 + aM + stp);
      ga[0][0] = *(LAS s16x8*)(L0 + aC);
      ga[0][1] = *(LAS s16x8*)(L0 + aC + stp);
      ga[0][2] = *(LAS s16x8*)(L0 + aC + 2 * stp);
      GU_M(3, 0, 0); GU_M(3, 0, 1); GU_M(3, 0, 2); GU_M(3, 1, 0); GU_M(3, 1, 1); GU_M(3, 1, 2);
      GU_M(0, 0, 0); GU_M(0, 0, 1); GU_M(0, 0, 2); GU_M(0, 1, 0); GU_M(0, 1, 1); GU_M(0, 1, 2);
    }
    const float r2a = *(LAS float*)(L + L_R2 + (64 * mh + t32) * 4), r2b = *(LAS float*)(L + L_R2 + (64 * mh + 32 + t32) * 4);
    BAR();
    STAMP(9);
#pragma unroll
    for (int tt = 0; tt < 2; ++tt) {
      const float r2 = tt ? r2b : r2a;
#pragma unroll
      for (int l3 = 0; l3 < 3; ++l3) {
        const int fblk = 6 * m + 3 * nh + l3;
        float hv[8];
#pragma unroll
        for (int i = 0; i < 2; ++i)
#pragma unroll
          for (int e = 0; e < 4; ++e) hv[4 * i + e] = (acc[tt][l3][8 * i + e] * r2) * acc[tt][l3][8 * i + 4 + e];
        const v4u o = {pk2(hv[0], hv[1]), pk2(hv[2], hv[3]), pk2(hv[4], hv[5]), pk2(hv[6], hv[7])};
        const unsigned off = fblk < 86 ? WS_HH + ((rt * 4 + 2 * mh + tt) * 86 + fblk) * 1024 + lane * 16 : 0x7FFFFFF0u;
        __builtin_amdgcn_raw_buffer_store_b128(o, rsWs, off, 0, 16);
      }
    }
    asm volatile("s_barrier" ::: "memory");
    WAITV(0);
    if (lane < 2) set_flag(p.ws + WS_F4 + (rt * 4 + 2 * mh + lane) * 256 + (m * 2 + nh) * 8, tag);

    STAMP(10);

    const unsigned kh2048 = kh * 2048;
    const unsigned pb0 = opaque(rel0 + dn_base(0) + dn_n(0) * kh2048), pb1 = opaque(rel1 + dn_base(0) + dn_n(0) * kh2048);
    BAR();
    s16x8 wpre[DN_PRE][2];
#pragma unroll
    for (int i = 0; i < DN_PRE; ++i) {
      wpre[i][0] = cat(trread(L0, pb0 + i * 2048), trread(L0, pb0 + i * 2048 + 1024));
      wpre[i][1] = cat(trread(L0, pb1 + i * 2048), trread(L0, pb1 + i * 2048 + 1024));
    }
#pragma unroll
    for (int i = 0; i < DN_PRE; ++i) { TIE(wpre[i][0]); TIE(wpre[i][1]); }
    BAR();
    STAMP(25);
    pollN<32>(p.ws + WS_F4 + (rt * 4 + tb32) * 256, tag, lane);
    STAMP(11); CSTAMP(56);
    v4u hf_[DN_RING];
    const unsigned hv = WS_HH + ((rt * 4 + tb32) * 86 + 43 * kh) * 1024 + lane * 16;
#pragma unroll
    for (int i = 0; i < DN_RING; ++i) hf_[i] = ld128_sc1(rsWs, hv, i * 1024);
#pragma unroll
    for (int i = 0; i < 16; ++i) { a0[i] = 0.f; a1[i] = 0.f; }
    s16x8 fw[2][2];
    DnStep<0>::run(hf_, wpre, fw, a0, a1, rel0, rel1, kh2048, pb0, pb1, rsWs, hv);
    STAMP(12); CSTAMP(57);
    {
      int tz = threadIdx.x;
      asm volatile("" : "+v"(tz));
      const int lane = tz & 63, hl = lane >> 5, t32 = lane & 31;
      const f32x16 give = kh ? a0 : a1;
      const f32x16 keep = kh ? a1 : a0;
#pragma unroll
      for (int i = 0; i < 4; ++i)
        *(LAS f32x4*)(L + L_RED_DN + (w * 4 + i) * 1024 + lane * 16) = (f32x4){give[4 * i], give[4 * i + 1], give[4 * i + 2], give[4 * i + 3]};
      BAR();
      unsigned ob[8];
#pragma unroll
      for (int i = 0; i < 4; ++i) {
        const f32x4 o = *(LAS f32x4*)(L + L_RED_DN + ((w ^ 2) * 4 + i) * 1024 + lane * 16);
        ob[2 * i] = pk2(keep[4 * i] + o[0] + bflo(hres[2 * i]), keep[4 * i + 1] + o[1] + bfhi(hres[2 * i]));
        ob[2 * i + 1] = pk2(keep[4 * i + 2] + o[2] + bflo(hres[2 * i + 1]), keep[4 * i + 3] + o[3] + bfhi(hres[2 * i + 1]));
      }
      const rsrc_t rsOut = mkrsrc(p.out, 2048u * 1024u);
      const unsigned oo = (128 * rt + 32 * tb32 + t32) * 1024 + (64 * cg + 32 * kh + 8 * hl) * 2;
#pragma unroll
      for (int pr = 0; pr < 2; ++pr) {
        swap32u(ob[4 * pr], ob[4 * pr + 2]);
        swap32u(ob[4 * pr + 1], ob[4 * pr + 3]);
        __builtin_amdgcn_raw_buffer_store_b128((v4u){ob[4 * pr], ob[4 * pr + 1], ob[4 * pr + 2], ob[4 * pr + 3]}, rsOut, oo + pr * 32, 0, 0);
      }
    }
    if (b == 0 && tid == 0) __hip_atomic_store((GAS u64*)(p.ws + WS_EPOCH), tag, __ATOMIC_RELAXED, __HIP_MEMORY_SCOPE_AGENT);
    STAMP(13);
    STAMP_FLUSH();
    asm volatile(".Lfb_end:");
  }
}

}

extern "C" void kernel_launch(void* const* d_in, const int* in_sizes, int n_in, void* d_out, int out_size, void* d_ws, size_t ws_size,
                              hipStream_t stream) {
  Params p;
  p.x = d_in[0];
  p.wqkv = d_in[1];
  p.wphi = d_in[2];
  p.wo = d_in[3];
  p.wg = d_in[5];
  p.wu = d_in[6];
  p.wd = d_in[7];
  p.out = d_out;
  p.ws = (unsigned char*)d_ws;
  fused_block_kernel<<<256, 512, 0, stream>>>(p);
}
```

```cpp
#include <hip/hip_runtime.h>
#include <hip/hip_bf16.h>
#include <stdint.h>

#define GAS __attribute__((address_space(1)))
#define LAS __attribute__((address_space(3)))
typedef unsigned v4u __attribute__((ext_vector_type(4)));
typedef unsigned v2u __attribute__((ext_vector_type(2)));
typedef float f32x2 __attribute__((ext_vector_type(2)));
typedef float f32x4 __attribute__((ext_vector_type(4)));
typedef float f32x16 __attribute__((ext_vector_type(16)));
typedef short s16x4 __attribute__((ext_vector_type(4)));
typedef short s16x8 __attribute__((ext_vector_type(8)));
typedef __bf16 bf16x2 __attribute__((ext_vector_type(2)));
typedef __amdgpu_buffer_rsrc_t rsrc_t;
typedef unsigned long long u64;

#define SB() __builtin_amdgcn_sched_barrier(0)

namespace {

constexpr unsigned WS_EPOCH = 0;
constexpr unsigned WS_F1 = 4096;
constexpr unsigned WS_F5 = 12288;
constexpr unsigned WS_F2 = 20480;
constexpr unsigned WS_F3 = 36864;
constexpr unsigned WS_F4 = 45056;
constexpr unsigned WS_KVP = 65536;
constexpr unsigned KVP_STRIDE = 4352;
constexpr unsigned WS_CSP = WS_KVP + 256 * KVP_STRIDE;
constexpr unsigned WS_ST = WS_CSP + 256 * 768;
constexpr unsigned ST_BYTES = 16 * 4 * 16 * 512;
constexpr unsigned WS_S1 = WS_ST + ST_BYTES;
constexpr unsigned WS_O = WS_S1 + 16 * 128 * 16;
constexpr unsigned O_BYTES = 16 * 4 * 32 * 1024;
constexpr unsigned WS_H = WS_O + O_BYTES;
constexpr unsigned H_BYTES = 16 * 4 * 32 * 1024;
constexpr unsigned WS_HH = WS_H + H_BYTES;
constexpr unsigned HH_BYTES = 16 * 4 * 86 * 1024;
constexpr unsigned WS_END = WS_HH + HH_BYTES;

constexpr unsigned A_SLOT = 24576;
constexpr unsigned L_WQ = 98304;
constexpr unsigned L_CSW = 131072;
constexpr unsigned L_KVIMG = 65536;
constexpr unsigned L_WO = 0;
constexpr unsigned GU_UPOFF = 12320, GU_SLOT = 24640;
constexpr unsigned L_GW0 = 65536, L_GW1 = L_GW0 + GU_SLOT, L_GW2 = L_GW1 + GU_SLOT, L_GW3 = 0;
constexpr unsigned L_GH0 = 24640, L_GH1 = 41024, L_GH2 = 139456, L_SPARE = 57408;
constexpr unsigned L_RED_WO = 139456;
constexpr unsigned L_RED_DN = L_GW1;
constexpr unsigned L_MISC = 155840;
constexpr unsigned L_CS = L_MISC;
constexpr unsigned L_WPHI = L_MISC + 1024;
constexpr unsigned L_TOUCH = L_MISC + 2048;
constexpr unsigned L_TOUCH2 = L_MISC + 512;
constexpr unsigned L_CSF = L_MISC;
constexpr unsigned L_KF = L_MISC + 3072;
constexpr unsigned L_MF = L_MISC + 3072;
constexpr unsigned L_R2 = L_MISC + 5120;
constexpr unsigned L_ZERO = L_MISC + 7808;
constexpr unsigned L_CNT = L_MISC + 7824;
constexpr unsigned LDS_BYTES = 163840;
static_assert(L_GW2 + GU_SLOT == L_GH2, "map");
static_assert(L_GH2 + 16384 == L_MISC, "map");
static_assert(L_ZERO + 16 <= LDS_BYTES, "map");
static_assert(L_CSW + 4 * 4608 <= L_MISC, "map");

constexpr unsigned SPIN_CAP = 1u << 18;
constexpr float LOG2E = 1.4426950408889634f;

struct Params {
  const void* x;
  const void* wqkv;
  const void* wphi;
  const void* wo;
  const void* wg;
  const void* wu;
  const void* wd;
  void* out;
  unsigned char* ws;
};

#define DI __device__ __forceinline__

DI rsrc_t mkrsrc(const void* p, unsigned bytes) { return __builtin_amdgcn_make_buffer_rsrc((void*)p, 0, (int)bytes, 0x00020000); }
DI void dma16(unsigned ldsaddr, unsigned voff, rsrc_t rs, unsigned soff) {
  asm volatile("s_mov_b32 m0, %0\n\ts_nop 0\n\tbuffer_load_dwordx4 %1, %2, %3 offen lds" ::"s"(ldsaddr), "v"(voff), "s"(rs), "s"(soff) : "memory");
}
template <unsigned OFF> DI void dma16o(unsigned ldsbase, unsigned voff, rsrc_t rs, unsigned soff) {
  asm volatile("s_add_u32 m0, %0, %4\n\ts_nop 0\n\tbuffer_load_dwordx4 %1, %2, %3 offen lds" ::"s"(ldsbase), "v"(voff), "s"(rs), "s"(soff), "n"(OFF) : "memory", "scc");
}
template <unsigned OFF> DI void dma16o_sc1(unsigned ldsbase, unsigned voff, rsrc_t rs, unsigned soff) {
  asm volatile("s_add_u32 m0, %0, %4\n\ts_nop 0\n\tbuffer_load_dwordx4 %1, %2, %3 offen sc1 lds" ::"s"(ldsbase), "v"(voff), "s"(rs), "s"(soff), "n"(OFF) : "memory", "scc");
}
DI void touch_global(unsigned ldsaddr, const unsigned char* q) {
  asm volatile("s_mov_b32 m0, %0\n\ts_nop 0\n\tglobal_load_lds_dword %1, off" ::"s"(ldsaddr), "v"(q) : "memory");
}
DI v4u ld128(rsrc_t rs, unsigned voff, unsigned soff) {
  v4u r;
  asm volatile("buffer_load_dwordx4 %0, %1, %2, %3 offen" : "=v"(r) : "v"(voff), "s"(rs), "s"(soff));
  return r;
}
DI v4u ld128_sc1(rsrc_t rs, unsigned voff, unsigned soff) {
  v4u r;
  asm volatile("buffer_load_dwordx4 %0, %1, %2, %3 offen sc1" : "=v"(r) : "v"(voff), "s"(rs), "s"(soff));
  return r;
}
DI v2u ld64(rsrc_t rs, unsigned voff, unsigned soff) {
  v2u r;
  asm volatile("buffer_load_dwordx2 %0, %1, %2, %3 offen" : "=v"(r) : "v"(voff), "s"(rs), "s"(soff));
  return r;
}
#define WAITV(n) asm volatile("s_waitcnt vmcnt(" #n ")" ::: "memory")
#define BAR() asm volatile("s_waitcnt lgkmcnt(0)\n\ts_barrier" ::: "memory")
#define WAITV_BAR(n) asm volatile("s_waitcnt vmcnt(" #n ") lgkmcnt(0)\n\ts_barrier" ::: "memory")
#define TIE(x) asm volatile("" : "+v"(x))

DI s16x4 trread(LAS unsigned char* L, unsigned off) { return __builtin_amdgcn_ds_read_tr16_b64_v4i16((LAS s16x4*)(L + off)); }
DI s16x8 cat(s16x4 a, s16x4 b) { return __builtin_shufflevector(a, b, 0, 1, 2, 3, 4, 5, 6, 7); }
DI s16x8 asfrag(v4u v) { return __builtin_bit_cast(s16x8, v); }
DI f32x4 mfma16(s16x8 a, s16x8 b, f32x4 c) { return __builtin_amdgcn_mfma_f32_16x16x32_bf16(a, b, c, 0, 0, 0); }
DI f32x16 mfma32(s16x8 a, s16x8 b, f32x16 c) { return __builtin_amdgcn_mfma_f32_32x32x16_bf16(a, b, c, 0, 0, 0); }
DI unsigned pk2(float lo, float hi) {
  f32x2 v = {lo, hi};
  return __builtin_bit_cast(unsigned, __builtin_convertvector(v, bf16x2));
}
DI float dot2(unsigned a, unsigned b, float c) {
  return __builtin_amdgcn_fdot2_f32_bf16(__builtin_bit_cast(bf16x2, a), __builtin_bit_cast(bf16x2, b), c, false);
}
DI float bflo(unsigned u) { return __builtin_bit_cast(float, u << 16); }
DI float bfhi(unsigned u) { return __builtin_bit_cast(float, u & 0xffff0000u); }
DI unsigned fu(float f) { return __builtin_bit_cast(unsigned, f); }
DI float uf(unsigned u) { return __builtin_bit_cast(float, u); }
DI void swap16(float& a, float& b) {
  auto r = __builtin_amdgcn_permlane16_swap(fu(a), fu(b), false, false);
  a = uf(r[0]);
  b = uf(r[1]);
}
DI void swap32(float& a, float& b) {
  auto r = __builtin_amdgcn_permlane32_swap(fu(a), fu(b), false, false);
  a = uf(r[0]);
  b = uf(r[1]);
}
DI void swap32u(unsigned& a, unsigned& b) {
  auto r = __builtin_amdgcn_permlane32_swap(a, b, false, false);
  a = r[0];
  b = r[1];
}
DI float rowsum16(float v) {
  v += uf(__builtin_amdgcn_update_dpp(0, (int)fu(v), 0x128, 0xf, 0xf, false));
  v += uf(__builtin_amdgcn_update_dpp(0, (int)fu(v), 0x124, 0xf, 0xf, false));
  v += uf(__builtin_amdgcn_update_dpp(0, (int)fu(v), 0x122, 0xf, 0xf, false));
  v += uf(__builtin_amdgcn_update_dpp(0, (int)fu(v), 0x121, 0xf, 0xf, false));
  return v;
}
DI void red4add(float& a, float& b, float& c, float& d) {
  swap16(a, b);
  float m1 = a + b;
  swap16(c, d);
  float m2 = c + d;
  swap32(m1, m2);
  float t = m1 + m2, t2 = t;
  swap32(t, t2);
  float y1 = t, y1b = t, y2 = t2, y2b = t2;
  swap16(y1, y1b);
  swap16(y2, y2b);
  a = y1;
  b = y1b;
  c = y2;
  d = y2b;
}
DI void dual_softmax16(const f32x16& p, unsigned (&pos)[8], unsigned (&neg)[8]) {
  float mx = fmaxf(fmaxf(p[0], p[1]), p[2]), mn = fminf(fminf(p[0], p[1]), p[2]);
#pragma unroll
  for (int i = 3; i < 15; i += 2) {
    mx = fmaxf(fmaxf(mx, p[i]), p[i + 1]);
    mn = fminf(fminf(mn, p[i]), p[i + 1]);
  }
  mx = fmaxf(mx, p[15]);
  mn = fminf(mn, p[15]);
  const float a = -mx * LOG2E, b = mn * LOG2E;
  float ep[16], en[16];
#pragma unroll
  for (int i = 0; i < 16; ++i) {
    ep[i] = __builtin_amdgcn_exp2f(fmaf(p[i], LOG2E, a));
    en[i] = __builtin_amdgcn_exp2f(fmaf(p[i], -LOG2E, b));
  }
  f32x2 sp = {ep[0], ep[1]}, sn = {en[0], en[1]};
#pragma unroll
  for (int i = 2; i < 16; i += 2) {
    sp += (f32x2){ep[i], ep[i + 1]};
    sn += (f32x2){en[i], en[i + 1]};
  }
  const float ip = __builtin_amdgcn_rcpf(sp[0] + sp[1]), in = __builtin_amdgcn_rcpf(sn[0] + sn[1]);
#pragma unroll
  for (int i = 0; i < 8; ++i) {
    pos[i] = pk2(ep[2 * i] * ip, ep[2 * i + 1] * ip);
    neg[i] = pk2(en[2 * i] * in, en[2 * i + 1] * in);
  }
}
DI u64 wait_epoch(v2u ep) {
  unsigned lo, hi;
  asm volatile("s_waitcnt vmcnt(0)\n\tv_readfirstlane_b32 %0, %2\n\tv_readfirstlane_b32 %1, %3" : "=s"(lo), "=s"(hi) : "v"(ep[0]), "v"(ep[1]) : "memory");
  return ((u64)hi << 32) | lo;
}
DI u64 mix64(u64 z) {
  z += 0x9E3779B97F4A7C15ull;
  z = (z ^ (z >> 30)) * 0xBF58476D1CE4E5B9ull;
  z = (z ^ (z >> 27)) * 0x94D049BB133111EBull;
  return z ^ (z >> 31);
}
template <int N> DI void pollN(const unsigned char* base, u64 tag, int lane) {
  const GAS u64* fl = (const GAS u64*)base;
#pragma unroll 1
  for (unsigned it = 0; it < SPIN_CAP; ++it) {
    const u64 v = __hip_atomic_load(fl + (lane & (N - 1)), __ATOMIC_RELAXED, __HIP_MEMORY_SCOPE_AGENT);
    if (__all(v == tag)) break;
  }
  asm volatile("" ::: "memory");
}
DI void set_flag(unsigned char* f, u64 tag) { __hip_atomic_store((GAS u64*)f, tag, __ATOMIC_RELAXED, __HIP_MEMORY_SCOPE_AGENT); }

DI unsigned opaque(unsigned v) {
  asm volatile("" : "+v"(v));
  return v;
}
template <int N> DI void waitv_tie(v4u& x) { asm volatile("s_waitcnt vmcnt(%1)" : "+v"(x) : "n"(N)); }
DI void lds_arrive(LAS unsigned char* c, int lane) {
  asm volatile("s_waitcnt lgkmcnt(0)" ::: "memory");
  if (lane == 0) __hip_atomic_fetch_add((LAS unsigned*)c, 1u, __ATOMIC_RELAXED, __HIP_MEMORY_SCOPE_WORKGROUP);
}
DI void lds_wait(LAS unsigned char* c, unsigned n) {
#pragma unroll 1
  for (unsigned it = 0; it < SPIN_CAP; ++it) {
    const unsigned v = __hip_atomic_load((LAS unsigned*)c, __ATOMIC_RELAXED, __HIP_MEMORY_SCOPE_WORKGROUP);
    if (__builtin_amdgcn_readfirstlane(v) >= n) break;
  }
  asm volatile("" ::: "memory");
}

DI void lds_wait_sleep(LAS unsigned char* c, unsigned n) {
#pragma unroll 1
  for (unsigned it = 0; it < SPIN_CAP; ++it) {
    const unsigned v = __hip_atomic_load((LAS unsigned*)c, __ATOMIC_RELAXED, __HIP_MEMORY_SCOPE_WORKGROUP);
    if (__builtin_amdgcn_readfirstlane(v) >= n) break;
    __builtin_amdgcn_s_sleep(2);
  }
  asm volatile("" ::: "memory");
}


constexpr int DN_RING = 20, DN_PRE = 6;
constexpr unsigned dn_base(int i) {
  return i < 6 ? L_GW0 : i < 7 ? L_SPARE : i < 13 ? L_GW1 : i < 17 ? L_GH2 : i < 23 ? L_GW2 : i < 27 ? L_GH0 : i < 33 ? L_GW3 : i < 37 ? L_GH1 : L_GW0;
}
constexpr int dn_i0(int i) { return i < 6 ? 0 : i < 7 ? 6 : i < 13 ? 7 : i < 17 ? 13 : i < 23 ? 17 : i < 27 ? 23 : i < 33 ? 27 : i < 37 ? 33 : 37; }
constexpr int dn_n(int i) { return i < 6 ? 6 : i < 7 ? 1 : i < 13 ? 6 : i < 17 ? 4 : i < 23 ? 6 : i < 27 ? 4 : i < 33 ? 6 : i < 37 ? 4 : 6; }

template <int I> struct DnStep {
  static DI void run(v4u (&hf)[DN_RING], s16x8 (&wpre)[DN_PRE][2], s16x8 (&fw)[2][2], f32x16& a0, f32x16& a1, unsigned rel0, unsigned rel1,
                     unsigned kh2048, unsigned b0, unsigned b1, rsrc_t rsWs, unsigned hv) {
    LAS unsigned char* const L0 = (LAS unsigned char*)0;
    if constexpr (I + 1 < 43 && I + 1 >= DN_PRE) {
      constexpr int J = I + 1;
      if constexpr (dn_i0(J) == J) {
        b0 = opaque(rel0 + dn_base(J) + dn_n(J) * kh2048);
        b1 = opaque(rel1 + dn_base(J) + dn_n(J) * kh2048);
      }
      if constexpr (J == 37) asm volatile("s_barrier" ::: "memory");
      constexpr unsigned o = (J - dn_i0(J)) * 2048;
      fw[J & 1][0] = cat(trread(L0, b0 + o), trread(L0, b0 + o + 1024));
      SB();
    }
    constexpr int younger = (I + DN_RING - 1 < 42 ? I + DN_RING - 1 : 42) - I;
    waitv_tie<younger>(hf[I % DN_RING]);
    const s16x8 bf = asfrag(hf[I % DN_RING]);
    if constexpr (I < DN_PRE) a0 = mfma32(wpre[I][0], bf, a0);
    else a0 = mfma32(fw[I & 1][0], bf, a0);
    TIE(a0);
    SB();
    if constexpr (I + 1 < 43 && I + 1 >= DN_PRE) {
      constexpr int J = I + 1;
      constexpr unsigned o = (J - dn_i0(J)) * 2048;
      fw[J & 1][1] = cat(trread(L0, b1 + o), trread(L0, b1 + o + 1024));
      SB();
    }
    if constexpr (I < DN_PRE) a1 = mfma32(wpre[I][1], bf, a1);
    else a1 = mfma32(fw[I & 1][1], bf, a1);
    TIE(a1);
    SB();
    if constexpr (I + DN_RING < 43) hf[I % DN_RING] = ld128_sc1(rsWs, hv, (I + DN_RING) * 1024);
    SB();
    if constexpr (I + 1 < 43) DnStep<I + 1>::run(hf, wpre, fw, a0, a1, rel0, rel1, kh2048, b0, b1, rsWs, hv);
  }
};

__global__ void __launch_bounds__(512, 2) fused_block_kernel(Params p) {
  __shared__ __attribute__((aligned(16))) unsigned char smem[LDS_BYTES];
  LAS unsigned char* L = (LAS unsigned char*)smem;
  LAS unsigned char* const L0 = (LAS unsigned char*)0;
  const unsigned lb = (unsigned)(uintptr_t)L;
  const int tid = threadIdx.x, lane = tid & 63;
  const int w = __builtin_amdgcn_readfirstlane(tid >> 6);
  const int w4 = w & 3;
  const int b = blockIdx.x, xcd = b & 7, jj = b >> 3;
  const int rt = 4 * (xcd >> 1) + (jj >> 3), m = 8 * (xcd & 1) + (jj & 7);
  const int kro = (m + 2 * (rt & 3)) & 7;
  const int l15 = lane & 15, g = lane >> 4, q4 = l15 >> 2, p4 = lane & 3;
  const int hl = lane >> 5, t32 = lane & 31;
  const int hf = m >> 3, cg = m & 7;

  const rsrc_t rsX = mkrsrc(p.x, 2048u * 1024u);
  const rsrc_t rsWs = mkrsrc(p.ws, WS_END);
  const s16x8 ones8 = {0x3F80, 0x3F80, 0x3F80, 0x3F80, 0x3F80, 0x3F80, 0x3F80, 0x3F80};
  v2u ep;
  asm volatile("global_load_dwordx2 %0, %1, off sc1" : "=v"(ep) : "v"(p.ws + WS_EPOCH));
  const unsigned trW = (8 * (g >> 1) + q4) * 64 + (g & 1) * 32 + 8 * p4;
  const unsigned trC = (8 * g + q4) * 64 + (w4 & 1) * 32 + 8 * p4;
  const unsigned voffL = lane * 16;
  const unsigned voffR = (lane >> 3) * 1024 + (((lane & 7) ^ (((lane >> 4) & 1) << 2)) * 16);

  if (__builtin_expect(w >= 4, 1)) {
    asm volatile(".Lfb_begin:");
    const rsrc_t rsWqkv = mkrsrc(p.wqkv, 512u * 3072u);
    const unsigned voffX0 = (128 * rt + 32 * w4 + (lane >> 3)) * 1024 + (((lane & 7) ^ (lane >> 4)) * 16);
    const unsigned voffX1 = (128 * rt + 32 * w4 + 8 + (lane >> 3)) * 1024 + (((lane & 7) ^ (4 + (lane >> 4))) * 16);
    const unsigned voffW = (lane >> 2) * 3072 + (lane & 3) * 16;
    const unsigned albase = lb + 4 * w4 * 1024, awbase = lb + 16384 + w4 * 1024, awsrc = 16 * w4 * 3072 + 1024 + 64 * m;
#define A_LOAD(st)                                                                                     \
  {                                                                                                    \
    const unsigned so_ = ((st) & 3) * A_SLOT, kb_ = (((st) + kro) & 7);                                \
    const unsigned lx_ = albase + so_, lw_ = awbase + so_, sx_ = kb_ * 128, sx2_ = sx_ + 16384, sw_ = awsrc + kb_ * (64 * 3072); \
    dma16o<0>(lx_, voffX0, rsX, sx_);                                                                  \
    dma16o<1024>(lx_, voffX1, rsX, sx_);                                                               \
    dma16o<0>(lw_, voffW, rsWqkv, sw_);                                                                \
    dma16o<2048>(lx_, voffX0, rsX, sx2_);                                                              \
    dma16o<3072>(lx_, voffX1, rsX, sx2_);                                                              \
    dma16o<4096>(lw_, voffW, rsWqkv, sw_ + 1024);                                                      \
  }
    if (w == 7) {
      const rsrc_t rsPhi = mkrsrc(p.wphi, 32u * 512u);
      dma16(lb + L_WPHI, voffL, rsPhi, m * 1024);
    }
    A_LOAD(0)
    A_LOAD(1)
    WAITV_BAR(6);
    A_LOAD(2)
    A_LOAD(3)
#pragma unroll 1
    for (int st = 1; st < 8; ++st) {
      if (st <= 5) WAITV(12);
      else if (st == 6) WAITV(10);
      else WAITV(8);
      BAR();
      if (st <= 4) A_LOAD(st + 3)
      else if (st <= 6) {
#pragma unroll
        for (int j = 0; j < 4; ++j) {
          const unsigned r = 128 * w4 + 64 * (st - 5) + 16 * j;
          dma16(lb + L_WQ + r * 64, voffW, rsWqkv, ((r + 64 * kro) & 511) * 3072 + 64 * m);
        }
      }
    }
    const u64 tag = mix64(wait_epoch(ep));
    BAR();
    {
      unsigned o = (b * 4 + w4) * 11 + (lane < 11 ? lane : 0);
      if (o >= 11008) o = 0;
      touch_global(lb + L_TOUCH + w4 * 256, (const unsigned char*)p.wd + (size_t)o * 128);
    }
    {
      const rsrc_t rsG = mkrsrc(p.wg, 512u * 2752u), rsU = mkrsrc(p.wu, 512u * 2752u);
      const unsigned vo = (lane >> 1) * 2752 + (lane & 1) * 16, so = 32 * rt * 2752 + 192 * m;
      const unsigned dst = lb + L_CSW + w4 * 4608;
      if (w4 < 2) {
        dma16(dst + 2304, vo, rsG, so + 32 * (w4 + 4));
        dma16(dst + 2304 + 1152, vo, rsU, so + 32 * (w4 + 4));
      }
      dma16(dst, vo, rsG, so + 32 * w4);
      dma16(dst + 1152, vo, rsU, so + 32 * w4);
      const rsrc_t rsWo = mkrsrc(p.wo, 512u * 1024u);
#pragma unroll 4
      for (int i = 0; i < 16; ++i) dma16(lb + L_WO + (w4 * 16 + i) * 1024, voffR, rsWo, (w4 * 16 + i) * 8192 + 128 * cg);
    }
    {
      const unsigned cq = lb + L_WQ + trC + (w4 >> 1) * 8 * 2048;
      f32x4 acq = {0, 0, 0, 0}, acq2 = acq;
#pragma unroll
      for (int s = 0; s < 8; s += 2) {
        acq = mfma16(cat(trread(L0, cq + s * 2048), trread(L0, cq + s * 2048 + 256)), ones8, acq);
        acq2 = mfma16(cat(trread(L0, cq + (s + 1) * 2048), trread(L0, cq + (s + 1) * 2048 + 256)), ones8, acq2);
      }
      if (l15 == 0) *(LAS f32x4*)(L + L_CS + 256 + (w4 >> 1) * 128 + (w4 & 1) * 64 + g * 16) = acq + acq2;
      lds_arrive(L + L_CNT + 4, lane);
    }
    WAITV(16);
#pragma unroll 1
    for (int bl = w4, k = 0; bl < 6; bl += 4, ++k) {
      const unsigned a = L_CSW + w4 * 4608 + k * 2304 + (p4 >> 1) * 1152 + (4 * (g >> 1) + q4) * 32 + 16 * (g & 1) + 8 * (p4 & 1);
      f32x16 c;
#pragma unroll
      for (int i = 0; i < 16; ++i) c[i] = 0.f;
      c = mfma32(cat(trread(L, a), trread(L, a + 8 * 32)), ones8, c);
      c = mfma32(cat(trread(L, a + 16 * 32), trread(L, a + 24 * 32)), ones8, c);
      const unsigned off = t32 == 0 ? WS_CSP + ((m * 16 + rt) * 6 + bl) * 128 + hl * 16 : 0x7FFFFF00u;
#pragma unroll
      for (int i = 0; i < 4; ++i)
        __builtin_amdgcn_raw_buffer_store_b128(__builtin_bit_cast(v4u, (f32x4){c[4 * i], c[4 * i + 1], c[4 * i + 2], c[4 * i + 3]}), rsWs, off, i * 32, 16);
    }
    lds_arrive(L + L_CNT + 12, lane);
    WAITV(0);
    if (lane == 0) set_flag(p.ws + WS_F5 + m * 512 + (rt * 4 + w4) * 8, tag);

    const rsrc_t rsGU = mkrsrc((w4 >> 1) ? p.wu : p.wg, 512u * 2752u);
    unsigned voffG[6];
#pragma unroll
    for (int j = 0; j < 6; ++j) {
      const unsigned u = 64 * (6 * (w4 & 1) + j) + lane, row = u / 12, c = u - row * 12;
      voffG[j] = row * 2752 + c * 16;
    }
    const unsigned gdst = (w4 >> 1) * GU_UPOFF + 6 * (w4 & 1) * 1024;
    const unsigned gsrc = 192 * m;
#define GU_LOADW(st, slot)                                                                       \
  {                                                                                              \
    const unsigned l_ = lb + (slot) + gdst, s_ = gsrc + (((st) + kro) & 7) * 64 * 2752;          \
    dma16o<0>(l_, voffG[0], rsGU, s_); dma16o<1024>(l_, voffG[1], rsGU, s_); dma16o<2048>(l_, voffG[2], rsGU, s_); \
    dma16o<3072>(l_, voffG[3], rsGU, s_); dma16o<4096>(l_, voffG[4], rsGU, s_); dma16o<5120>(l_, voffG[5], rsGU, s_); \
  }
    lds_wait(L + L_CNT + 8, 4);
    GU_LOADW(0, L_GW0)
    lds_wait(L + L_CNT + 12, 8);
    GU_LOADW(1, L_GW1)
    BAR();
    pollN<64>(p.ws + WS_F5 + m * 512, tag, lane);
#pragma unroll 1
    for (int bl = w4; bl < 6; bl += 4) {
      float s = 0.f;
      unsigned cv[8];
#pragma unroll
      for (int j = 0; j < 8; ++j) cv[j] = __builtin_amdgcn_raw_buffer_load_b32(rsWs, WS_CSP + ((m * 16 + 8 * hl) * 6 + bl) * 128 + t32 * 4, j * 768, 16);
#pragma unroll
      for (int j = 0; j < 8; ++j) s += uf(cv[j]);
      float s2 = s;
      swap32(s, s2);
      s += s2;
      const unsigned hi = pk2(s, 0.f) & 0xffffu, lo = pk2(s - uf(hi << 16), 0.f) & 0xffffu;
      if (lane < 32) *(LAS v4u*)(L + L_CSF + bl * 512 + lane * 16) = (v4u){hi | (hi << 16), lo, 0, 0};
    }
    const rsrc_t rsWd = mkrsrc(p.wd, 1376u * 1024u);
    const unsigned dsrc = (16 * 43 * (w4 >> 1) + 8 * (w4 & 1)) * 1024 + 128 * cg;
    v4u wreg[16];
    lds_wait_sleep(L + L_CNT + 16, 4);
#pragma unroll
    for (int j = 0; j < 16; ++j) wreg[j] = ld128(rsWd, voffR, dsrc + (27 + j) * 16384);
    BAR();
    BAR();
    GU_LOADW(2, L_GW2)
    GU_LOADW(3, L_GW3)
    pollN<16>(p.ws + WS_F3 + (rt * 4 + w4) * 128, tag, lane);
    const unsigned hsrc = WS_H + (rt * 4 + w4) * 32 * 1024;
    const unsigned hdst = w4 * 4096;
    const unsigned voffH1 = voffL + 1024 - 0, voffH2 = voffL + 2048, voffH3 = voffL + 3072;
#define GU_LOADH(st, slot)                                           \
  {                                                                  \
    const unsigned l_ = lb + (slot) + hdst, s_ = hsrc + (((st) + kro) & 7) * 4096; \
    dma16o_sc1<0>(l_, voffL, rsWs, s_); dma16o_sc1<1024>(l_, voffH1, rsWs, s_);    \
    dma16o_sc1<2048>(l_, voffH2, rsWs, s_); dma16o_sc1<3072>(l_, voffH3, rsWs, s_); \
  }
    GU_LOADH(0, L_GH0)
    WAITV_BAR(0);
    GU_LOADH(1, L_GH1)
    GU_LOADH(2, L_GH2)
    WAITV_BAR(4);
    GU_LOADH(3, L_GH0)
    v4u sv[8];
    const unsigned svo = WS_ST + ((rt * 4 + w4) * 16 + 8 * hl) * 512 + t32 * 16;
#pragma unroll
    for (int i = 0; i < 8; ++i) sv[i] = ld128_sc1(rsWs, svo, i * 512);
    GU_LOADW(4, L_GW0)
    WAITV_BAR(18);
    GU_LOADH(4, L_GH1)
    GU_LOADW(5, L_GW1)
    asm volatile("s_waitcnt vmcnt(16)" : "+v"(sv[0]), "+v"(sv[1]), "+v"(sv[2]), "+v"(sv[3]), "+v"(sv[4]), "+v"(sv[5]), "+v"(sv[6]), "+v"(sv[7])::"memory");
    {
      bool ok = true;
#pragma unroll
      for (int i = 0; i < 8; ++i) ok &= (sv[i][1] == (unsigned)tag) & (sv[i][3] == (unsigned)tag);
#pragma unroll 1
      for (unsigned it = 0; !__all(ok) && it < SPIN_CAP; ++it) {
        ok = true;
#pragma unroll
        for (int i = 0; i < 8; ++i) {
          sv[i] = __builtin_amdgcn_raw_buffer_load_b128(rsWs, svo, i * 512, 16);
          ok &= (sv[i][1] == (unsigned)tag) & (sv[i][3] == (unsigned)tag);
        }
      }
      float s = 0.f, q = 0.f;
#pragma unroll
      for (int i = 0; i < 8; ++i) {
        s += uf(sv[i][0]);
        q += uf(sv[i][2]);
      }
      swap32(s, q);
      float t = s + q, t2 = t;
      swap32(t, t2);
      const float mean2 = t * (1.f / 512.f);
      const float var = fmaxf(t2 * (1.f / 512.f) - mean2 * mean2, 0.f) + 1e-5f;
      const float nm = -mean2;
      const unsigned hi = pk2(nm, 0.f) & 0xffffu, lo = pk2(nm - uf(hi << 16), 0.f) & 0xffffu;
      if (lane < 32) {
        *(LAS v4u*)(L + L_MF + w4 * 512 + lane * 16) = (v4u){hi | (lo << 16), hi, 0, 0};
        *(LAS float*)(L + L_R2 + (32 * w4 + lane) * 4) = __builtin_amdgcn_rcpf(var);
      }
    }
#define DN_LOAD(i0)                                                                                            \
  {                                                                                                            \
    _Pragma("unroll") for (int j = 0; j < dn_n(i0); ++j)                                                       \
        dma16(lb + dn_base(i0) + ((w4 >> 1) * dn_n(i0) + j) * 2048 + (w4 & 1) * 1024, voffR, rsWd, dsrc + ((i0) + j) * 16384); \
  }
    WAITV_BAR(16);
    GU_LOADH(5, L_GH2)
    GU_LOADW(6, L_GW2)
    WAITV_BAR(16);
    GU_LOADH(6, L_GH0)
    GU_LOADW(7, L_GW3)
    WAITV_BAR(16);
    GU_LOADH(7, L_GH1)
    DN_LOAD(0) DN_LOAD(6)
    WAITV_BAR(17);
    DN_LOAD(7) DN_LOAD(13)
    WAITV_BAR(17);
    DN_LOAD(17) DN_LOAD(23)
#pragma unroll
    for (int j = 0; j < 16; ++j) TIE(wreg[j]);
#define DN_PUT(i0, j0)                                                                                         \
  {                                                                                                            \
    _Pragma("unroll") for (int j = 0; j < dn_n(i0); ++j)                                                       \
        *(LAS v4u*)(L + dn_base(i0) + ((w4 >> 1) * dn_n(i0) + j) * 2048 + (w4 & 1) * 1024 + lane * 16) = wreg[(j0) + j]; \
  }
    BAR();
    DN_PUT(27, 0) DN_PUT(33, 6)
    BAR();
    WAITV_BAR(0);
    BAR();
    DN_PUT(37, 10)
    BAR();
    BAR();
  } else {
    __builtin_amdgcn_s_setprio(2);
    if (tid < 4) *(LAS unsigned*)(L + L_CNT + tid * 4) = 0;
    if (tid == 4) *(LAS unsigned*)(L + L_CNT + 16) = 0;
    if (tid == 5) *(LAS v4u*)(L + L_ZERO) = (v4u){0, 0, 0, 0};
    {
      u64 pc;
      unsigned back, len;
      asm volatile("s_getpc_b64 %0\n.Lfb_here:\n\ts_mov_b32 %1, .Lfb_here-.Lfb_begin\n\ts_mov_b32 %2, .Lfb_end-.Lfb_begin" : "=s"(pc), "=s"(back), "=s"(len));
      const unsigned o = (jj * 14 + (lane < 14 ? lane : 0)) * 128;
      if (w4 == 0) touch_global(lb + L_TOUCH2, (const unsigned char*)pc - back + ((int)len > 0 && o + 128 <= len ? o : back));
    }
    const unsigned t0s = 128 * rt + 8 * m + 2 * w4;
    v4u xs0 = ld128(rsX, t0s * 1024 + lane * 16, 0), xs1 = ld128(rsX, t0s * 1024 + lane * 16, 1024);
    u64 tag;
#define A_STATS()                                                                                                      \
  {                                                                                                                    \
    tag = mix64(wait_epoch(ep));                                                                                       \
    asm volatile("" : "+v"(xs0), "+v"(xs1));                                                                 \
    float s0 = 0.f, q0 = 0.f, s1 = 0.f, q1 = 0.f;                                                                      \
    _Pragma("unroll") for (int e = 0; e < 4; ++e) {                                                                    \
      s0 = dot2(xs0[e], 0x3F803F80u, s0);                                                                              \
      q0 = dot2(xs0[e], xs0[e], q0);                                                                                   \
      s1 = dot2(xs1[e], 0x3F803F80u, s1);                                                                              \
      q1 = dot2(xs1[e], xs1[e], q1);                                                                                   \
    }                                                                                                                  \
    s0 = rowsum16(s0); q0 = rowsum16(q0); s1 = rowsum16(s1); q1 = rowsum16(q1);                                        \
    red4add(s0, q0, s1, q1);                                                                                       \
    const float sm = (lane & 1) ? s1 : s0, sq = (lane & 1) ? q1 : q0;                                                  \
    const float mean = sm * (1.f / 512.f);                                                                             \
    const float r = __builtin_amdgcn_rsqf(fmaxf(sq * (1.f / 512.f) - mean * mean, 0.f) + 1e-5f);                       \
    __builtin_amdgcn_raw_buffer_store_b128((v4u){fu(r), (unsigned)tag, fu(-mean * r), (unsigned)tag}, rsWs,            \
                                           lane < 2 ? WS_S1 + (rt * 128 + 8 * m + 2 * w4 + lane) * 16 : 0x7FFFFFF0u, 0, 16); \
    SB();                                                                                                              \
  }
    v4u s1v;
    v4u xf[32];
    f32x16 aK, aV;
#pragma unroll
    for (int i = 0; i < 16; ++i) { aK[i] = 0.f; aV[i] = 0.f; }
    const f32x4 z4 = {0, 0, 0, 0};
    f32x16 z16;
#pragma unroll
    for (int i = 0; i < 16; ++i) z16[i] = 0.f;
    {
      const unsigned xr0 = lb + (32 * w4 + t32) * 128, xsw = (t32 >> 1) & 7;
      unsigned xlo[4], xhi[4];
#pragma unroll
      for (int j = 0; j < 4; ++j) {
        xlo[j] = opaque(xr0 + (((2 * j + hl) ^ xsw) << 4));
        xhi[j] = opaque(xlo[j] + 2 * A_SLOT);
      }
      const unsigned wlo = opaque(lb + 16384 + trW), whi = opaque(wlo + 2 * A_SLOT);
      const unsigned clo = opaque(lb + 16384 + (w4 >> 1) * 4096 + trC), chi = opaque(clo + 2 * A_SLOT);
      s16x8 fk[2], fv[2], fc[2];
      f32x4 acs = z4;
#define A1_X(s) xf[s] = *(LAS v4u*)(L0 + ((((s) >> 3) & 1) ? xhi[(s) & 3] : xlo[(s) & 3]) + (((s) >> 2) & 1) * A_SLOT); SB()
#define A1_WA(s) (((((s) >> 3) & 1) ? whi : wlo) + (((s) >> 2) & 1) * A_SLOT + ((s) & 3) * 1024)
#define A1_WK(s) fk[(s) & 1] = cat(trread(L0, A1_WA(s)), trread(L0, A1_WA(s) + 256)); SB()
#define A1_WV(s) fv[(s) & 1] = cat(trread(L0, A1_WA(s) + 4096), trread(L0, A1_WA(s) + 4096 + 256)); SB()
#define A1_CA(s) (((((s) >> 3) & 1) ? chi : clo) + (((s) >> 2) & 1) * A_SLOT + (((s) >> 1) & 1) * 2048)
#define A1_C(s) fc[((s) >> 1) & 1] = cat(trread(L0, A1_CA(s)), trread(L0, A1_CA(s) + 256)); SB()
#define A1_MK(s) aK = mfma32(fk[(s) & 1], asfrag(xf[s]), aK); TIE(aK); SB()
#define A1_MV(s) aV = mfma32(fv[(s) & 1], asfrag(xf[s]), aV); TIE(aV); SB()
#define A1_CM(s) acs = mfma16(fc[((s) >> 1) & 1], ones8, acs); TIE(acs); SB()
#define A1_RM(sn, sc)                          \
  A1_X(sn);                                    \
  A1_MK(sc);                                   \
  A1_WK(sn);                                   \
  A1_MV(sc);                                   \
  A1_WV(sn);                                   \
  if (((sn) & 1) == 0) { A1_C(sn); }           \
  if (((sc) & 1) == 1) { A1_CM(sc); }
#define A1_STAGE(st) BAR(); A1_RM(4 * (st), 4 * (st) - 1) A1_RM(4 * (st) + 1, 4 * (st)) A1_RM(4 * (st) + 2, 4 * (st) + 1) A1_RM(4 * (st) + 3, 4 * (st) + 2)
      BAR();
      A1_X(0); A1_WK(0); A1_WV(0); A1_C(0);
      A1_RM(1, 0) A1_RM(2, 1) A1_RM(3, 2)
      A_STATS()
      A1_STAGE(1) A1_STAGE(2) A1_STAGE(3) A1_STAGE(4) A1_STAGE(5)
      s1v = ld128_sc1(rsWs, WS_S1 + (rt * 128 + 32 * w4 + t32) * 16, 0);
      A1_STAGE(6) A1_STAGE(7)
      A1_MK(31); A1_MV(31); A1_CM(31);
      if (l15 == 0) *(LAS f32x4*)(L + L_CS + w4 * 64 + g * 16) = acs;
      asm volatile("s_waitcnt vmcnt(0)" : "+v"(s1v));
      bool ok = (s1v[1] == (unsigned)tag) & (s1v[3] == (unsigned)tag);
#pragma unroll 1
      for (unsigned it = 0; !__all(ok) && it < SPIN_CAP; ++it) {
        s1v = __builtin_amdgcn_raw_buffer_load_b128(rsWs, WS_S1 + (rt * 128 + 32 * w4 + t32) * 16, 0, 16);
        ok = (s1v[1] == (unsigned)tag) & (s1v[3] == (unsigned)tag);
      }
    }
    const float rstd = uf(s1v[0]), nb = uf(s1v[2]);
    BAR();

    s16x8 aphi0, aphi1;
    {
      const unsigned real = lb + L_WPHI + (4 * (g >> 1) + q4) * 32 + ((p4 >> 1) + 2 * (g & 1)) * 8, zero = lb + L_ZERO;
      const unsigned a00 = (p4 & 1) ? zero : real, a01 = (p4 & 1) ? zero : real + 256;
      const unsigned a10 = (p4 & 1) ? real + 512 : zero, a11 = (p4 & 1) ? real + 512 + 256 : zero;
      aphi0 = cat(trread(L0, a00), trread(L0, a01));
      aphi1 = cat(trread(L0, a10), trread(L0, a11));
    }
    const unsigned tsw = (t32 >> 2) & 1;
    {
      f32x16 kk, vv;
#pragma unroll
      for (int i = 0; i < 4; ++i) {
        const f32x4 ck = *(LAS f32x4*)(L + L_CS + (8 * i + 4 * hl) * 4), cv = *(LAS f32x4*)(L + L_CS + 128 + (8 * i + 4 * hl) * 4);
#pragma unroll
        for (int e = 0; e < 4; ++e) {
          kk[4 * i + e] = fmaf(aK[4 * i + e], rstd, ck[e] * nb);
          vv[4 * i + e] = fmaf(aV[4 * i + e], rstd, cv[e] * nb);
        }
      }
      const v4u kb0 = {pk2(kk[0], kk[1]), pk2(kk[2], kk[3]), pk2(kk[4], kk[5]), pk2(kk[6], kk[7])};
      const v4u kb1 = {pk2(kk[8], kk[9]), pk2(kk[10], kk[11]), pk2(kk[12], kk[13]), pk2(kk[14], kk[15])};
      f32x16 pk = mfma32(aphi0, asfrag(kb0), z16);
      pk = mfma32(aphi1, asfrag(kb1), pk);
      const unsigned row = L_KVIMG + (32 * w4 + t32) * 32;
#pragma unroll
      for (int i = 0; i < 4; ++i)
        *(LAS v2u*)(L + row + (4 + (i >> 1)) * 4096 + 8 * ((hl + 2 * (i & 1)) ^ (2 * tsw))) = (v2u){pk2(vv[4 * i], vv[4 * i + 1]), pk2(vv[4 * i + 2], vv[4 * i + 3])};
      unsigned pos[8], neg[8];
      dual_softmax16(pk, pos, neg);
      const unsigned rp = row + 2 * hl * 4096;
      *(LAS v4u*)(L + rp + 16 * tsw) = (v4u){pos[0], pos[1], pos[2], pos[3]};
      *(LAS v4u*)(L + rp + 16 * (tsw ^ 1)) = (v4u){pos[4], pos[5], pos[6], pos[7]};
      *(LAS v4u*)(L + rp + 4096 + 16 * tsw) = (v4u){neg[0], neg[1], neg[2], neg[3]};
      *(LAS v4u*)(L + rp + 4096 + 16 * (tsw ^ 1)) = (v4u){neg[4], neg[5], neg[6], neg[7]};
    }
    lds_arrive(L + L_CNT, lane);
    lds_wait(L + L_CNT, 4);
    {
      const unsigned ra = L_KVIMG + (4 * g + q4) * 32 + 8 * (p4 ^ (2 * (g & 1)));
      const unsigned raA = ra + w4 * 4096, raB = ra + (4 + (w4 >> 1)) * 4096;
      f32x4 acc = z4, accz = z4;
      s16x8 fA[4], fB[4];
#pragma unroll
      for (int u = 0; u < 4; ++u) {
        fA[u] = cat(trread(L, raA + u * 1024), trread(L, raA + u * 1024 + 512));
        fB[u] = cat(trread(L, raB + u * 1024), trread(L, raB + u * 1024 + 512));
      }
#pragma unroll
      for (int u = 0; u < 4; ++u) { TIE(fA[u]); TIE(fB[u]); }
      lds_arrive(L + L_CNT + 8, lane);
#pragma unroll
      for (int u = 0; u < 4; ++u) {
        acc = mfma16(fA[u], fB[u], acc);
        accz = mfma16(fA[u], ones8, accz);
      }
      const unsigned base = WS_KVP + (m * 16 + rt) * KVP_STRIDE;
      __builtin_amdgcn_raw_buffer_store_b128(__builtin_bit_cast(v4u, acc), rsWs, base + w4 * 1024 + lane * 16, 0, 16);
      __builtin_amdgcn_raw_buffer_store_b128(__builtin_bit_cast(v4u, accz), rsWs, l15 == 0 ? base + 4096 + w4 * 64 + g * 16 : 0x7FFFFFF0u, 0, 16);
      WAITV(0);
      if (lane == 0) set_flag(p.ws + WS_F1 + m * 512 + (rt * 4 + w4) * 8, tag);
    }

    v4u bq[2][2];
    v2u f1v;
    {
      f32x16 aQ = z16;
      const unsigned trQ = opaque(lb + L_WQ + trW);
      s16x8 fq[2];
#define A2_W(s) fq[(s) & 1] = cat(trread(L0, trQ + (s) * 1024), trread(L0, trQ + (s) * 1024 + 256)); SB()
#define A2_STEP(s)                                    \
  if ((s) < 31) { A2_W(((s) + 1) & 31); }             \
  aQ = mfma32(fq[(s) & 1], asfrag(xf[s]), aQ);        \
  TIE(aQ);                                            \
  SB();
#define A2_STEP4(s) A2_STEP(s) A2_STEP((s) + 1) A2_STEP((s) + 2) A2_STEP((s) + 3)
      A2_W(0);
      A2_STEP4(0) A2_STEP4(4) A2_STEP4(8) A2_STEP4(12) A2_STEP4(16) A2_STEP4(20) A2_STEP4(24) A2_STEP4(28)
      lds_arrive(L + L_CNT + 12, lane);
      lds_wait(L + L_CNT + 4, 4);
      f32x16 qq;
#pragma unroll
      for (int i = 0; i < 4; ++i) {
        const f32x4 c = *(LAS f32x4*)(L + L_CS + 256 + (8 * i + 4 * hl) * 4) + *(LAS f32x4*)(L + L_CS + 384 + (8 * i + 4 * hl) * 4);
#pragma unroll
        for (int e = 0; e < 4; ++e) qq[4 * i + e] = fmaf(aQ[4 * i + e], rstd, c[e] * nb);
      }
      const v4u qb0 = {pk2(qq[0], qq[1]), pk2(qq[2], qq[3]), pk2(qq[4], qq[5]), pk2(qq[6], qq[7])};
      const v4u qb1 = {pk2(qq[8], qq[9]), pk2(qq[10], qq[11]), pk2(qq[12], qq[13]), pk2(qq[14], qq[15])};
      f32x16 pq = mfma32(aphi0, asfrag(qb0), z16);
      pq = mfma32(aphi1, asfrag(qb1), pq);
      asm volatile("global_load_dwordx2 %0, %1, off sc1" : "=v"(f1v) : "v"(p.ws + WS_F1 + m * 512 + lane * 8));
      SB();
      unsigned pos[8], neg[8];
      dual_softmax16(pq, pos, neg);
#pragma unroll
      for (int j = 0; j < 4; ++j) {
        swap32u(pos[j], pos[j + 4]);
        swap32u(neg[j], neg[j + 4]);
      }
      bq[0][0] = (v4u){pos[0], pos[1], pos[2], pos[3]};
      bq[1][0] = (v4u){pos[4], pos[5], pos[6], pos[7]};
      bq[0][1] = (v4u){neg[0], neg[1], neg[2], neg[3]};
      bq[1][1] = (v4u){neg[4], neg[5], neg[6], neg[7]};
    }

    {
      TIE(bq[0][0]); TIE(bq[0][1]); TIE(bq[1][0]); TIE(bq[1][1]);
      asm volatile("s_waitcnt vmcnt(0)" : "+v"(f1v)::"memory");
      if (!__all(f1v[0] == (unsigned)tag && f1v[1] == (unsigned)(tag >> 32))) pollN<64>(p.ws + WS_F1 + m * 512, tag, lane);
      const unsigned base = WS_KVP + m * 16 * KVP_STRIDE;
      v4u pv[16], zv[4];
#pragma unroll
      for (int i = 0; i < 4; ++i) zv[i] = __builtin_amdgcn_raw_buffer_load_b128(rsWs, base + g * KVP_STRIDE + 4096 + l15 * 16, 4 * i * KVP_STRIDE, 16);
#pragma unroll
      for (int i = 0; i < 16; ++i) pv[i] = __builtin_amdgcn_raw_buffer_load_b128(rsWs, base + tid * 16, i * KVP_STRIDE, 16);
      const unsigned kfb = L_KF + w4 * 1024;
      if (t32 > 16) *(LAS v4u*)(L + kfb + lane * 16) = (v4u){0, 0, 0, 0};
      f32x4 sz = __builtin_bit_cast(f32x4, zv[0]);
#pragma unroll
      for (int i = 1; i < 4; ++i) sz += __builtin_bit_cast(f32x4, zv[i]);
      float z0 = sz[0], z1 = sz[1], z2 = sz[2], z3 = sz[3];
      red4add(z0, z1, z2, z3);
      if (g == 0 && q4 == w4) *(LAS v2u*)(L + kfb + ((p4 >> 1) * 32 + 16) * 16 + (p4 & 1) * 8) = (v2u){pk2(z0, z1), pk2(z2, z3)};
      f32x4 s = __builtin_bit_cast(f32x4, pv[0]);
#pragma unroll
      for (int i = 1; i < 16; ++i) s += __builtin_bit_cast(f32x4, pv[i]);
      *(LAS v2u*)(L + kfb + ((g >> 1) * 32 + l15) * 16 + (g & 1) * 8) = (v2u){pk2(s[0], s[1]), pk2(s[2], s[3])};
    }
    BAR();
    {
#pragma unroll
      for (int hh = 0; hh < 2; ++hh) {
        const s16x8 kp = *(LAS s16x8*)(L + L_KF + (2 * hh) * 1024 + lane * 16), kn = *(LAS s16x8*)(L + L_KF + (2 * hh + 1) * 1024 + lane * 16);
        f32x16 c = mfma32(kp, asfrag(bq[hh][0]), z16);
        c = mfma32(kn, asfrag(bq[hh][1]), c);
        float d = c[8], d2 = d;
        swap32(d, d2);
        const float inv = __builtin_amdgcn_rcpf(d + 1e-6f);
        const v4u ov = {pk2(c[0] * inv, c[1] * inv), pk2(c[2] * inv, c[3] * inv), pk2(c[4] * inv, c[5] * inv), pk2(c[6] * inv, c[7] * inv)};
        __builtin_amdgcn_raw_buffer_store_b128(ov, rsWs, WS_O + ((rt * 4 + w4) * 32 + 2 * m + hh) * 1024 + lane * 16, 0, 16);
      }
      WAITV(0);
      if (lane == 0) set_flag(p.ws + WS_F2 + ((rt * 4 + w4) * 2 + hf) * 128 + cg * 8, tag);
    }

    const int tb32 = 2 * hf + (w & 1), kh = (w >> 1) & 1;
    unsigned hres[8];
    const unsigned tok = 128 * rt + 32 * tb32 + t32;
    v2u xr[4];
#pragma unroll
    for (int i = 0; i < 4; ++i) xr[i] = ld64(rsX, tok * 1024 + (64 * cg + 32 * kh + 4 * hl) * 2, i * 16);
    const unsigned relR = (4 * (g >> 1) + q4) * 128 + (g & 1) * 32 + 8 * p4;
    const unsigned rel0 = lb + relR + ((0 ^ (q4 >> 1)) << 6), rel1 = lb + relR + ((1 ^ (q4 >> 1)) << 6);
    f32x16 a0, a1;
#pragma unroll
    for (int i = 0; i < 16; ++i) { a0[i] = 0.f; a1[i] = 0.f; }
    {
      const unsigned rl0 = opaque(rel0 + kh * 32768), rl1 = opaque(rel1 + kh * 32768);
      s16x8 fw[2][2];
#define WO_RD1(i, c)                                                                                    \
  {                                                                                                     \
    constexpr unsigned o_ = (i) * 2048;                                                                 \
    fw[(i) & 1][c] = cat(trread(L0, ((c) ? rl1 : rl0) + o_), trread(L0, ((c) ? rl1 : rl0) + o_ + 1024)); \
    SB();                                                                                               \
  }
#define WO_RD(i) WO_RD1(i, 0) WO_RD1(i, 1)
      WO_RD(0)
      pollN<8>(p.ws + WS_F2 + ((rt * 4 + tb32) * 2 + kh) * 128, tag, lane);
      v4u of[16];
      const unsigned ov = WS_O + ((rt * 4 + tb32) * 32 + 16 * kh) * 1024 + lane * 16;
#pragma unroll
      for (int i = 0; i < 16; ++i) of[i] = ld128_sc1(rsWs, ov, i * 1024);
      lds_arrive(L + L_CNT + 16, lane);
#define WO_STEP(i)                                          \
  {                                                         \
    if ((i) < 15) WO_RD1(((i) + 1) & 15, 0)                 \
    waitv_tie<15 - (i)>(of[i]);                             \
    a0 = mfma32(fw[(i) & 1][0], asfrag(of[i]), a0);         \
    TIE(a0);                                                \
    SB();                                                   \
    if ((i) < 15) WO_RD1(((i) + 1) & 15, 1)                 \
    a1 = mfma32(fw[(i) & 1][1], asfrag(of[i]), a1);         \
    TIE(a1);                                                \
    SB();                                                   \
  }
      WO_STEP(0) WO_STEP(1) WO_STEP(2) WO_STEP(3) WO_STEP(4) WO_STEP(5) WO_STEP(6) WO_STEP(7)
      WO_STEP(8) WO_STEP(9) WO_STEP(10) WO_STEP(11) WO_STEP(12) WO_STEP(13) WO_STEP(14) WO_STEP(15)
    }
    {
      const f32x16 give = kh ? a0 : a1;
      f32x16 keep = kh ? a1 : a0;
#pragma unroll
      for (int i = 0; i < 4; ++i)
        *(LAS f32x4*)(L + L_RED_WO + (w * 4 + i) * 1024 + lane * 16) = (f32x4){give[4 * i], give[4 * i + 1], give[4 * i + 2], give[4 * i + 3]};
      BAR();
      float ss = 0.f, sq = 0.f;
#pragma unroll
      for (int i = 0; i < 4; ++i) {
        const f32x4 o = *(LAS f32x4*)(L + L_RED_WO + ((w ^ 2) * 4 + i) * 1024 + lane * 16);
        TIE(xr[i]);
        hres[2 * i] = pk2(keep[4 * i] + o[0] + bflo(xr[i][0]), keep[4 * i + 1] + o[1] + bfhi(xr[i][0]));
        hres[2 * i + 1] = pk2(keep[4 * i + 2] + o[2] + bflo(xr[i][1]), keep[4 * i + 3] + o[3] + bfhi(xr[i][1]));
      }
      const int Sg = 2 * cg + kh;
      const unsigned ho = WS_H + ((rt * 4 + tb32) * 32 + 2 * Sg) * 1024 + lane * 16;
      __builtin_amdgcn_raw_buffer_store_b128((v4u){hres[0], hres[1], hres[2], hres[3]}, rsWs, ho, 0, 16);
      __builtin_amdgcn_raw_buffer_store_b128((v4u){hres[4], hres[5], hres[6], hres[7]}, rsWs, ho + 1024, 0, 16);
      WAITV(0);
      if (lane == 0) set_flag(p.ws + WS_F3 + (rt * 4 + tb32) * 128 + Sg * 8, tag);
#pragma unroll
      for (int i = 0; i < 8; ++i) {
        const float lo = bflo(hres[i]), hi = bfhi(hres[i]);
        ss += lo + hi;
        sq = fmaf(lo, lo, fmaf(hi, hi, sq));
      }
      swap32(ss, sq);
      __builtin_amdgcn_raw_buffer_store_b64((v2u){fu(ss + sq), (unsigned)tag}, rsWs, WS_ST + ((rt * 4 + tb32) * 16 + Sg) * 512 + t32 * 16 + hl * 8, 0, 16);
    }
    BAR();

    const int mh = w & 1, nh = (w >> 1) & 1;
    f32x16 acc[2][3];
#pragma unroll
    for (int i = 0; i < 16; ++i) {
      acc[0][0][i] = 0.f; acc[0][1][i] = 0.f; acc[0][2][i] = 0.f;
      acc[1][0][i] = 0.f; acc[1][1][i] = 0.f; acc[1][2][i] = 0.f;
    }
    {
      const unsigned relT = lb + mh * 8192 + lane * 16;
      const unsigned relW = lb + (p4 >> 1) * GU_UPOFF + (4 * (g >> 1) + q4) * 192 + 16 * (g & 1) + 8 * (p4 & 1) + 96 * nh;
      v4u tf[2][2];
      s16x8 ga[2][3];
      tf[1][0] = tf[1][1] = (v4u){0, 0, 0, 0};
      ga[1][0] = ga[1][1] = ga[1][2] = asfrag(tf[1][0]);
#define GU_RT(j, tt) tf[(j) & 1][tt] = *(LAS v4u*)(L0 + aT + (tt) * 4096 + (j) * 1024); SB()
#define GU_RW(j, l3) ga[(j) & 1][l3] = cat(trread(L0, aW + (j) * 16 * 192 + 32 * (l3)), trread(L0, aW + ((j) * 16 + 8) * 192 + 32 * (l3))); SB()
#define GU_M(j, tt, l3)                                                       \
  acc[tt][l3] = mfma32(ga[(j) & 1][l3], asfrag(tf[(j) & 1][tt]), acc[tt][l3]); \
  TIE(acc[tt][l3]);                                                            \
  SB()
#define GU_STEP(jn, jc)            \
  GU_RT(jn, 0);                    \
  GU_M(jc, 0, 0);                  \
  GU_RW(jn, 0);                    \
  GU_M(jc, 0, 1);                  \
  GU_RW(jn, 1);                    \
  GU_M(jc, 0, 2);                  \
  GU_RT(jn, 1);                    \
  GU_M(jc, 1, 0);                  \
  GU_RW(jn, 2);                    \
  GU_M(jc, 1, 1);                  \
  GU_M(jc, 1, 2);                  \
  SB();
      const bool real = 6 * m + 3 * nh < 86;
      const unsigned aT0 = opaque(relT + L_GH0), aT1 = opaque(relT + L_GH1), aT2 = opaque(relT + L_GH2);
      const unsigned aW0 = opaque(relW + L_GW0), aW1 = opaque(relW + L_GW1), aW2 = opaque(relW + L_GW2), aW3 = opaque(relW + L_GW3);
#define GU_STAGE(T_, W_)  \
  BAR();                  \
  if (real) {             \
    const unsigned aT = T_, aW = W_; \
    GU_STEP(0, 3)         \
    GU_STEP(1, 0)         \
    GU_STEP(2, 1)         \
    GU_STEP(3, 2)         \
  }
      BAR();
      if (real) {
        const unsigned aT = aT0, aW = aW0;
        GU_RT(0, 0); GU_RW(0, 0); GU_RW(0, 1); GU_RT(0, 1); GU_RW(0, 2);
        GU_STEP(1, 0)
        GU_STEP(2, 1)
        GU_STEP(3, 2)
      }
      GU_STAGE(aT1, aW1)
      GU_STAGE(aT2, aW2)
      GU_STAGE(aT0, aW3)
      GU_STAGE(aT1, aW0)
      GU_STAGE(aT2, aW1)
      GU_STAGE(aT0, aW2)
      GU_STAGE(aT1, aW3)
      const unsigned zo = lb + L_ZERO;
      const unsigned aC = lane < 32 ? lb + L_CSF + 3 * nh * 512 + lane * 16 : zo - 0, aM = lane < 32 ? lb + L_MF + 2 * mh * 512 + lane * 16 : zo;
      const unsigned stp = lane < 32 ? 512 : 0;
      tf[0][0] = *(LAS v4u*)(L0 + aM);
      tf[0][1] = *(LAS v4u*)(L0 + aM + stp);
      ga[0][0] = *(LAS s16x8*)(L0 + aC);
      ga[0][1] = *(LAS s16x8*)(L0 + aC + stp);
      ga[0][2] = *(LAS s16x8*)(L0 + aC + 2 * stp);
      GU_M(3, 0, 0); GU_M(3, 0, 1); GU_M(3, 0, 2); GU_M(3, 1, 0); GU_M(3, 1, 1); GU_M(3, 1, 2);
      GU_M(0, 0, 0); GU_M(0, 0, 1); GU_M(0, 0, 2); GU_M(0, 1, 0); GU_M(0, 1, 1); GU_M(0, 1, 2);
    }
    const float r2a = *(LAS float*)(L + L_R2 + (64 * mh + t32) * 4), r2b = *(LAS float*)(L + L_R2 + (64 * mh + 32 + t32) * 4);
    BAR();
#pragma unroll
    for (int tt = 0; tt < 2; ++tt) {
      const float r2 = tt ? r2b : r2a;
#pragma unroll
      for (int l3 = 0; l3 < 3; ++l3) {
        const int fblk = 6 * m + 3 * nh + l3;
        float hv[8];
#pragma unroll
        for (int i = 0; i < 2; ++i)
#pragma unroll
          for (int e = 0; e < 4; ++e) hv[4 * i + e] = (acc[tt][l3][8 * i + e] * r2) * acc[tt][l3][8 * i + 4 + e];
        const v4u o = {pk2(hv[0], hv[1]), pk2(hv[2], hv[3]), pk2(hv[4], hv[5]), pk2(hv[6], hv[7])};
        const unsigned off = fblk < 86 ? WS_HH + ((rt * 4 + 2 * mh + tt) * 86 + fblk) * 1024 + lane * 16 : 0x7FFFFFF0u;
        __builtin_amdgcn_raw_buffer_store_b128(o, rsWs, off, 0, 16);
      }
    }
    asm volatile("s_barrier" ::: "memory");
    WAITV(0);
    if (lane < 2) set_flag(p.ws + WS_F4 + (rt * 4 + 2 * mh + lane) * 256 + (m * 2 + nh) * 8, tag);


    const unsigned kh2048 = kh * 2048;
    const unsigned pb0 = opaque(rel0 + dn_base(0) + dn_n(0) * kh2048), pb1 = opaque(rel1 + dn_base(0) + dn_n(0) * kh2048);
    BAR();
    s16x8 wpre[DN_PRE][2];
#pragma unroll
    for (int i = 0; i < DN_PRE; ++i) {
      wpre[i][0] = cat(trread(L0, pb0 + i * 2048), trread(L0, pb0 + i * 2048 + 1024));
      wpre[i][1] = cat(trread(L0, pb1 + i * 2048), trread(L0, pb1 + i * 2048 + 1024));
    }
#pragma unroll
    for (int i = 0; i < DN_PRE; ++i) { TIE(wpre[i][0]); TIE(wpre[i][1]); }
    BAR();
    pollN<32>(p.ws + WS_F4 + (rt * 4 + tb32) * 256, tag, lane);
    v4u hf_[DN_RING];
    const unsigned hv = WS_HH + ((rt * 4 + tb32) * 86 + 43 * kh) * 1024 + lane * 16;
#pragma unroll
    for (int i = 0; i < DN_RING; ++i) hf_[i] = ld128_sc1(rsWs, hv, i * 1024);
#pragma unroll
    for (int i = 0; i < 16; ++i) { a0[i] = 0.f; a1[i] = 0.f; }
    s16x8 fw[2][2];
    DnStep<0>::run(hf_, wpre, fw, a0, a1, rel0, rel1, kh2048, pb0, pb1, rsWs, hv);
    {
      int tz = threadIdx.x;
      asm volatile("" : "+v"(tz));
      const int lane = tz & 63, hl = lane >> 5, t32 = lane & 31;
      const f32x16 give = kh ? a0 : a1;
      const f32x16 keep = kh ? a1 : a0;
#pragma unroll
      for (int i = 0; i < 4; ++i)
        *(LAS f32x4*)(L + L_RED_DN + (w * 4 + i) * 1024 + lane * 16) = (f32x4){give[4 * i], give[4 * i + 1], give[4 * i + 2], give[4 * i + 3]};
      BAR();
      unsigned ob[8];
#pragma unroll
      for (int i = 0; i < 4; ++i) {
        const f32x4 o = *(LAS f32x4*)(L + L_RED_DN + ((w ^ 2) * 4 + i) * 1024 + lane * 16);
        ob[2 * i] = pk2(keep[4 * i] + o[0] + bflo(hres[2 * i]), keep[4 * i + 1] + o[1] + bfhi(hres[2 * i]));
        ob[2 * i + 1] = pk2(keep[4 * i + 2] + o[2] + bflo(hres[2 * i + 1]), keep[4 * i + 3] + o[3] + bfhi(hres[2 * i + 1]));
      }
      const rsrc_t rsOut = mkrsrc(p.out, 2048u * 1024u);
      const unsigned oo = (128 * rt + 32 * tb32 + t32) * 1024 + (64 * cg + 32 * kh + 8 * hl) * 2;
#pragma unroll
      for (int pr = 0; pr < 2; ++pr) {
        swap32u(ob[4 * pr], ob[4 * pr + 2]);
        swap32u(ob[4 * pr + 1], ob[4 * pr + 3]);
        __builtin_amdgcn_raw_buffer_store_b128((v4u){ob[4 * pr], ob[4 * pr + 1], ob[4 * pr + 2], ob[4 * pr + 3]}, rsOut, oo + pr * 32, 0, 0);
      }
    }
    if (b == 0 && tid == 0) __hip_atomic_store((GAS u64*)(p.ws + WS_EPOCH), tag, __ATOMIC_RELAXED, __HIP_MEMORY_SCOPE_AGENT);
    asm volatile(".Lfb_end:");
  }
}

}

extern "C" void kernel_launch(void* const* d_in, const int* in_sizes, int n_in, void* d_out, int out_size, void* d_ws, size_t ws_size,
                              hipStream_t stream) {
  Params p;
  p.x = d_in[0];
  p.wqkv = d_in[1];
  p.wphi = d_in[2];
  p.wo = d_in[3];
  p.wg = d_in[5];
  p.wu = d_in[6];
  p.wd = d_in[7];
  p.out = d_out;
  p.ws = (unsigned char*)d_ws;
  fused_block_kernel<<<256, 512, 0, stream>>>(p);
}
```
